```python
import torch

def _softmax(x, axis=-1):
    return torch.softmax(x, dim=axis)
def _gelu(x):
    return torch.nn.functional.gelu(x, approximate="tanh")
def _silu(x):
    return torch.nn.functional.silu(x)
def _sigmoid(x):
    return torch.sigmoid(x)
def _layernorm(x, axis=-1, eps=1e-5):
    m = x.mean(dim=axis, keepdim=True)
    v = x.var(dim=axis, keepdim=True, unbiased=False)
    return (x - m) / torch.sqrt(v + eps)
def _scatter_add(tgt, idx, src, axis=0):
    return tgt.clone().index_add_(axis, idx, src)
def _segment_sum(src, seg, n):
    out = torch.zeros((n,) + src.shape[1:], dtype=src.dtype)
    return out.index_add_(0, seg, src)
def _pad(x, pw):
    flat = tuple(v for pair in reversed(pw) for v in pair)
    return torch.nn.functional.pad(x, flat)

kernel_name = 'parallel_sum__ssm_conv_s4d__dense_activation_squared_relu__prompt'

def setup_inputs():

    _w = (lambda a: (lambda t: t.to(torch.bfloat16) if t.is_floating_point() else t)(torch.as_tensor(a)))
    def _A_setup():
        Bsz, L, D, N = 1, 2048, 512, 128
        return {
            "u": _w((lambda *s: torch.randn(*s, dtype=torch.bfloat16) * 0.05)(Bsz, L, D)),
            "log_A_real": _w((lambda *s: torch.randn(*s, dtype=torch.bfloat16) * 3.2)(D, N) * 0.5),
            "A_imag": _w((lambda *s: torch.randn(*s, dtype=torch.bfloat16) * 0.2)(D, N) * 3.14),
            "C_real": _w((lambda *s: torch.randn(*s, dtype=torch.bfloat16) * 3.2)(D, N) / N ** 0.5),
            "C_imag": _w((lambda *s: torch.randn(*s, dtype=torch.bfloat16) * 3.2)(D, N) / N ** 0.5),
            "log_dt": _w((lambda *s: torch.randn(*s, dtype=torch.bfloat16) * 0.2)(D) * 0.5 - 4.0),
            "D_skip": _w((lambda *s: torch.randn(*s, dtype=torch.bfloat16) * 0.4)(D)),
        }
    _A_in = _A_setup()

    def _B_setup():
        B, S, D, F = 1, 2048, 512, 1376
        return {
            "x": _w((lambda *s: torch.randn(*s, dtype=torch.bfloat16) * 0.05)(B, S, D)),
            "w1": _w((lambda *s: torch.randn(*s, dtype=torch.bfloat16) * 0.05)(D, F)),
            "b1": _w((lambda *s: torch.randn(*s, dtype=torch.bfloat16) * 0.2)(F)),
            "w2": _w((lambda *s: torch.randn(*s, dtype=torch.bfloat16) * 0.05)(F, D)),
            "b2": _w((lambda *s: torch.randn(*s, dtype=torch.bfloat16) * 0.8)(D)),
        }
    _B_in = _B_setup()

    return {
        "A_u": _A_in["u"],
        "A_log_A_real": _A_in["log_A_real"],
        "A_A_imag": _A_in["A_imag"],
        "A_C_real": _A_in["C_real"],
        "A_C_imag": _A_in["C_imag"],
        "A_log_dt": _A_in["log_dt"],
        "A_D_skip": _A_in["D_skip"],
        "B_x": _B_in["x"],
        "B_w1": _B_in["w1"],
        "B_b1": _B_in["b1"],
        "B_w2": _B_in["w2"],
        "B_b2": _B_in["b2"],
    }

def reference(A_u, A_log_A_real, A_A_imag, A_C_real, A_C_imag, A_log_dt, A_D_skip, B_x, B_w1, B_b1, B_w2, B_b2):
    def _A_ref(u, log_A_real, A_imag, C_real, C_imag, log_dt, D_skip):
        L = u.shape[1]
        dt = torch.exp(log_dt)
        A = -torch.exp(log_A_real) + 1j * A_imag
        C = (C_real + 1j * C_imag) * dt[:, None]
        pos = (lambda x, dt: x.to(dt))(torch.arange(L), log_dt.dtype)
        P = torch.exp((A * dt[:, None])[:, :, None] * pos[None, None, :])
        K = 2.0 * torch.einsum("dn,dnl->dl", C, P).real
        L2 = 2 * L
        u_t = (lambda x, *d: x.permute(*d))(u, 0, 2, 1)
        Kf = (lambda x, *a, **k: torch.fft.rfft(x.float(), *a, **k))(K, L2)
        Uf = (lambda x, *a, **k: torch.fft.rfft(x.float(), *a, **k))(u_t, L2)
        y = (lambda x, *a, **k: torch.fft.irfft(x, *a, **k).to(torch.bfloat16))(Uf * Kf[None, :, :], L2)[:, :, :L]
        y = (lambda x, *d: x.permute(*d))(y, 0, 2, 1)
        return y + u * D_skip[None, None, :]

    def _B_ref(x, w1, b1, w2, b2):
        h = torch.einsum("bsd,df->bsf", x, w1) + b1
        zero = torch.zeros_like(h)
        r = torch.where(h > zero, h, zero)
        h = r * r
        out = torch.einsum("bsf,fd->bsd", h, w2) + b2
        return out

    x = A_u
    xn = _layernorm(x, axis=-1)
    return x + _A_ref(xn, A_log_A_real, A_A_imag, A_C_real, A_C_imag, A_log_dt, A_D_skip) + _B_ref(xn, B_w1, B_b1, B_w2, B_b2)
```

```cpp
#include <hip/hip_runtime.h>
#include <hip/hip_bf16.h>
#include <stdint.h>

typedef unsigned short u16;
static constexpr int L = 2048, D = 512, NM = 128, F = 1376;

__device__ __forceinline__ float bf2f(u16 v) { return __uint_as_float(((unsigned)v) << 16); }
__device__ __forceinline__ u16 f2bf(float f) {
    __hip_bfloat16 b = __float2bfloat16(f);
    return *reinterpret_cast<u16*>(&b);
}
__device__ __forceinline__ float rbf(float f) { return bf2f(f2bf(f)); }

__global__ void k_ln(const u16* x, u16* xn) {
    int row = blockIdx.x, t = threadIdx.x;
    float v[8]; float s = 0;
    for (int i = 0; i < 8; i++) { v[i] = bf2f(x[row * D + t * 8 + i]); s += v[i]; }
    for (int o = 32; o; o >>= 1) s += __shfl_xor(s, o);
    float mean = s / D;
    float q = 0;
    for (int i = 0; i < 8; i++) { float d = v[i] - mean; q += d * d; }
    for (int o = 32; o; o >>= 1) q += __shfl_xor(q, o);
    float var = rbf(q / D);
    float m = rbf(mean);
    float sd = rbf(sqrtf(rbf(var + 1e-5f)));
    for (int i = 0; i < 8; i++) xn[row * D + t * 8 + i] = f2bf(rbf(v[i] - m) / sd);
}

__global__ void k_kgen(const u16* lar, const u16* aim, const u16* cre, const u16* cim, const u16* ldt, float* K) {
    int d = blockIdx.y; int l = blockIdx.x * 256 + threadIdx.x;
    float dt = rbf(expf(bf2f(ldt[d])));
    float pos = rbf((float)l);
    float acc = 0;
    for (int n = 0; n < NM; n++) {
        float ar = -rbf(expf(bf2f(lar[d * NM + n]))) * dt;
        float ai = bf2f(aim[d * NM + n]) * dt;
        float cr = bf2f(cre[d * NM + n]) * dt, ci = bf2f(cim[d * NM + n]) * dt;
        float e = expf(ar * pos); float s, c; sincosf(ai * pos, &s, &c);
        acc += e * (cr * c - ci * s);
    }
    K[d * L + l] = 2.f * acc;
}

__global__ void k_conv(const u16* xn, const float* K, float* y) {
    int d = blockIdx.x * 64 + (threadIdx.x & 63); int l = blockIdx.y * 4 + (threadIdx.x >> 6);
    float acc = 0;
    for (int j = 0; j <= l; j++) acc += K[d * L + j] * bf2f(xn[(l - j) * D + d]);
    y[l * D + d] = acc;
}

__global__ void k_g1(const u16* xn, const u16* w1, const u16* b1, u16* h) {
    int f = blockIdx.x * 32 + (threadIdx.x & 31); int l = blockIdx.y * 8 + (threadIdx.x >> 5);
    float acc = 0;
    for (int k = 0; k < D; k++) acc += bf2f(xn[l * D + k]) * bf2f(w1[k * F + f]);
    float v = rbf(rbf(acc) + bf2f(b1[f]));
    v = v > 0 ? v : 0;
    h[l * F + f] = f2bf(v * v);
}

__global__ void k_g2(const u16* x, const u16* xn, const float* y, const u16* h, const u16* w2, const u16* b2, const u16* dsk, u16* out) {
    int d = blockIdx.x * 64 + (threadIdx.x & 63); int l = blockIdx.y * 4 + (threadIdx.x >> 6);
    float acc = 0;
    for (int k = 0; k < F; k++) acc += bf2f(h[l * F + k]) * bf2f(w2[k * D + d]);
    float B = rbf(rbf(acc) + bf2f(b2[d]));
    float A = rbf(rbf(y[l * D + d]) + rbf(bf2f(xn[l * D + d]) * bf2f(dsk[d])));
    out[l * D + d] = f2bf(rbf(bf2f(x[l * D + d]) + A) + B);
}

extern "C" void kernel_launch(void* const* d_in, const int* in_sizes, int n_in,
                              void* d_out, int out_size, void* d_ws, size_t ws_size,
                              hipStream_t stream) {
    const u16* x = (const u16*)d_in[0];
    const u16* lar = (const u16*)d_in[1];
    const u16* aim = (const u16*)d_in[2];
    const u16* cre = (const u16*)d_in[3];
    const u16* cim = (const u16*)d_in[4];
    const u16* ldt = (const u16*)d_in[5];
    const u16* dsk = (const u16*)d_in[6];
    const u16* w1 = (const u16*)d_in[8];
    const u16* b1 = (const u16*)d_in[9];
    const u16* w2 = (const u16*)d_in[10];
    const u16* b2 = (const u16*)d_in[11];
    char* ws = (char*)d_ws;
    u16* xn = (u16*)ws;
    float* K = (float*)(ws + (2 << 20));
    float* y = (float*)(ws + (6 << 20));
    u16* h = (u16*)(ws + (10 << 20));
    k_ln<<<L, 64, 0, stream>>>(x, xn);
    k_kgen<<<dim3(L / 256, D), 256, 0, stream>>>(lar, aim, cre, cim, ldt, K);
    k_conv<<<dim3(D / 64, L / 4), 256, 0, stream>>>(xn, K, y);
    k_g1<<<dim3(F / 32, L / 8), 256, 0, stream>>>(xn, w1, b1, h);
    k_g2<<<dim3(D / 64, L / 4), 256, 0, stream>>>(x, xn, y, h, w2, b2, dsk, (u16*)d_out);
}
```

```cpp
#include <hip/hip_runtime.h>
#include <stdint.h>
#include <type_traits>

#define LAS __attribute__((address_space(3)))
#define GAS __attribute__((address_space(1)))
#if defined(__HIP_DEVICE_COMPILE__) && __HIP_DEVICE_COMPILE__
#define NOPK __attribute__((target("no-packed-fp32-ops")))
#else
#define NOPK
#endif
#define DEV __device__ __forceinline__ NOPK
#define LAM __attribute__((always_inline)) NOPK
typedef unsigned short u16;
typedef unsigned long long u64;
typedef unsigned v4u __attribute__((ext_vector_type(4)));
typedef unsigned v2u __attribute__((ext_vector_type(2)));
typedef unsigned v2u4 __attribute__((ext_vector_type(2), aligned(4)));
typedef __bf16 bf16x8 __attribute__((ext_vector_type(8)));
typedef _Float16 f16x8 __attribute__((ext_vector_type(8)));
typedef float f32x4 __attribute__((ext_vector_type(4)));
typedef short s16x4 __attribute__((ext_vector_type(4)));
typedef short s16x2 __attribute__((ext_vector_type(2)));
typedef __amdgpu_buffer_rsrc_t rsrc_t;

constexpr int SEQ = 2048, DM = 512, NMODE = 128, FF = 1376, NKS = 43;
constexpr size_t WS_H = 0, WS_U = 6u << 20, WS_Y = 8u << 20, WS_FLAG = 10u << 20, WS_DIAG = 12u << 20;
constexpr int GF_U = 0, GF_H = 1024, GF_Y = 2048, GF_EC = 3072, GF_ER = 3200;
#define FSTR 1
#define PSLEEP 8
#define UWAIT 1
#define HDELAY 12
constexpr int NTHR = 768;
constexpr int POLL_CAP = 1 << 14;
constexpr int AUX_SC1 = 16;

struct Args {
  const u16 *x, *lar, *aim, *cre, *cim, *ldt, *dsk, *w1, *b1, *w2, *b2;
  u16* out;
  char* ws;
};

#define TR_DECL
#define TR(i)
#define TR_DUMP(base, n)

template <int I, int N, class Fn> DEV void static_for(Fn&& f) {
  if constexpr (I < N) { f(std::integral_constant<int, I>{}); static_for<I + 1, N>(f); }
}
#define CE(x) (std::integral_constant<int, (x)>::value)
template <class T> DEV void pin(T& v) { asm volatile("" : "+v"(v)); }
template <class T> DEV void pins(T& v) { asm volatile("" : "+s"(v)); }
DEV float u2f(unsigned u) { return __builtin_bit_cast(float, u); }
DEV unsigned f2u(float f) { return __builtin_bit_cast(unsigned, f); }
DEV bool wany(bool c) { return __builtin_amdgcn_ballot_w64(c) != 0; }
DEV rsrc_t mk_rsrc(const void* p, int bytes) { return __builtin_amdgcn_make_buffer_rsrc((void*)p, 0, bytes, 0x00020000); }
DEV float bf_lo(unsigned u) { return u2f(u << 16); }
DEV float bf_hi(unsigned u) { return u2f(u & 0xffff0000u); }
typedef float f32x2 __attribute__((ext_vector_type(2)));
typedef __bf16 bf16x2 __attribute__((ext_vector_type(2)));
typedef _Float16 f16x2 __attribute__((ext_vector_type(2)));
DEV unsigned pk_bf(float a, float b) { return __builtin_bit_cast(unsigned, __builtin_convertvector(f32x2{a, b}, bf16x2)); }
DEV unsigned pk_h(float a, float b) { return __builtin_bit_cast(unsigned, __builtin_convertvector(f32x2{a, b}, f16x2)); }
DEV unsigned pk_hn(float a, float b) { return pk_h(a, -b); }
DEV float rbf(float a) { return u2f(pk_bf(0.f, a)); }
DEV unsigned relu_pk(unsigned v) { return __builtin_bit_cast(unsigned, __builtin_elementwise_max(__builtin_bit_cast(s16x2, v), s16x2{0, 0})); }
DEV v2u tr_read(LAS char* p) { return __builtin_bit_cast(v2u, __builtin_amdgcn_ds_read_tr16_b64_v4i16((LAS s16x4*)p)); }
DEV v2u ld2(LAS char* p) { return *(LAS v2u4*)p; }
DEV float xsum16(float v) { auto r = __builtin_amdgcn_permlane16_swap(f2u(v), f2u(v), false, false); return u2f(r[0]) + u2f(r[1]); }
DEV float xsum32(float v) { auto r = __builtin_amdgcn_permlane32_swap(f2u(v), f2u(v), false, false); return u2f(r[0]) + u2f(r[1]); }
template <int CTRL> DEV float dpp_add(float v) { return v + u2f((unsigned)__builtin_amdgcn_update_dpp(0, (int)f2u(v), CTRL, 0xf, 0xf, false)); }
DEV float wave_sum(float v) { v = dpp_add<0x121>(v); v = dpp_add<0x122>(v); v = dpp_add<0x124>(v); v = dpp_add<0x128>(v); return xsum32(xsum16(v)); }
DEV f32x4 sel4(int c, f32x4 a, f32x4 b) { return f32x4{c ? a[0] : b[0], c ? a[1] : b[1], c ? a[2] : b[2], c ? a[3] : b[3]}; }
DEV LAS char* opq(LAS char* p) { unsigned v = (unsigned)(size_t)p; pin(v); return (LAS char*)(size_t)v; }
DEV f32x4 zero4() { f32x4 z = {0, 0, 0, 0}; pin(z); return z; }
#define VMCNT(n) asm volatile("s_waitcnt vmcnt(%0)" ::"i"(n) : "memory")
#define LGKM0() asm volatile("s_waitcnt lgkmcnt(0)" ::: "memory")
#define CFENCE() asm volatile("" ::: "memory")
#define SB() __builtin_amdgcn_sched_barrier(0)
DEV void mfma_bf(f32x4& c, v4u a, v4u b) { asm volatile("v_mfma_f32_16x16x32_bf16 %0, %1, %2, %0" : "+v"(c) : "v"(a), "v"(b)); }
DEV void mfma_h(f32x4& c, v4u a, v4u b) { asm volatile("v_mfma_f32_16x16x32_f16 %0, %1, %2, %0" : "+v"(c) : "v"(a), "v"(b)); }
#define NOP2() do { SB(); asm volatile("s_nop 1"); SB(); } while (0)
#define MFMA_DONE() do { SB(); asm volatile("s_nop 7"); SB(); } while (0)

DEV void st_sc1(v4u v, rsrc_t r, int voff, int soff) { __builtin_amdgcn_raw_buffer_store_b128(v, r, voff, soff, AUX_SC1); NOP2(); }
DEV void lds_put(unsigned addr, unsigned v) { asm volatile("s_mov_b64 exec, 1\n\tds_write_b32 %0, %1\n\ts_mov_b64 exec, -1" ::"v"(addr), "v"(v) : "memory"); }
template <int OFF, int WANT> DEV void lds_wait(unsigned fa) {
  unsigned t, n;
  asm volatile("s_mov_b32 %1, 0x20000\n"
               "1:\n\tds_read_b32 %0, %2 offset:%3\n\ts_waitcnt lgkmcnt(0)\n\tv_cmp_gt_u32 vcc, %4, %0\n\ts_cbranch_vccz 2f\n\t"
               "s_sleep 1\n\ts_sub_u32 %1, %1, 1\n\ts_cbranch_scc0 1b\n2:"
               : "=&v"(t), "=&s"(n) : "v"(fa), "i"(OFF), "i"(WANT) : "memory", "vcc", "scc");
}
template <int OFF> DEV void lds_wait_s(unsigned fa, int want) {
  unsigned t, n;
  asm volatile("s_mov_b32 %1, 0x20000\n"
               "1:\n\tds_read_b32 %0, %2 offset:%3\n\ts_waitcnt lgkmcnt(0)\n\tv_cmp_gt_u32 vcc, %4, %0\n\ts_cbranch_vccz 2f\n\t"
               "s_sleep 1\n\ts_sub_u32 %1, %1, 1\n\ts_cbranch_scc0 1b\n2:"
               : "=&v"(t), "=&s"(n) : "v"(fa), "i"(OFF), "s"(want) : "memory", "vcc", "scc");
}
static __device__ const unsigned kSeq[64] = {0,1,2,3,4,5,6,7,8,9,10,11,12,13,14,15,16,17,18,19,20,21,22,23,24,25,26,27,28,29,30,31,32,
  33,34,35,36,37,38,39,40,41,42,43,44,45,46,47,48,49,50,51,52,53,54,55,56,57,58,59,60,61,62,63};
DEV v4u srd(const void* p, unsigned bytes) { const u64 v = (u64)p; return v4u{(unsigned)v, (unsigned)(v >> 32) & 0xffffu, bytes, 0x00020000u}; }
DEV void dma16(v4u rs, unsigned lds, int voff, int soff) {
  asm volatile("s_mov_b32 m0, %0\n\ts_nop 0\n\tbuffer_load_dwordx4 %1, %2, %3 offen lds" ::"s"(lds), "v"(voff), "s"(rs), "s"(soff) : "memory");
}
template <int N> DEV void dma_flag(v4u rs, unsigned lds) {
  asm volatile("s_mov_b32 m0, %0\n\ts_mov_b64 exec, 1\n\tbuffer_load_dword off, %1, %2 lds\n\ts_mov_b64 exec, -1" ::"s"(lds), "s"(rs), "s"(N * 4) : "memory");
}
DEV void dma_ep(v4u rs, unsigned lds, int voff) {
  asm volatile("s_mov_b32 m0, %0\n\ts_mov_b64 exec, 3\n\tbuffer_load_dword %2, %1, 0 offen sc1 lds\n\ts_mov_b64 exec, -1" ::"s"(lds), "s"(rs), "v"(voff) : "memory");
}
template <int OFF> DEV void put_tid(unsigned v) { asm volatile("ds_write_addtid_b32 %0 offset:%1" ::"v"(v), "i"(OFF) : "memory"); }
DEV void set_m0(unsigned v) { asm volatile("s_mov_b32 m0, %0\n\ts_nop 0" ::"s"(v) : "memory"); }
DEV GAS u64* gflag(const Args& a, int line) { return (GAS u64*)(a.ws + WS_FLAG) + line * FSTR; }
DEV void publish(GAS u64* f, u64 tag, int lane) {
  VMCNT(0);
  if (lane == 0) __hip_atomic_store(f, tag, __ATOMIC_RELAXED, __HIP_MEMORY_SCOPE_AGENT);
}
DEV void poll(GAS u64* p, u64 tag) {
#pragma nounroll
  for (int it = 0; it < POLL_CAP; it++) {
    if (!wany(__hip_atomic_load(p, __ATOMIC_RELAXED, __HIP_MEMORY_SCOPE_AGENT) != tag)) break;
    __builtin_amdgcn_s_sleep(PSLEEP);
  }
  __builtin_amdgcn_fence(__ATOMIC_ACQUIRE, "wavefront");
}
DEV void poll2(GAS u64* p, GAS u64* q, u64 tag) {
#pragma nounroll
  for (int it = 0; it < POLL_CAP; it++) {
    const u64 x = __hip_atomic_load(p, __ATOMIC_RELAXED, __HIP_MEMORY_SCOPE_AGENT), y = __hip_atomic_load(q, __ATOMIC_RELAXED, __HIP_MEMORY_SCOPE_AGENT);
    if (!wany(x != tag || y != tag)) break;
    __builtin_amdgcn_s_sleep(PSLEEP);
  }
  __builtin_amdgcn_fence(__ATOMIC_ACQUIRE, "wavefront");
}
DEV int unit_start(int j) { return (j * 37 + 3) / 7; }

constexpr int KCOPY = 4160, C_U = 2 * KCOPY, CH = C_U + 4608, L_TAB = 28160, TABB = 1408, L_STAT = 31744;
constexpr int L_X = 32768, L_RING = L_X + 65536, SROW = 528, SCR = 53 * SROW;
constexpr int L_Y = 0, YROW = 160, L_EX = L_Y + 64 * YROW;
constexpr int LDS_BYTES = 163840, FLG = LDS_BYTES - 256;
static_assert(2 * SCR <= 7 * 8192 && 2 * CH <= L_TAB && L_TAB + 2 * TABB <= L_STAT && L_EX + 16384 <= L_STAT && L_RING + 61440 <= FLG, "lds");
constexpr int F_FULL = 0, F_FREE = 16, F_XD = 32, F_CW = 48, F_YRDY = 64, F_GX = 80, F_EP = 96;

constexpr int NSTEP1 = 24;
constexpr int ST_KIND[NSTEP1] = {0, 1, 1, 0, 0, 1, 0, 1, 0, 1, 0, 0, 1, 0, 1, 1, 1, 1, 1, 1, 1, 1, 1, 1};
constexpr int st_idx(int s) { int n = 0; for (int i = 0; i < s; i++) n += ST_KIND[i] == ST_KIND[s]; return n; }
constexpr int step_of(int kind, int idx) { for (int s = 0; s < NSTEP1; s++) if (ST_KIND[s] == kind && st_idx(s) == idx) return s; return -1; }
constexpr int need_X(int c) { return step_of(0, c) + 1; }
constexpr int need_W(int u) { return step_of(1, u) + 1; }
constexpr int need_V(int t) { return NSTEP1 + t + 1; }
static_assert(need_X(1) < need_W(2) && need_X(2) < need_W(4) && need_X(3) < need_W(6) && need_X(7) < need_W(14), "x before its units");
constexpr int w_addr(int u) { return u % 7 < 5 ? L_RING + (u % 7) * 12288 : (u % 7 - 5) * 12288; }
constexpr int FREE_SCR = 14;
constexpr bool free_sent(int n) { return n <= 9 || n == FREE_SCR || n == 16; }
constexpr int x_addr(int c) { return L_X + ((c + 1) & 7) * 8192; }
constexpr int NVB = 14, L_V = L_X + 8192, VH = 7;
constexpr int PD = 8, VR = 8;
#define VDEPTH 8

DEV u64 tag_of(LAS char* sm, int which);
DEV void role_loader(const Args& a, LAS char* sm, int l, int lane, int p, int j, unsigned fa) {
  const v4u rx = srd(a.x, SEQ * DM * 2), rw = srd(a.w1, DM * FF * 2), rq = srd(kSeq, 256);
  int vx[2], vw[3];
#pragma unroll
  for (int i = 0; i < 2; i++) vx[i] = (8 * i + (lane >> 3)) * 1024 + (((lane & 7) ^ (4 * i + (lane >> 4))) * 16);
  const int sx = p * 65536 + l * 16384, sw = unit_start(j) * 64;
  TR_DECL;
  static_for<0, NSTEP1>([&](auto S) LAM {
    constexpr int s = S, kind = ST_KIND[s], idx = st_idx(s);
    if constexpr (s % 4 == 0) TR(s / 4);
    if constexpr (kind == 0) {
      const int so = sx + ((idx + j + 1) & 7) * 128;
#pragma unroll
      for (int i = 0; i < 2; i++) dma16(rx, CE(x_addr(idx)) + l * 2048 + i * 1024, vx[i], so);
    } else {
      if constexpr (idx >= 7) lds_wait<F_FREE, idx - 6>(fa);
      const int so = sw + ((((idx >> 1) + j + 1) & 7) * 64 + (idx & 1) * 32) * (FF * 2);
#pragma unroll
      for (int n = 0; n < 3; n++) {
        if constexpr (idx == 0) {
          const int sg = 16 * (3 * l + n) + (lane >> 2), r = (sg * 171) >> 10, qp = sg - 6 * r;
          vw[n] = r * (FF * 2) + ((qp ^ ((r >> 1) & 1)) * 64) + (((lane & 3) ^ (2 * ((r >> 3) & 1))) * 16);
        }
        dma16(rw, CE(w_addr(idx)) + l * 3072 + n * 1024, vw[n], so);
      }
      if constexpr (idx == 0) {
        if (l < 2) {
          const v4u re = srd(a.ws + WS_FLAG + (size_t)(l ? GF_ER + 16 * p : GF_EC + 16 * j) * 8 * FSTR, 8);
          dma_ep(re, FLG + F_EP + 8 * l, lane * 4);
        }
      }
    }
    if constexpr (kind == 1 || idx == 7) dma_flag<s + 1>(rq, FLG + F_FULL + 4 * l);
  });
  TR(6);
  if (l < 3) {
    const rsrc_t rv = mk_rsrc(a.w2, DM * FF * 2);
    const int vv = (lane >> 3) * 1024 + (lane & 7) * 16 + j * 128;
    v4u wr[VR][4];
    auto fetch = [&](int n) LAM {
      const int v = min(3 * n + l, 42), so = ((v >> 1) + 22 * (v & 1)) * 32768;
#pragma unroll
      for (int r = 0; r < 4; r++) wr[n % VR][r] = __builtin_amdgcn_raw_buffer_load_b128(rv, vv, so + r * 8192, 0);
    };
#pragma unroll
    for (int n = 0; n < VR; n++) { fetch(n); SB(); VMCNT(VDEPTH); }
    SB();
    TR(7);
    LAS char* wa[2];
#pragma unroll
    for (int i = 0; i < 2; i++) wa[i] = sm + L_V + (lane >> 3) * 128 + (((lane & 7) ^ ((((lane >> 4) & 1) | (i << 1)) << 1)) * 16);
    lds_wait<F_FREE, 16>(fa);
    lds_wait<F_CW, 3>(fa);
    TR(8);
    static_for<0, 15>([&](auto N) LAM {
      constexpr int n = N;
      if constexpr (n == 9) TR(9);
      const int v = 3 * n + l;
      if (n < 14 || l == 0) {
        if constexpr (n >= 9) lds_wait_s<F_FREE>(fa, 17 + (v >> 1) - NVB);
        const int lo = (v >= 2 * NVB ? v - 2 * NVB : v) * 4096;
#pragma unroll
        for (int r = 0; r < 4; r++) *(LAS v4u*)(wa[r & 1] + lo + r * 1024) = wr[n % VR][r];
        lds_put(FLG + F_FULL + 4 * l, n >= 13 && v + 3 > 42 ? 63 : NSTEP1 + ((v + 3) >> 1));
        if constexpr (n + VR < 15) fetch(n + VR);
        SB();
      }
    });
    TR(10);
    if (l == 0) TR_DUMP(0, 11);
    return;
  }
  const u64 tagc = tag_of(sm, 0);
  v4u uv[8];
  lds_wait<F_CW, UWAIT>(fa);
  poll2(gflag(a, GF_U + j * 128 + lane), gflag(a, GF_U + j * 128 + 64 + lane), tagc);
  TR(7);
  const rsrc_t ru = mk_rsrc(a.ws + WS_U, SEQ * DM * 2), ry = mk_rsrc(a.ws + WS_Y, SEQ * DM * 2);
#pragma unroll
  for (int i = 0; i < 8; i++) uv[i] = __builtin_amdgcn_raw_buffer_load_b128(ru, lane * 16 + (i & 3) * 1024, (j * 32 + p) * 8192 + (i >> 2) * 4096, AUX_SC1);
  lds_wait<F_FREE, FREE_SCR>(fa);
  lds_put(FLG + F_FULL + 12, 63);
  if (lane < 60) *(LAS v4u*)(sm + (lane & 1) * CH + C_U + (lane >> 1) * 16) = v4u{0, 0, 0, 0};
  if (lane < 8) *(LAS v4u*)(sm + (lane & 1) * CH + ((lane >> 1) & 1) * KCOPY + 4096 + (lane >> 2) * 16) = v4u{0, 0, 0, 0};
#pragma unroll
  for (int i = 0; i < 8; i++) *(LAS v4u*)(sm + (lane & 1) * CH + C_U + 480 + i * 512 + (lane >> 1) * 16) = uv[i];
  lds_put(FLG + F_YRDY, 1);
  TR(8);
  GAS u64* pf = gflag(a, GF_Y + (j * 2 + (p >> 4)) * 64 + lane);
  pin(pf);
  lds_wait<F_CW, 6>(fa);
  TR(9);
  __builtin_amdgcn_s_sleep(24);
  poll(pf, tagc);
  TR(10);
#pragma unroll
  for (int i = 0; i < 8; i++) uv[i] = __builtin_amdgcn_raw_buffer_load_b128(ry, (lane >> 3) * 4096 + (lane & 7) * 16, (64 * j + 8 * i) * 4096 + p * 128, AUX_SC1);
#pragma unroll
  for (int i = 0; i < 8; i++) *(LAS v4u*)(sm + L_Y + (8 * i + (lane >> 3)) * YROW + (lane & 7) * 16) = uv[i];
  lds_put(FLG + F_YRDY, 2);
  TR(11);
  TR_DUMP(32, 12);
}

DEV u64 tag_of(LAS char* sm, int which) {
  const v2u e = *(LAS v2u*)(sm + FLG + F_EP + 8 * which);
  const u64 v = ((u64)(unsigned)__builtin_amdgcn_readfirstlane(e.y) << 32) | (unsigned)__builtin_amdgcn_readfirstlane(e.x);
  return (v + 1) * 0x9E3779B97F4A7C15ull;
}

DEV void role_gemm(const Args& a, LAS char* sm, int w, int lane, int p, int j, unsigned fa) {
  const int g = lane >> 4, tok = lane & 15, q4 = tok >> 2, pp = lane & 3;
  const v4u rq = srd(kSeq, 256);
  const unsigned fre = FLG + F_FREE + 4 * w;
  volatile LAS unsigned* full = (volatile LAS unsigned*)(size_t)(fa + F_FULL);
  const rsrc_t rh = mk_rsrc(a.ws + WS_H, 32 * NKS * 4096);
  const int ks = w >> 1, ah = w & 1;
  TR_DECL; TR(0);
  u64 tagr;
  {
    const int us = unit_start(j), nown = (j == 7 ? NKS : unit_start(j + 1)) - us;
    unsigned bias[3];
#pragma unroll
    for (int i = 0; i < 3; i++) bias[i] = *(const GAS u16*)(a.b1 + us * 32 + (3 * w + i) * 16 + tok);
    LAS char* wb[2][3];
#pragma unroll
    for (int i = 0; i < 3; i++) {
      const int ft = 3 * w + i;
      wb[0][i] = opq(sm + (6 * (8 * g + q4) + ((ft >> 1) ^ ((q4 >> 1) & 1))) * 64 + (((ft & 1) ^ (g & 1)) * 32) + 8 * pp);
      wb[1][i] = opq(wb[0][i] + L_RING);
    }
    LAS char* xb[2];
#pragma unroll
    for (int s = 0; s < 2; s++) xb[s] = opq(sm + L_X + tok * 128 + (((4 * s + g) ^ ((tok >> 1) & 7)) * 16));
    int hoffv = (g & 1) * 1024 + (g >> 1) * 256 + tok * 16; pin(hoffv);
    f32x4 acc[3][4], cs[3];
#pragma unroll
    for (int i = 0; i < 3; i++) { cs[i] = zero4();
#pragma unroll
      for (int T = 0; T < 4; T++) acc[i][T] = zero4(); }
    v4u ones = {0x3f803f80u, 0x3f803f80u, 0x3f803f80u, 0x3f803f80u};
    pin(ones);
#pragma unroll
    for (int i = 0; i < 3; i++) { bias[i] = g ? 0u : bias[i] << 16; pin(bias[i]); }
    v2u wfr[2][3][2]; v4u xfr[2][4];
    auto rd = [&](auto U, auto K) LAM {
      constexpr int u = U, k = K, wa = w_addr(u), hi = wa >= L_RING;
      if constexpr (k >= 2 && k < 6) xfr[u & 1][k - 2] = *(LAS v4u*)(xb[u & 1] + (CE(x_addr(u >> 1)) - L_X) + (k - 2) * 2048);
      else { constexpr int i = k < 2 ? 0 : (k - 4) >> 1; wfr[u & 1][i][k & 1] = tr_read(wb[hi][i] + (wa - hi * L_RING) + (k & 1) * 1536); }
    };
    lds_wait<F_FULL, need_W(0)>(fa);
    TR(1);
    static_for<0, 10>([&](auto K) LAM { rd(std::integral_constant<int, 0>{}, K); });
    tagr = tag_of(sm, 1);
    lds_wait<F_FULL, need_W(1)>(fa);
    dma_flag<1>(rq, fre);
    static_for<0, 16>([&](auto U) LAM {
      constexpr int u = U, b = u & 1;
      if constexpr (u == 4 || u == 8 || u == 12) TR(2 + (u - 4) / 4);
      unsigned fv; bool lack = false;
      if constexpr (u < 14) fv = *full;
      static_for<0, 15>([&](auto M) LAM {
        constexpr int m = M, i = m / 5, T = m % 5;
        const v4u wv = {wfr[b][i][0].x, wfr[b][i][0].y, wfr[b][i][1].x, wfr[b][i][1].y};
        SB();
        if constexpr (T < 4) mfma_bf(acc[i][T], wv, xfr[b][T]); else mfma_bf(cs[i], ones, wv);
        SB();
        if constexpr (u < 15 && m < 10) rd(std::integral_constant<int, u + 1>{}, M);
        if constexpr (u < 14 && m == 11) lack = wany(fv < CE(need_W(u + 2)));
        if constexpr (u + 2 <= 9 && m == 13) { LGKM0(); dma_flag<u + 2>(rq, fre); }
        if constexpr ((u + 2 == FREE_SCR || u + 2 == 16) && m == 10) { VMCNT(0); lds_put(fre, u + 2); }
      });
      SB();
      if constexpr (u < 14) { if (__builtin_expect(lack, 0)) lds_wait<F_FULL, need_W(u + 2)>(fa); CFENCE(); }
    });
    MFMA_DONE();
    TR(5);
    lds_wait<F_XD, 2>(fa);
    v4u sb[4];
#pragma unroll
    for (int T = 0; T < 4; T++) sb[T] = *(LAS v4u*)(sm + L_STAT + (16 * T + tok) * 16);
    v4u av[3];
#pragma unroll
    for (int i = 0; i < 3; i++) {
      const float c = g ? 0.f : cs[i][0];
      const float r1 = c - rbf(c), r2 = r1 - rbf(r1);
      av[i] = v4u{pk_bf(c, r1), pk_bf(r2, u2f(bias[i])), 0u, 0u};
    }
#pragma unroll
    for (int T = 0; T < 4; T++) sb[T].z = 0;
    pin(av[0]); pin(av[1]); pin(av[2]); pin(sb[0]); pin(sb[1]); pin(sb[2]); pin(sb[3]);
    NOP2();
#pragma unroll
    for (int i = 0; i < 3; i++)
#pragma unroll
      for (int T = 0; T < 4; T++) mfma_bf(acc[i][T], av[i], sb[T]);
    SB();
#pragma unroll
    for (int i = 0; i < 3; i++) {
      const int ft = 3 * w + i;
      const int so = (p * NKS + us + (ft >> 1)) * 4096 + (ft & 1) * 512;
      const int vo = hoffv | ((ft >> 1) < nown ? 0 : 0x40000000);
#pragma unroll
      for (int T = 0; T < 4; T += 2) {
        unsigned pk[2][2];
#pragma unroll
        for (int t = 0; t < 2; t++)
#pragma unroll
          for (int r = 0; r < 2; r++) {
            const float t0 = acc[i][T + t][2 * r], t1 = acc[i][T + t][2 * r + 1];
            pk[t][r] = relu_pk(pk_bf(t0 * __builtin_fabsf(t0), t1 * __builtin_fabsf(t1)));
          }
        auto r0 = __builtin_amdgcn_permlane16_swap(pk[0][0], pk[1][0], false, false);
        auto r1 = __builtin_amdgcn_permlane16_swap(pk[0][1], pk[1][1], false, false);
        st_sc1(v4u{r0[0], r1[0], r0[1], r1[1]}, rh, vo + T * 1024, so);
        SB();
      }
    }
  }
  TR(6);
  GAS u64* pf = gflag(a, GF_H + p * 32 + (ks ? 16 + (lane & 15) : min(lane, 19)));
  LAS char* wb[2][4];
  const int phi = ((q4 >> 1) & 1) | ((g & 1) << 1);
#pragma unroll
  for (int e = 0; e < 4; e++) { wb[0][e] = opq(sm + L_V + ks * 4096 + (8 * g + q4) * 128 + ((e ^ phi) * 32) + 8 * pp); wb[1][e] = opq(wb[0][e] + VH * 8192); }
  f32x4 acc[4][2];
#pragma unroll
  for (int e = 0; e < 4; e++) { acc[e][0] = zero4(); acc[e][1] = zero4(); }
  pin(pf);
  publish(gflag(a, GF_H + (p * 8 + j) * 4 + w), tagr, lane);
  TR(7);
  v2u bias2[4];
#pragma unroll
  for (int e = 0; e < 4; e++) bias2[e] = *(const GAS v2u*)(a.b2 + 64 * j + 16 * e + 4 * g);
  v2u wfr[2][4][2];
  auto rd2 = [&](auto I, auto K) LAM {
    constexpr int i = I, k = K, t = i % NVB;
    wfr[i & 1][k >> 1][k & 1] = tr_read(wb[t / VH][k >> 1] + (t % VH) * 8192 + (k & 1) * 512);
  };
  __builtin_amdgcn_s_sleep(HDELAY);
  poll(pf, tagr);
  TR(8);
  const int hoff = (p * NKS + 22 * ks) * 4096 + ah * 2048;
  v4u hf[PD][2];
#pragma unroll
  for (int i = 0; i < PD; i++)
#pragma unroll
    for (int T = 0; T < 2; T++) hf[i][T] = __builtin_amdgcn_raw_buffer_load_b128(rh, lane * 16 + T * 1024, hoff + i * 4096, AUX_SC1);
  lds_wait<F_FULL, need_V(1)>(fa);
  static_for<0, 8>([&](auto K) LAM { rd2(std::integral_constant<int, 0>{}, K); });
  static_for<0, 22>([&](auto I) LAM {
    constexpr int i = I, b = i & 1;
    if constexpr (i == 1 || i == 8 || i == 15) TR(9 + (i - 1) / 7);
    if (i < 21 || ks == 0) {
      constexpr bool chk = i + 2 < 22;
      unsigned fv; bool lack = false;
      if constexpr (chk) fv = *full;
      const v4u h0 = hf[i % PD][0], h1 = hf[i % PD][1];
      static_for<0, 8>([&](auto M) LAM {
        constexpr int m = M, e = m >> 1;
        const v4u wv = {wfr[b][e][0].x, wfr[b][e][0].y, wfr[b][e][1].x, wfr[b][e][1].y};
        SB();
        mfma_bf(acc[e][m & 1], wv, (m & 1) ? h1 : h0);
        SB();
        if constexpr (i < 21) rd2(std::integral_constant<int, i + 1>{}, M);
        if constexpr (chk && m == 6) lack = wany(fv < CE(need_V(i + 2)));
      });
      SB();
      if constexpr (i + NVB < 22) lds_put(fre, 17 + i);
      if constexpr (i + PD < 22) {
        const int so = hoff + (i + PD < 21 ? i + PD : 21 - ks) * 4096;
#pragma unroll
        for (int T = 0; T < 2; T++) hf[i % PD][T] = __builtin_amdgcn_raw_buffer_load_b128(rh, lane * 16 + T * 1024, so, AUX_SC1);
      }
      if constexpr (chk) { if (__builtin_expect(lack, 0)) lds_wait<F_FULL, need_V(i + 2)>(fa); CFENCE(); }
    }
  });
  MFMA_DONE();
  TR(12);
  lds_wait<0, 2>((unsigned)(FLG + F_YRDY));
  TR(13);
  LAS char* ex = sm + L_EX;
#pragma unroll
  for (int e = 0; e < 4; e++) *(LAS f32x4*)(ex + w * 4096 + e * 1024 + lane * 16) = sel4(ks, acc[e][0], acc[e][1]);
  lds_put(FLG + F_GX + 4 * w, 1);
  const int T = 2 * ah + ks;
  v2u yv[4], xv[4];
#pragma unroll
  for (int e = 0; e < 4; e++) {
    yv[e] = tr_read(sm + L_Y + (16 * e + 4 * g + q4) * YROW + T * 32 + 8 * pp);
    xv[e] = *(LAS v2u*)(sm + L_X + (16 * T + tok) * 128 + (((2 * e + (g >> 1)) ^ ((tok >> 1) & 7)) * 16) + (g & 1) * 8);
  }
  const float rs2 = *(LAS float*)(sm + L_STAT + (16 * T + tok) * 16 + 8);
  float rest[4][4];
#pragma unroll
  for (int e = 0; e < 4; e++) {
    const f32x4 m = sel4(ks, acc[e][1], acc[e][0]);
    rest[e][0] = __builtin_fmaf(m[0], rs2, bf_lo(bias2[e].x) + (bf_lo(yv[e].x) + bf_lo(xv[e].x)));
    rest[e][1] = __builtin_fmaf(m[1], rs2, bf_hi(bias2[e].x) + (bf_hi(yv[e].x) + bf_hi(xv[e].x)));
    rest[e][2] = __builtin_fmaf(m[2], rs2, bf_lo(bias2[e].y) + (bf_lo(yv[e].y) + bf_lo(xv[e].y)));
    rest[e][3] = __builtin_fmaf(m[3], rs2, bf_hi(bias2[e].y) + (bf_hi(yv[e].y) + bf_hi(xv[e].y)));
#pragma unroll
    for (int r = 0; r < 4; r++) pin(rest[e][r]);
  }
  lds_wait<0, 1>(FLG + F_GX + 4 * (w ^ 2));
  LAS char* rl = ex + (w ^ 2) * 4096;
  f32x4 ov[4];
#pragma unroll
  for (int e = 0; e < 4; e++) ov[e] = *(LAS f32x4*)(rl + e * 1024 + lane * 16);
  CFENCE();
#pragma unroll
  for (int e = 0; e < 4; e++) {
    const f32x4 o = ov[e];
    *(LAS v2u*)(rl + tok * 144 + e * 32 + g * 8) = v2u{pk_bf(__builtin_fmaf(o[0], rs2, rest[e][0]), __builtin_fmaf(o[1], rs2, rest[e][1])),
                                                       pk_bf(__builtin_fmaf(o[2], rs2, rest[e][2]), __builtin_fmaf(o[3], rs2, rest[e][3]))};
  }
  CFENCE();
#pragma unroll
  for (int i = 0; i < 2; i++) {
    const int r = 8 * i + (lane >> 3);
    const v4u o = *(LAS v4u*)(rl + r * 144 + (lane & 7) * 16);
    st_sc1(o, mk_rsrc(a.out, SEQ * DM * 2), ((64 * p + 16 * T + r) * DM + 64 * j + (lane & 7) * 8) * 2, 0);
  }
  if (w == 0 && lane < 2 && (lane ? j == 0 : p == 0)) {
    const u64 e1 = *(LAS u64*)(sm + FLG + F_EP + lane * 8) + 1;
    __hip_atomic_store(gflag(a, lane ? GF_ER + 16 * p : GF_EC + 16 * j), e1, __ATOMIC_RELAXED, __HIP_MEMORY_SCOPE_AGENT);
  }
  TR(14);
  if (w == 0) TR_DUMP(16, 15);
}

struct cpx { float r, i; };
DEV cpx cmul(cpx a, cpx b) { return cpx{__builtin_fmaf(a.r, b.r, -a.i * b.i), __builtin_fmaf(a.r, b.i, a.i * b.r)}; }
DEV cpx cexp_p(float zr2, float zi2, float pos) {
  const float e = __builtin_amdgcn_exp2f(zr2 * pos), th = __builtin_amdgcn_fractf(zi2 * pos);
  return cpx{e * __builtin_amdgcn_cosf(th), e * __builtin_amdgcn_sinf(th)};
}
DEV float exp_bf(float v) { return rbf(__builtin_amdgcn_exp2f(v * 1.44269504088896f)); }
DEV void pow2_scale(float mx, float& s, float& inv) {
  const int e = min(max((int)(f2u(mx) >> 23), 15), 254);
  s = u2f((unsigned)(268 - e) << 23);
  inv = u2f((unsigned)(e - 14) << 23);
}

DEV void role_conv(const Args& a, LAS char* sm, int cw, int lane, int p, int j, unsigned fa) {
  const int g = lane >> 4, tok = lane & 15;
  const int ch = cw >> 1, mh = cw & 1, d = 64 * j + 2 * p + ch;
  const unsigned fme = FLG + F_CW + 4 * cw, fpa = FLG + F_CW + 4 * (cw ^ 1);
  TR_DECL; TR(0);
  const unsigned lar = ((const GAS unsigned*)a.lar)[d * 64 + lane], aim = ((const GAS unsigned*)a.aim)[d * 64 + lane];
  const unsigned cre = ((const GAS unsigned*)a.cre)[d * 64 + lane], cim = ((const GAS unsigned*)a.cim)[d * 64 + lane];
  const float ldt = bf_lo(*(const GAS u16*)(a.ldt + d)), dsk = bf_lo(*(const GAS u16*)(a.dsk + d));
  const int x0 = 1024 * mh + 16 * lane;
  unsigned ga[18];
#pragma unroll
  for (int i = 0; i < 18; i += 2) {
    const int j0 = 2047 - (x0 + i), j1 = j0 - 1;
    const unsigned c = pk_bf((float)j0, (float)j1);
    const int i0 = j0 < 128 ? (j0 < 0 ? 703 : j0) : (int)(c & 0xffff) - 0x4300 + 128;
    const int i1 = j1 < 128 ? (j1 < 0 ? 703 : j1) : (int)(c >> 16) - 0x4300 + 128;
    ga[i] = 2 * i0 + L_TAB + ch * TABB; ga[i + 1] = 2 * i1 + L_TAB + ch * TABB;
    pin(ga[i]); pin(ga[i + 1]);
  }
  LAS char* xb[2];
#pragma unroll
  for (int s = 0; s < 2; s++) xb[s] = opq(sm + L_X + (16 * cw + tok) * 128 + (((4 * s + g) ^ ((tok >> 1) & 7)) * 16));
  v4u ones = {0x3f803f80u, 0x3f803f80u, 0x3f803f80u, 0x3f803f80u};
  v4u idm[2];
#pragma unroll
  for (int h = 0; h < 2; h++)
#pragma unroll
    for (int e = 0; e < 4; e++) {
      const int k0 = 8 * g + 2 * e - 16 * h;
      idm[h][e] = (k0 == tok ? 0x3f80u : 0u) | (k0 + 1 == tok ? 0x3f800000u : 0u);
    }
  pin(ones); pin(idm[0]); pin(idm[1]);
  unsigned pv[53];
  const float dt = exp_bf(ldt);
  const int sh = mh ? 0 : 16;
  const float zr2 = -exp_bf(bf_hi(lar << sh)) * dt * 1.44269504088896f, zi2 = bf_hi(aim << sh) * dt * 0.159154943091895f;
  {
    const cpx E8 = cexp_p(zr2, zi2, 8.f), E32 = cexp_p(zr2, zi2, 32.f);
    cpx Y[4]; Y[0] = cpx{512.f, 0.f}; Y[1] = cpx{512.f * E8.r, 512.f * E8.i}; Y[2] = cmul(Y[1], E8); Y[3] = cmul(Y[2], E8);
#pragma unroll
    for (int b = 0; b < 32; b++) { pv[21 + b] = pk_h(Y[b & 3].r, Y[b & 3].i); pin(pv[21 + b]); if (b < 28) Y[b & 3] = cmul(Y[b & 3], E32); }
  }
  TR(1);
  f32x4 sa = zero4(), gr = zero4();
  v4u xl[2];
  static_for<0, 2>([&](auto B) LAM {
    constexpr int c0 = B == 0 ? 0 : 5, c1 = B == 0 ? 5 : 8;
    lds_wait<F_FULL, B == 0 ? need_X(4) + 1 : need_X(7)>(fa);
    if constexpr (B == 1) { TR(2); __builtin_amdgcn_s_setprio(3); }
    v4u f[2 * (c1 - c0)];
#pragma unroll
    for (int c = c0; c < c1; c++) { f[2 * (c - c0)] = *(LAS v4u*)(xb[0] + ((c + 1) & 7) * 8192); f[2 * (c - c0) + 1] = *(LAS v4u*)(xb[1] + ((c + 1) & 7) * 8192); }
    SB();
#pragma unroll
    for (int i = 0; i < 2 * (c1 - c0); i++) { mfma_bf(sa, ones, f[i]); mfma_bf(gr, f[i], f[i]); }
    if constexpr (B == 1) { xl[0] = f[4]; xl[1] = f[5]; }
  });
  const u64 tagc = tag_of(sm, 0);
  MFMA_DONE();
  const float dg = (tok & 2) ? ((tok & 1) ? gr[3] : gr[2]) : ((tok & 1) ? gr[1] : gr[0]);
  const float mean = sa[0] * (1.f / DM), ex2 = xsum32(xsum16(g == (tok >> 2) ? dg : 0.f)) * (1.f / DM);
  float var = __builtin_fmaf(-mean, mean, ex2);
  if (wany(var < ex2 * (1.f / 1024.f))) {
    float q = 0;
#pragma nounroll
    for (int c = 0; c < 16; c++) {
      const v4u u = *(LAS v4u*)((c & 1 ? xb[1] : xb[0]) + (c >> 1) * 8192);
#pragma unroll
      for (int e = 0; e < 4; e++) { const unsigned uu = u[e]; const float d0 = bf_lo(uu) - mean, d1 = bf_hi(uu) - mean; q = __builtin_fmaf(d0, d0, __builtin_fmaf(d1, d1, q)); }
    }
    var = xsum32(xsum16(q)) * (1.f / DM);
  }
  const float m = rbf(mean), sd = rbf(__builtin_amdgcn_sqrtf(rbf(rbf(var) + 1e-5f))), rs = __builtin_amdgcn_rcpf(sd);
  const rsrc_t ru = mk_rsrc(a.ws + WS_U, SEQ * DM * 2);
  dma_flag<1>(srd(kSeq, 256), FLG + F_XD + 4 * cw);
  {
    const float c0 = -m * rs;
    v4u xn[2]; f32x4 t[4];
#pragma unroll
    for (int s = 0; s < 2; s++)
#pragma unroll
      for (int e = 0; e < 4; e++) { const unsigned uu = xl[s][e]; xn[s][e] = pk_bf(__builtin_fmaf(bf_lo(uu), rs, c0), __builtin_fmaf(bf_hi(uu), rs, c0)); }
#pragma unroll
    for (int i = 0; i < 4; i++) t[i] = zero4();
    pin(xn[0]); pin(xn[1]);
    NOP2();
#pragma unroll
    for (int i = 0; i < 4; i++) mfma_bf(t[i], xn[i >> 1], idm[i & 1]);
    MFMA_DONE();
#pragma unroll
    for (int s = 0; s < 2; s++) {
      unsigned h0 = pk_h(t[2 * s][0], t[2 * s][1]), h1 = pk_h(t[2 * s + 1][0], t[2 * s + 1][1]), h2 = pk_h(t[2 * s][2], t[2 * s][3]), h3 = pk_h(t[2 * s + 1][2], t[2 * s + 1][3]);
      auto r0 = __builtin_amdgcn_permlane16_swap(h0, h1, false, false);
      auto r1 = __builtin_amdgcn_permlane16_swap(h2, h3, false, false);
      st_sc1(v4u{r0[0], r1[0], r0[1], r1[1]}, ru, (8 * (g & 1) + (tok >> 1)) * 8192 + (g >> 1) * 32 + (tok & 1) * 16,
                                             (j * 32 + 16 * s) * 8192 + p * 256 + cw * 64);
    }
  }
  TR(3);
  v4u stat = {pk_bf(-m, -m), pk_bf(-m, sd), f2u(rs * rs), 0u};
  pin(stat);
  SB();
  publish(gflag(a, GF_U + (j * 32 + p) * 4 + cw), tagc, lane);
  __builtin_amdgcn_s_setprio(0);
  SB();
  TR(4);
  float kq, kd, kinv;
  {
    const float csum = wave_sum((__builtin_fabsf(bf_lo(cre)) + __builtin_fabsf(bf_hi(cre))) + (__builtin_fabsf(bf_lo(cim)) + __builtin_fabsf(bf_hi(cim))));
    float cs, cinv, ks;
    pow2_scale(csum, cs, cinv);
    pow2_scale(__builtin_fmaf(2.f * dt, csum, __builtin_fabsf(dsk)), ks, kinv);
    kq = cinv * dt * (2.f / 512.f) * ks; kd = (lane | mh) == 0 ? dsk * ks : 0.f;
    const cpx E1 = cexp_p(zr2, -zi2, 1.f), E256 = cexp_p(zr2, -zi2, 256.f);
    auto put = [&](int row, cpx v) LAM { pv[row] = pk_h(v.r, v.i); pin(pv[row]); };
    cpx X[8]; X[0] = cpx{bf_hi(cre << sh) * cs, bf_hi(cim << sh) * -cs};
#pragma unroll
    for (int o = 1; o < 8; o++) X[o] = cmul(X[o - 1], E1);
#pragma unroll
    for (int o = 0; o < 8; o++) put(o, X[o]);
    cpx A = cmul(X[0], E256), B = cmul(X[4], E256);
    put(8, A); put(9, cmul(X[2], E256)); put(10, B); put(11, cmul(X[6], E256));
    A = cmul(A, E256); B = cmul(B, E256); put(12, A); put(13, B);
    A = cmul(A, E256); B = cmul(B, E256); put(14, A); put(15, B);
#pragma unroll
    for (int o = 16; o < 21; o++) { A = cmul(A, E256); put(o, A); }
    pin(kq); pin(kd); pin(kinv);
  }
  LAS char* pa0 = opq(sm + L_V + ch * SCR + tok * SROW + g * 16);
  LAS char* pa1 = opq(sm + L_V + ch * SCR + min(16 + tok, 20) * SROW + g * 16);
  LAS char* pb = opq(sm + L_V + ch * SCR + (21 + 16 * mh + tok) * SROW + g * 16);
  f32x4 k0 = zero4(), k1 = zero4();
  const int bq = 16 * mh + tok;
  LAS char* tb = sm + L_TAB + ch * TABB;
  LAS char* t0a = opq(tb + 2 * (g < 2 ? 8 * bq + 4 * g : (g == 2 ? 256 + 4 * bq : 384 + 2 * bq)));
  LAS char* t0b = opq(t0a + (g == 3 ? 128 : 4));
  LAS char* t1a = opq(tb + 2 * (g == 0 ? 512 + bq : (g == 1 ? 640 + bq : 672 + bq)));
  const int t1s = g == 0 ? 64 : 0;
  TR(12);
  lds_wait<F_XD, 1>(fa);
  lds_wait<F_FULL, need_W(15)>(fa);
  if (g == 0) *(LAS v4u*)(sm + L_STAT + (16 * cw + tok) * 16) = stat;
  lds_put(FLG + F_XD + 4 * cw, 2);
  lds_wait<F_FREE, FREE_SCR>(fa);
  TR(5);
  set_m0(L_X + ch * SCR + mh * 256);
  static_for<0, 53>([&](auto R) LAM { constexpr int r = R; put_tid<8192 + r * SROW>(pv[r]); });
  lds_put(fme, 1);
  lds_wait<0, 1>(fpa);
  TR(6);
  {
    v4u f0[8], f1[8], fb[8];
#pragma unroll
    for (int s = 0; s < 8; s++) { fb[s] = *(LAS v4u*)(pb + 64 * s); f0[s] = *(LAS v4u*)(pa0 + 64 * s); f1[s] = *(LAS v4u*)(pa1 + 64 * s); }
    SB();
#pragma unroll
    for (int s = 0; s < 8; s++) { mfma_h(k0, f0[s], fb[s]); mfma_h(k1, f1[s], fb[s]); }
  }
  MFMA_DONE();
  {
    const float kz = g < 2 ? kq : 0.f;
    *(LAS unsigned*)t0a = pk_h(__builtin_fmaf(k0[0], kq, kd), k0[1] * kq);
    *(LAS unsigned*)t0b = pk_h(k0[2] * kq, k0[3] * kq);
    const unsigned q0 = pk_h(k1[0] * kz, k1[1] * kz), q1 = pk_h(k1[2] * kz, k1[3] * kz);
    *(LAS u16*)(t1a) = (u16)q0; *(LAS u16*)(t1a + t1s) = (u16)(q0 >> 16); *(LAS u16*)(t1a + 2 * t1s) = (u16)q1; *(LAS u16*)(t1a + 3 * t1s) = (u16)(q1 >> 16);
  }
  lds_put(fme, 2);
  TR(7);
  lds_wait<0, 2>(fpa);
  LAS char* cr = sm + ch * CH;
  {
    unsigned P[9], Q[8];
#pragma unroll
    for (int i = 0; i < 9; i++) P[i] = (unsigned)*(LAS u16*)(size_t)ga[2 * i] | ((unsigned)*(LAS u16*)(size_t)ga[2 * i + 1] << 16);
#pragma unroll
    for (int i = 0; i < 8; i++) Q[i] = __builtin_amdgcn_alignbit(P[i + 1], P[i], 16);
    LAS char* kc = cr + x0 * 2;
    *(LAS v4u*)(kc) = v4u{P[0], P[1], P[2], P[3]}; *(LAS v4u*)(kc + 16) = v4u{P[4], P[5], P[6], P[7]};
    *(LAS v4u*)(kc + KCOPY) = v4u{Q[0], Q[1], Q[2], Q[3]}; *(LAS v4u*)(kc + KCOPY + 16) = v4u{Q[4], Q[5], Q[6], Q[7]};
  }
  lds_put(fme, 3);
  LAS char* ab[4];
#pragma unroll
  for (int i = 0; i < 4; i++) ab[i] = opq(cr + ((15 - tok) & 1) * KCOPY + 2 * (((15 - tok) & ~1) + 8 * g) + 64 * (4 - 4 * mh) + 1024 * i);
  LAS char* bb = opq(cr + C_U + 448 + 32 * tok + 16 * g - 64 * (4 * mh + 3));
  f32x4 acc[8];
#pragma unroll
  for (int c = 0; c < 8; c++) acc[c] = zero4();
  auto lda = [&](int i, int r, int k) LAM { return ld2(ab[(7 - i) >> 1] + 64 * (59 - 8 * i - r) - 1024 * ((7 - i) >> 1) + 8 * k); };
  TR(13);
  __builtin_amdgcn_s_setprio(3);
  lds_wait<0, 3>(fpa);
  lds_wait<0, 1>((unsigned)(FLG + F_YRDY));
  TR(8);
  v2u A[8][2]; v4u B[2][8];
#pragma unroll
  for (int i = 0; i < 8; i++) {
    B[0][i] = *(LAS v4u*)(bb + 64 * (8 * i + 3));
    A[7 - i][0] = lda(7 - i, 0, 0); A[7 - i][1] = lda(7 - i, 0, 1);
  }
  static_for<0, 4>([&](auto R) LAM {
    constexpr int r = R;
    static_for<0, 8>([&](auto II) LAM {
      constexpr int i = 7 - II;
      static_for<0, 8 - i>([&](auto K) LAM {
        constexpr int k = K;
        SB();
        mfma_h(acc[i + k], v4u{A[i][0].x, A[i][0].y, A[i][1].x, A[i][1].y}, B[r & 1][k]);
        SB();
        if constexpr (r < 3) {
          if constexpr (i < 7 && k < 2) A[i + 1][k] = lda(i + 1, r + 1, k);
          if constexpr (i <= 4 && i >= 1 && (k == 2 || k == 3)) B[(r + 1) & 1][2 * (4 - i) + k - 2] = *(LAS v4u*)(bb + 64 * (8 * (2 * (4 - i) + k - 2) + 3 - (r + 1)));
        }
        if constexpr (r == 3 && i == 0 && k == 1) lds_put(fme, 4);
      });
    });
    if constexpr (r < 3) { A[0][0] = lda(0, r + 1, 0); A[0][1] = lda(0, r + 1, 1); }
  });
  MFMA_DONE();
  TR(9);
  lds_wait<0, 4>(fpa);
  LAS char* exm = cr + mh * 4096;
  LAS char* exo = cr + (mh ^ 1) * 4096;
#pragma unroll
  for (int c = 0; c < 4; c++) *(LAS f32x4*)(exm + c * 1024 + lane * 16) = sel4(mh, acc[c], acc[4 + c]);
  lds_put(fme, 5);
  lds_wait<0, 5>(fpa);
  LAS char* yb = cr + 8192 + mh * 2048;
#pragma unroll
  for (int c = 0; c < 4; c++) {
    const f32x4 o = *(LAS f32x4*)(exo + c * 1024 + lane * 16), mm = sel4(mh, acc[4 + c], acc[c]);
    *(LAS v2u*)(yb + 512 * c + 32 * tok + 8 * g) = v2u{pk_bf((mm[0] + o[0]) * kinv, (mm[1] + o[1]) * kinv), pk_bf((mm[2] + o[2]) * kinv, (mm[3] + o[3]) * kinv)};
  }
  CFENCE();
  const rsrc_t ry = mk_rsrc(a.ws + WS_Y, SEQ * DM * 2);
#pragma unroll
  for (int i = 0; i < 2; i++) st_sc1(*(LAS v4u*)(yb + i * 1024 + lane * 16), ry, lane * 16 + i * 1024, d * 4096 + mh * 2048);
  lds_put(fme, 6);
  TR(10);
  publish(gflag(a, GF_Y + (j * 2 + mh) * 64 + 2 * p + ch), tagc, lane);
  TR(11);
  if (cw == 0) TR_DUMP(48, 14);
}

__global__ __launch_bounds__(NTHR, 3) NOPK void k_fused(Args a) {
  __shared__ __attribute__((aligned(16))) char smem[LDS_BYTES];
  LAS char* sm = (LAS char*)smem;
  const int lane = threadIdx.x & 63, w = __builtin_amdgcn_readfirstlane(threadIdx.x >> 6);
  const int b = blockIdx.x, p = (b & 7) * 4 + (b >> 6), j = (b >> 3) & 7;
  unsigned fa = FLG + 4 * (lane & 3);
  pin(fa);
  if (w == 0) { ((LAS unsigned*)(sm + FLG))[lane] = 0; LGKM0(); }
  __builtin_amdgcn_s_barrier();
  if (w >= 8) { __builtin_amdgcn_s_setprio(1); role_loader(a, sm, w - 8, lane, p, j, fa); }
  else if (w < 4) { __builtin_amdgcn_s_setprio(2); role_gemm(a, sm, w, lane, p, j, fa); }
  else { role_conv(a, sm, w - 4, lane, p, j, fa); }
}

extern "C" void kernel_launch(void* const* d_in, const int* in_sizes, int n_in, void* d_out, int out_size, void* d_ws, size_t ws_size, hipStream_t stream) {
  Args a;
  a.x = (const u16*)d_in[0]; a.lar = (const u16*)d_in[1]; a.aim = (const u16*)d_in[2]; a.cre = (const u16*)d_in[3]; a.cim = (const u16*)d_in[4];
  a.ldt = (const u16*)d_in[5]; a.dsk = (const u16*)d_in[6]; a.w1 = (const u16*)d_in[8]; a.b1 = (const u16*)d_in[9]; a.w2 = (const u16*)d_in[10];
  a.b2 = (const u16*)d_in[11]; a.out = (u16*)d_out; a.ws = (char*)d_ws;
  k_fused<<<256, NTHR, 0, stream>>>(a);
}
```

```cpp
#include <hip/hip_runtime.h>
#include <stdint.h>
#include <type_traits>

#define LAS __attribute__((address_space(3)))
#define GAS __attribute__((address_space(1)))
#if defined(__HIP_DEVICE_COMPILE__) && __HIP_DEVICE_COMPILE__
#define NOPK __attribute__((target("no-packed-fp32-ops")))
#else
#define NOPK
#endif
#define DEV __device__ __forceinline__ NOPK
#define LAM __attribute__((always_inline)) NOPK
typedef unsigned short u16;
typedef unsigned long long u64;
typedef unsigned v4u __attribute__((ext_vector_type(4)));
typedef unsigned v2u __attribute__((ext_vector_type(2)));
typedef unsigned v2u4 __attribute__((ext_vector_type(2), aligned(4)));
typedef __bf16 bf16x8 __attribute__((ext_vector_type(8)));
typedef _Float16 f16x8 __attribute__((ext_vector_type(8)));
typedef float f32x4 __attribute__((ext_vector_type(4)));
typedef short s16x4 __attribute__((ext_vector_type(4)));
typedef short s16x2 __attribute__((ext_vector_type(2)));
typedef __amdgpu_buffer_rsrc_t rsrc_t;

constexpr int SEQ = 2048, DM = 512, NMODE = 128, FF = 1376, NKS = 43;
constexpr size_t WS_H = 0, WS_U = 6u << 20, WS_Y = 8u << 20, WS_FLAG = 10u << 20, WS_DIAG = 12u << 20;
constexpr int GF_U = 0, GF_H = 1024, GF_Y = 2048, GF_EC = 3072, GF_ER = 3200;
#define FSTR 1
#define PSLEEP 8
#define UWAIT 1
#define HDELAY 12
constexpr int NTHR = 768;
constexpr int POLL_CAP = 1 << 14;
constexpr int AUX_SC1 = 16;

struct Args {
  const u16 *x, *lar, *aim, *cre, *cim, *ldt, *dsk, *w1, *b1, *w2, *b2;
  u16* out;
  char* ws;
};

#define TR_DECL unsigned tr_[24]
#define TR(i) do { if ((i) == 0 || (i) == PIDX) { SB(); u64 t_; asm volatile("s_memrealtime %0\n\ts_waitcnt lgkmcnt(0)" : "=s"(t_) :: "memory"); tr_[i] = (unsigned)t_; pins(tr_[i]); SB(); } } while (0)
#define TR_DUMP(base, n) do { if ((base) == PBASE) { for (int it_ = 0; it_ < 200000; it_++) { u64 t_; asm volatile("s_memrealtime %0\n\ts_waitcnt lgkmcnt(0)" : "=s"(t_) :: "memory"); if ((unsigned)t_ - tr_[0] >= 4000u + (tr_[PIDX < n ? PIDX : 0] - tr_[0])) break; } } } while (0)
#define PBASE 16
#define PIDX 0

template <int I, int N, class Fn> DEV void static_for(Fn&& f) {
  if constexpr (I < N) { f(std::integral_constant<int, I>{}); static_for<I + 1, N>(f); }
}
#define CE(x) (std::integral_constant<int, (x)>::value)
template <class T> DEV void pin(T& v) { asm volatile("" : "+v"(v)); }
template <class T> DEV void pins(T& v) { asm volatile("" : "+s"(v)); }
DEV float u2f(unsigned u) { return __builtin_bit_cast(float, u); }
DEV unsigned f2u(float f) { return __builtin_bit_cast(unsigned, f); }
DEV bool wany(bool c) { return __builtin_amdgcn_ballot_w64(c) != 0; }
DEV rsrc_t mk_rsrc(const void* p, int bytes) { return __builtin_amdgcn_make_buffer_rsrc((void*)p, 0, bytes, 0x00020000); }
DEV float bf_lo(unsigned u) { return u2f(u << 16); }
DEV float bf_hi(unsigned u) { return u2f(u & 0xffff0000u); }
typedef float f32x2 __attribute__((ext_vector_type(2)));
typedef __bf16 bf16x2 __attribute__((ext_vector_type(2)));
typedef _Float16 f16x2 __attribute__((ext_vector_type(2)));
DEV unsigned pk_bf(float a, float b) { return __builtin_bit_cast(unsigned, __builtin_convertvector(f32x2{a, b}, bf16x2)); }
DEV unsigned pk_h(float a, float b) { return __builtin_bit_cast(unsigned, __builtin_convertvector(f32x2{a, b}, f16x2)); }
DEV unsigned pk_hn(float a, float b) { return pk_h(a, -b); }
DEV float rbf(float a) { return u2f(pk_bf(0.f, a)); }
DEV unsigned relu_pk(unsigned v) { return __builtin_bit_cast(unsigned, __builtin_elementwise_max(__builtin_bit_cast(s16x2, v), s16x2{0, 0})); }
DEV v2u tr_read(LAS char* p) { return __builtin_bit_cast(v2u, __builtin_amdgcn_ds_read_tr16_b64_v4i16((LAS s16x4*)p)); }
DEV v2u ld2(LAS char* p) { return *(LAS v2u4*)p; }
DEV float xsum16(float v) { auto r = __builtin_amdgcn_permlane16_swap(f2u(v), f2u(v), false, false); return u2f(r[0]) + u2f(r[1]); }
DEV float xsum32(float v) { auto r = __builtin_amdgcn_permlane32_swap(f2u(v), f2u(v), false, false); return u2f(r[0]) + u2f(r[1]); }
template <int CTRL> DEV float dpp_add(float v) { return v + u2f((unsigned)__builtin_amdgcn_update_dpp(0, (int)f2u(v), CTRL, 0xf, 0xf, false)); }
DEV float wave_sum(float v) { v = dpp_add<0x121>(v); v = dpp_add<0x122>(v); v = dpp_add<0x124>(v); v = dpp_add<0x128>(v); return xsum32(xsum16(v)); }
DEV f32x4 sel4(int c, f32x4 a, f32x4 b) { return f32x4{c ? a[0] : b[0], c ? a[1] : b[1], c ? a[2] : b[2], c ? a[3] : b[3]}; }
DEV LAS char* opq(LAS char* p) { unsigned v = (unsigned)(size_t)p; pin(v); return (LAS char*)(size_t)v; }
DEV f32x4 zero4() { f32x4 z = {0, 0, 0, 0}; pin(z); return z; }
#define VMCNT(n) asm volatile("s_waitcnt vmcnt(%0)" ::"i"(n) : "memory")
#define LGKM0() asm volatile("s_waitcnt lgkmcnt(0)" ::: "memory")
#define CFENCE() asm volatile("" ::: "memory")
#define SB() __builtin_amdgcn_sched_barrier(0)
DEV void mfma_bf(f32x4& c, v4u a, v4u b) { asm volatile("v_mfma_f32_16x16x32_bf16 %0, %1, %2, %0" : "+v"(c) : "v"(a), "v"(b)); }
DEV void mfma_h(f32x4& c, v4u a, v4u b) { asm volatile("v_mfma_f32_16x16x32_f16 %0, %1, %2, %0" : "+v"(c) : "v"(a), "v"(b)); }
#define NOP2() do { SB(); asm volatile("s_nop 1"); SB(); } while (0)
#define MFMA_DONE() do { SB(); asm volatile("s_nop 7"); SB(); } while (0)

DEV void st_sc1(v4u v, rsrc_t r, int voff, int soff) { __builtin_amdgcn_raw_buffer_store_b128(v, r, voff, soff, AUX_SC1); NOP2(); }
DEV void lds_put(unsigned addr, unsigned v) { asm volatile("s_mov_b64 exec, 1\n\tds_write_b32 %0, %1\n\ts_mov_b64 exec, -1" ::"v"(addr), "v"(v) : "memory"); }
template <int OFF, int WANT> DEV void lds_wait(unsigned fa) {
  unsigned t, n;
  asm volatile("s_mov_b32 %1, 0x20000\n"
               "1:\n\tds_read_b32 %0, %2 offset:%3\n\ts_waitcnt lgkmcnt(0)\n\tv_cmp_gt_u32 vcc, %4, %0\n\ts_cbranch_vccz 2f\n\t"
               "s_sleep 1\n\ts_sub_u32 %1, %1, 1\n\ts_cbranch_scc0 1b\n2:"
               : "=&v"(t), "=&s"(n) : "v"(fa), "i"(OFF), "i"(WANT) : "memory", "vcc", "scc");
}
template <int OFF> DEV void lds_wait_s(unsigned fa, int want) {
  unsigned t, n;
  asm volatile("s_mov_b32 %1, 0x20000\n"
               "1:\n\tds_read_b32 %0, %2 offset:%3\n\ts_waitcnt lgkmcnt(0)\n\tv_cmp_gt_u32 vcc, %4, %0\n\ts_cbranch_vccz 2f\n\t"
               "s_sleep 1\n\ts_sub_u32 %1, %1, 1\n\ts_cbranch_scc0 1b\n2:"
               : "=&v"(t), "=&s"(n) : "v"(fa), "i"(OFF), "s"(want) : "memory", "vcc", "scc");
}
static __device__ const unsigned kSeq[64] = {0,1,2,3,4,5,6,7,8,9,10,11,12,13,14,15,16,17,18,19,20,21,22,23,24,25,26,27,28,29,30,31,32,
  33,34,35,36,37,38,39,40,41,42,43,44,45,46,47,48,49,50,51,52,53,54,55,56,57,58,59,60,61,62,63};
DEV v4u srd(const void* p, unsigned bytes) { const u64 v = (u64)p; return v4u{(unsigned)v, (unsigned)(v >> 32) & 0xffffu, bytes, 0x00020000u}; }
DEV void dma16(v4u rs, unsigned lds, int voff, int soff) {
  asm volatile("s_mov_b32 m0, %0\n\ts_nop 0\n\tbuffer_load_dwordx4 %1, %2, %3 offen lds" ::"s"(lds), "v"(voff), "s"(rs), "s"(soff) : "memory");
}
template <int N> DEV void dma_flag(v4u rs, unsigned lds) {
  asm volatile("s_mov_b32 m0, %0\n\ts_mov_b64 exec, 1\n\tbuffer_load_dword off, %1, %2 lds\n\ts_mov_b64 exec, -1" ::"s"(lds), "s"(rs), "s"(N * 4) : "memory");
}
DEV void dma_ep(v4u rs, unsigned lds, int voff) {
  asm volatile("s_mov_b32 m0, %0\n\ts_mov_b64 exec, 3\n\tbuffer_load_dword %2, %1, 0 offen sc1 lds\n\ts_mov_b64 exec, -1" ::"s"(lds), "s"(rs), "v"(voff) : "memory");
}
template <int OFF> DEV void put_tid(unsigned v) { asm volatile("ds_write_addtid_b32 %0 offset:%1" ::"v"(v), "i"(OFF) : "memory"); }
DEV void set_m0(unsigned v) { asm volatile("s_mov_b32 m0, %0\n\ts_nop 0" ::"s"(v) : "memory"); }
DEV GAS u64* gflag(const Args& a, int line) { return (GAS u64*)(a.ws + WS_FLAG) + line * FSTR; }
DEV void publish(GAS u64* f, u64 tag, int lane) {
  VMCNT(0);
  if (lane == 0) __hip_atomic_store(f, tag, __ATOMIC_RELAXED, __HIP_MEMORY_SCOPE_AGENT);
}
DEV void poll(GAS u64* p, u64 tag) {
#pragma nounroll
  for (int it = 0; it < POLL_CAP; it++) {
    if (!wany(__hip_atomic_load(p, __ATOMIC_RELAXED, __HIP_MEMORY_SCOPE_AGENT) != tag)) break;
    __builtin_amdgcn_s_sleep(PSLEEP);
  }
  __builtin_amdgcn_fence(__ATOMIC_ACQUIRE, "wavefront");
}
DEV void poll2(GAS u64* p, GAS u64* q, u64 tag) {
#pragma nounroll
  for (int it = 0; it < POLL_CAP; it++) {
    const u64 x = __hip_atomic_load(p, __ATOMIC_RELAXED, __HIP_MEMORY_SCOPE_AGENT), y = __hip_atomic_load(q, __ATOMIC_RELAXED, __HIP_MEMORY_SCOPE_AGENT);
    if (!wany(x != tag || y != tag)) break;
    __builtin_amdgcn_s_sleep(PSLEEP);
  }
  __builtin_amdgcn_fence(__ATOMIC_ACQUIRE, "wavefront");
}
DEV int unit_start(int j) { return (j * 37 + 3) / 7; }

constexpr int KCOPY = 4160, C_U = 2 * KCOPY, CH = C_U + 4608, L_TAB = 28160, TABB = 1408, L_STAT = 31744;
constexpr int L_X = 32768, L_RING = L_X + 65536, SROW = 528, SCR = 53 * SROW;
constexpr int L_Y = 0, YROW = 160, L_EX = L_Y + 64 * YROW;
constexpr int LDS_BYTES = 163840, FLG = LDS_BYTES - 256;
static_assert(2 * SCR <= 7 * 8192 && 2 * CH <= L_TAB && L_TAB + 2 * TABB <= L_STAT && L_EX + 16384 <= L_STAT && L_RING + 61440 <= FLG, "lds");
constexpr int F_FULL = 0, F_FREE = 16, F_XD = 32, F_CW = 48, F_YRDY = 64, F_GX = 80, F_EP = 96;

constexpr int NSTEP1 = 24;
constexpr int ST_KIND[NSTEP1] = {0, 1, 1, 0, 0, 1, 0, 1, 0, 1, 0, 0, 1, 0, 1, 1, 1, 1, 1, 1, 1, 1, 1, 1};
constexpr int st_idx(int s) { int n = 0; for (int i = 0; i < s; i++) n += ST_KIND[i] == ST_KIND[s]; return n; }
constexpr int step_of(int kind, int idx) { for (int s = 0; s < NSTEP1; s++) if (ST_KIND[s] == kind && st_idx(s) == idx) return s; return -1; }
constexpr int need_X(int c) { return step_of(0, c) + 1; }
constexpr int need_W(int u) { return step_of(1, u) + 1; }
constexpr int need_V(int t) { return NSTEP1 + t + 1; }
static_assert(need_X(1) < need_W(2) && need_X(2) < need_W(4) && need_X(3) < need_W(6) && need_X(7) < need_W(14), "x before its units");
constexpr int w_addr(int u) { return u % 7 < 5 ? L_RING + (u % 7) * 12288 : (u % 7 - 5) * 12288; }
constexpr int FREE_SCR = 14;
constexpr bool free_sent(int n) { return n <= 9 || n == FREE_SCR || n == 16; }
constexpr int x_addr(int c) { return L_X + ((c + 1) & 7) * 8192; }
constexpr int NVB = 14, L_V = L_X + 8192, VH = 7;
constexpr int PD = 8, VR = 8;
#define VDEPTH 8

DEV u64 tag_of(LAS char* sm, int which);
DEV void role_loader(const Args& a, LAS char* sm, int l, int lane, int p, int j, unsigned fa) {
  const v4u rx = srd(a.x, SEQ * DM * 2), rw = srd(a.w1, DM * FF * 2), rq = srd(kSeq, 256);
  int vx[2], vw[3];
#pragma unroll
  for (int i = 0; i < 2; i++) vx[i] = (8 * i + (lane >> 3)) * 1024 + (((lane & 7) ^ (4 * i + (lane >> 4))) * 16);
  const int sx = p * 65536 + l * 16384, sw = unit_start(j) * 64;
  TR_DECL;
  static_for<0, NSTEP1>([&](auto S) LAM {
    constexpr int s = S, kind = ST_KIND[s], idx = st_idx(s);
    if constexpr (s % 4 == 0) TR(s / 4);
    if constexpr (kind == 0) {
      const int so = sx + ((idx + j + 1) & 7) * 128;
#pragma unroll
      for (int i = 0; i < 2; i++) dma16(rx, CE(x_addr(idx)) + l * 2048 + i * 1024, vx[i], so);
    } else {
      if constexpr (idx >= 7) lds_wait<F_FREE, idx - 6>(fa);
      const int so = sw + ((((idx >> 1) + j + 1) & 7) * 64 + (idx & 1) * 32) * (FF * 2);
#pragma unroll
      for (int n = 0; n < 3; n++) {
        if constexpr (idx == 0) {
          const int sg = 16 * (3 * l + n) + (lane >> 2), r = (sg * 171) >> 10, qp = sg - 6 * r;
          vw[n] = r * (FF * 2) + ((qp ^ ((r >> 1) & 1)) * 64) + (((lane & 3) ^ (2 * ((r >> 3) & 1))) * 16);
        }
        dma16(rw, CE(w_addr(idx)) + l * 3072 + n * 1024, vw[n], so);
      }
      if constexpr (idx == 0) {
        if (l < 2) {
          const v4u re = srd(a.ws + WS_FLAG + (size_t)(l ? GF_ER + 16 * p : GF_EC + 16 * j) * 8 * FSTR, 8);
          dma_ep(re, FLG + F_EP + 8 * l, lane * 4);
        }
      }
    }
    if constexpr (kind == 1 || idx == 7) dma_flag<s + 1>(rq, FLG + F_FULL + 4 * l);
  });
  TR(6);
  if (l < 3) {
    const rsrc_t rv = mk_rsrc(a.w2, DM * FF * 2);
    const int vv = (lane >> 3) * 1024 + (lane & 7) * 16 + j * 128;
    v4u wr[VR][4];
    auto fetch = [&](int n) LAM {
      const int v = min(3 * n + l, 42), so = ((v >> 1) + 22 * (v & 1)) * 32768;
#pragma unroll
      for (int r = 0; r < 4; r++) wr[n % VR][r] = __builtin_amdgcn_raw_buffer_load_b128(rv, vv, so + r * 8192, 0);
    };
#pragma unroll
    for (int n = 0; n < VR; n++) { fetch(n); SB(); VMCNT(VDEPTH); }
    SB();
    TR(7);
    LAS char* wa[2];
#pragma unroll
    for (int i = 0; i < 2; i++) wa[i] = sm + L_V + (lane >> 3) * 128 + (((lane & 7) ^ ((((lane >> 4) & 1) | (i << 1)) << 1)) * 16);
    lds_wait<F_FREE, 16>(fa);
    lds_wait<F_CW, 3>(fa);
    TR(8);
    static_for<0, 15>([&](auto N) LAM {
      constexpr int n = N;
      if constexpr (n == 9) TR(9);
      const int v = 3 * n + l;
      if (n < 14 || l == 0) {
        if constexpr (n >= 9) lds_wait_s<F_FREE>(fa, 17 + (v >> 1) - NVB);
        const int lo = (v >= 2 * NVB ? v - 2 * NVB : v) * 4096;
#pragma unroll
        for (int r = 0; r < 4; r++) *(LAS v4u*)(wa[r & 1] + lo + r * 1024) = wr[n % VR][r];
        lds_put(FLG + F_FULL + 4 * l, n >= 13 && v + 3 > 42 ? 63 : NSTEP1 + ((v + 3) >> 1));
        if constexpr (n + VR < 15) fetch(n + VR);
        SB();
      }
    });
    TR(10);
    if (l == 0) TR_DUMP(0, 11);
    return;
  }
  const u64 tagc = tag_of(sm, 0);
  v4u uv[8];
  lds_wait<F_CW, UWAIT>(fa);
  poll2(gflag(a, GF_U + j * 128 + lane), gflag(a, GF_U + j * 128 + 64 + lane), tagc);
  TR(7);
  const rsrc_t ru = mk_rsrc(a.ws + WS_U, SEQ * DM * 2), ry = mk_rsrc(a.ws + WS_Y, SEQ * DM * 2);
#pragma unroll
  for (int i = 0; i < 8; i++) uv[i] = __builtin_amdgcn_raw_buffer_load_b128(ru, lane * 16 + (i & 3) * 1024, (j * 32 + p) * 8192 + (i >> 2) * 4096, AUX_SC1);
  lds_wait<F_FREE, FREE_SCR>(fa);
  lds_put(FLG + F_FULL + 12, 63);
  if (lane < 60) *(LAS v4u*)(sm + (lane & 1) * CH + C_U + (lane >> 1) * 16) = v4u{0, 0, 0, 0};
  if (lane < 8) *(LAS v4u*)(sm + (lane & 1) * CH + ((lane >> 1) & 1) * KCOPY + 4096 + (lane >> 2) * 16) = v4u{0, 0, 0, 0};
#pragma unroll
  for (int i = 0; i < 8; i++) *(LAS v4u*)(sm + (lane & 1) * CH + C_U + 480 + i * 512 + (lane >> 1) * 16) = uv[i];
  lds_put(FLG + F_YRDY, 1);
  TR(8);
  GAS u64* pf = gflag(a, GF_Y + (j * 2 + (p >> 4)) * 64 + lane);
  pin(pf);
  lds_wait<F_CW, 6>(fa);
  TR(9);
  __builtin_amdgcn_s_sleep(24);
  poll(pf, tagc);
  TR(10);
#pragma unroll
  for (int i = 0; i < 8; i++) uv[i] = __builtin_amdgcn_raw_buffer_load_b128(ry, (lane >> 3) * 4096 + (lane & 7) * 16, (64 * j + 8 * i) * 4096 + p * 128, AUX_SC1);
#pragma unroll
  for (int i = 0; i < 8; i++) *(LAS v4u*)(sm + L_Y + (8 * i + (lane >> 3)) * YROW + (lane & 7) * 16) = uv[i];
  lds_put(FLG + F_YRDY, 2);
  TR(11);
  TR_DUMP(32, 12);
}

DEV u64 tag_of(LAS char* sm, int which) {
  const v2u e = *(LAS v2u*)(sm + FLG + F_EP + 8 * which);
  const u64 v = ((u64)(unsigned)__builtin_amdgcn_readfirstlane(e.y) << 32) | (unsigned)__builtin_amdgcn_readfirstlane(e.x);
  return (v + 1) * 0x9E3779B97F4A7C15ull;
}

DEV void role_gemm(const Args& a, LAS char* sm, int w, int lane, int p, int j, unsigned fa) {
  const int g = lane >> 4, tok = lane & 15, q4 = tok >> 2, pp = lane & 3;
  const v4u rq = srd(kSeq, 256);
  const unsigned fre = FLG + F_FREE + 4 * w;
  volatile LAS unsigned* full = (volatile LAS unsigned*)(size_t)(fa + F_FULL);
  const rsrc_t rh = mk_rsrc(a.ws + WS_H, 32 * NKS * 4096);
  const int ks = w >> 1, ah = w & 1;
  TR_DECL; TR(0);
  u64 tagr;
  {
    const int us = unit_start(j), nown = (j == 7 ? NKS : unit_start(j + 1)) - us;
    unsigned bias[3];
#pragma unroll
    for (int i = 0; i < 3; i++) bias[i] = *(const GAS u16*)(a.b1 + us * 32 + (3 * w + i) * 16 + tok);
    LAS char* wb[2][3];
#pragma unroll
    for (int i = 0; i < 3; i++) {
      const int ft = 3 * w + i;
      wb[0][i] = opq(sm + (6 * (8 * g + q4) + ((ft >> 1) ^ ((q4 >> 1) & 1))) * 64 + (((ft & 1) ^ (g & 1)) * 32) + 8 * pp);
      wb[1][i] = opq(wb[0][i] + L_RING);
    }
    LAS char* xb[2];
#pragma unroll
    for (int s = 0; s < 2; s++) xb[s] = opq(sm + L_X + tok * 128 + (((4 * s + g) ^ ((tok >> 1) & 7)) * 16));
    int hoffv = (g & 1) * 1024 + (g >> 1) * 256 + tok * 16; pin(hoffv);
    f32x4 acc[3][4], cs[3];
#pragma unroll
    for (int i = 0; i < 3; i++) { cs[i] = zero4();
#pragma unroll
      for (int T = 0; T < 4; T++) acc[i][T] = zero4(); }
    v4u ones = {0x3f803f80u, 0x3f803f80u, 0x3f803f80u, 0x3f803f80u};
    pin(ones);
#pragma unroll
    for (int i = 0; i < 3; i++) { bias[i] = g ? 0u : bias[i] << 16; pin(bias[i]); }
    v2u wfr[2][3][2]; v4u xfr[2][4];
    auto rd = [&](auto U, auto K) LAM {
      constexpr int u = U, k = K, wa = w_addr(u), hi = wa >= L_RING;
      if constexpr (k >= 2 && k < 6) xfr[u & 1][k - 2] = *(LAS v4u*)(xb[u & 1] + (CE(x_addr(u >> 1)) - L_X) + (k - 2) * 2048);
      else { constexpr int i = k < 2 ? 0 : (k - 4) >> 1; wfr[u & 1][i][k & 1] = tr_read(wb[hi][i] + (wa - hi * L_RING) + (k & 1) * 1536); }
    };
    lds_wait<F_FULL, need_W(0)>(fa);
    TR(1);
    static_for<0, 10>([&](auto K) LAM { rd(std::integral_constant<int, 0>{}, K); });
    tagr = tag_of(sm, 1);
    lds_wait<F_FULL, need_W(1)>(fa);
    dma_flag<1>(rq, fre);
    static_for<0, 16>([&](auto U) LAM {
      constexpr int u = U, b = u & 1;
      if constexpr (u == 4 || u == 8 || u == 12) TR(2 + (u - 4) / 4);
      unsigned fv; bool lack = false;
      if constexpr (u < 14) fv = *full;
      static_for<0, 15>([&](auto M) LAM {
        constexpr int m = M, i = m / 5, T = m % 5;
        const v4u wv = {wfr[b][i][0].x, wfr[b][i][0].y, wfr[b][i][1].x, wfr[b][i][1].y};
        SB();
        if constexpr (T < 4) mfma_bf(acc[i][T], wv, xfr[b][T]); else mfma_bf(cs[i], ones, wv);
        SB();
        if constexpr (u < 15 && m < 10) rd(std::integral_constant<int, u + 1>{}, M);
        if constexpr (u < 14 && m == 11) lack = wany(fv < CE(need_W(u + 2)));
        if constexpr (u + 2 <= 9 && m == 13) { LGKM0(); dma_flag<u + 2>(rq, fre); }
        if constexpr ((u + 2 == FREE_SCR || u + 2 == 16) && m == 10) { VMCNT(0); lds_put(fre, u + 2); }
      });
      SB();
      if constexpr (u < 14) { if (__builtin_expect(lack, 0)) lds_wait<F_FULL, need_W(u + 2)>(fa); CFENCE(); }
    });
    MFMA_DONE();
    TR(5);
    lds_wait<F_XD, 2>(fa);
    v4u sb[4];
#pragma unroll
    for (int T = 0; T < 4; T++) sb[T] = *(LAS v4u*)(sm + L_STAT + (16 * T + tok) * 16);
    v4u av[3];
#pragma unroll
    for (int i = 0; i < 3; i++) {
      const float c = g ? 0.f : cs[i][0];
      const float r1 = c - rbf(c), r2 = r1 - rbf(r1);
      av[i] = v4u{pk_bf(c, r1), pk_bf(r2, u2f(bias[i])), 0u, 0u};
    }
#pragma unroll
    for (int T = 0; T < 4; T++) sb[T].z = 0;
    pin(av[0]); pin(av[1]); pin(av[2]); pin(sb[0]); pin(sb[1]); pin(sb[2]); pin(sb[3]);
    NOP2();
#pragma unroll
    for (int i = 0; i < 3; i++)
#pragma unroll
      for (int T = 0; T < 4; T++) mfma_bf(acc[i][T], av[i], sb[T]);
    SB();
#pragma unroll
    for (int i = 0; i < 3; i++) {
      const int ft = 3 * w + i;
      const int so = (p * NKS + us + (ft >> 1)) * 4096 + (ft & 1) * 512;
      const int vo = hoffv | ((ft >> 1) < nown ? 0 : 0x40000000);
#pragma unroll
      for (int T = 0; T < 4; T += 2) {
        unsigned pk[2][2];
#pragma unroll
        for (int t = 0; t < 2; t++)
#pragma unroll
          for (int r = 0; r < 2; r++) {
            const float t0 = acc[i][T + t][2 * r], t1 = acc[i][T + t][2 * r + 1];
            pk[t][r] = relu_pk(pk_bf(t0 * __builtin_fabsf(t0), t1 * __builtin_fabsf(t1)));
          }
        auto r0 = __builtin_amdgcn_permlane16_swap(pk[0][0], pk[1][0], false, false);
        auto r1 = __builtin_amdgcn_permlane16_swap(pk[0][1], pk[1][1], false, false);
        st_sc1(v4u{r0[0], r1[0], r0[1], r1[1]}, rh, vo + T * 1024, so);
        SB();
      }
    }
  }
  TR(6);
  GAS u64* pf = gflag(a, GF_H + p * 32 + (ks ? 16 + (lane & 15) : min(lane, 19)));
  LAS char* wb[2][4];
  const int phi = ((q4 >> 1) & 1) | ((g & 1) << 1);
#pragma unroll
  for (int e = 0; e < 4; e++) { wb[0][e] = opq(sm + L_V + ks * 4096 + (8 * g + q4) * 128 + ((e ^ phi) * 32) + 8 * pp); wb[1][e] = opq(wb[0][e] + VH * 8192); }
  f32x4 acc[4][2];
#pragma unroll
  for (int e = 0; e < 4; e++) { acc[e][0] = zero4(); acc[e][1] = zero4(); }
  pin(pf);
  publish(gflag(a, GF_H + (p * 8 + j) * 4 + w), tagr, lane);
  TR(7);
  v2u bias2[4];
#pragma unroll
  for (int e = 0; e < 4; e++) bias2[e] = *(const GAS v2u*)(a.b2 + 64 * j + 16 * e + 4 * g);
  v2u wfr[2][4][2];
  auto rd2 = [&](auto I, auto K) LAM {
    constexpr int i = I, k = K, t = i % NVB;
    wfr[i & 1][k >> 1][k & 1] = tr_read(wb[t / VH][k >> 1] + (t % VH) * 8192 + (k & 1) * 512);
  };
  __builtin_amdgcn_s_sleep(HDELAY);
  poll(pf, tagr);
  TR(8);
  const int hoff = (p * NKS + 22 * ks) * 4096 + ah * 2048;
  v4u hf[PD][2];
#pragma unroll
  for (int i = 0; i < PD; i++)
#pragma unroll
    for (int T = 0; T < 2; T++) hf[i][T] = __builtin_amdgcn_raw_buffer_load_b128(rh, lane * 16 + T * 1024, hoff + i * 4096, AUX_SC1);
  lds_wait<F_FULL, need_V(1)>(fa);
  static_for<0, 8>([&](auto K) LAM { rd2(std::integral_constant<int, 0>{}, K); });
  static_for<0, 22>([&](auto I) LAM {
    constexpr int i = I, b = i & 1;
    if constexpr (i == 1 || i == 8 || i == 15) TR(9 + (i - 1) / 7);
    if (i < 21 || ks == 0) {
      constexpr bool chk = i + 2 < 22;
      unsigned fv; bool lack = false;
      if constexpr (chk) fv = *full;
      const v4u h0 = hf[i % PD][0], h1 = hf[i % PD][1];
      static_for<0, 8>([&](auto M) LAM {
        constexpr int m = M, e = m >> 1;
        const v4u wv = {wfr[b][e][0].x, wfr[b][e][0].y, wfr[b][e][1].x, wfr[b][e][1].y};
        SB();
        mfma_bf(acc[e][m & 1], wv, (m & 1) ? h1 : h0);
        SB();
        if constexpr (i < 21) rd2(std::integral_constant<int, i + 1>{}, M);
        if constexpr (chk && m == 6) lack = wany(fv < CE(need_V(i + 2)));
      });
      SB();
      if constexpr (i + NVB < 22) lds_put(fre, 17 + i);
      if constexpr (i + PD < 22) {
        const int so = hoff + (i + PD < 21 ? i + PD : 21 - ks) * 4096;
#pragma unroll
        for (int T = 0; T < 2; T++) hf[i % PD][T] = __builtin_amdgcn_raw_buffer_load_b128(rh, lane * 16 + T * 1024, so, AUX_SC1);
      }
      if constexpr (chk) { if (__builtin_expect(lack, 0)) lds_wait<F_FULL, need_V(i + 2)>(fa); CFENCE(); }
    }
  });
  MFMA_DONE();
  TR(12);
  lds_wait<0, 2>((unsigned)(FLG + F_YRDY));
  TR(13);
  LAS char* ex = sm + L_EX;
#pragma unroll
  for (int e = 0; e < 4; e++) *(LAS f32x4*)(ex + w * 4096 + e * 1024 + lane * 16) = sel4(ks, acc[e][0], acc[e][1]);
  lds_put(FLG + F_GX + 4 * w, 1);
  const int T = 2 * ah + ks;
  v2u yv[4], xv[4];
#pragma unroll
  for (int e = 0; e < 4; e++) {
    yv[e] = tr_read(sm + L_Y + (16 * e + 4 * g + q4) * YROW + T * 32 + 8 * pp);
    xv[e] = *(LAS v2u*)(sm + L_X + (16 * T + tok) * 128 + (((2 * e + (g >> 1)) ^ ((tok >> 1) & 7)) * 16) + (g & 1) * 8);
  }
  const float rs2 = *(LAS float*)(sm + L_STAT + (16 * T + tok) * 16 + 8);
  float rest[4][4];
#pragma unroll
  for (int e = 0; e < 4; e++) {
    const f32x4 m = sel4(ks, acc[e][1], acc[e][0]);
    rest[e][0] = __builtin_fmaf(m[0], rs2, bf_lo(bias2[e].x) + (bf_lo(yv[e].x) + bf_lo(xv[e].x)));
    rest[e][1] = __builtin_fmaf(m[1], rs2, bf_hi(bias2[e].x) + (bf_hi(yv[e].x) + bf_hi(xv[e].x)));
    rest[e][2] = __builtin_fmaf(m[2], rs2, bf_lo(bias2[e].y) + (bf_lo(yv[e].y) + bf_lo(xv[e].y)));
    rest[e][3] = __builtin_fmaf(m[3], rs2, bf_hi(bias2[e].y) + (bf_hi(yv[e].y) + bf_hi(xv[e].y)));
#pragma unroll
    for (int r = 0; r < 4; r++) pin(rest[e][r]);
  }
  lds_wait<0, 1>(FLG + F_GX + 4 * (w ^ 2));
  LAS char* rl = ex + (w ^ 2) * 4096;
  f32x4 ov[4];
#pragma unroll
  for (int e = 0; e < 4; e++) ov[e] = *(LAS f32x4*)(rl + e * 1024 + lane * 16);
  CFENCE();
#pragma unroll
  for (int e = 0; e < 4; e++) {
    const f32x4 o = ov[e];
    *(LAS v2u*)(rl + tok * 144 + e * 32 + g * 8) = v2u{pk_bf(__builtin_fmaf(o[0], rs2, rest[e][0]), __builtin_fmaf(o[1], rs2, rest[e][1])),
                                                       pk_bf(__builtin_fmaf(o[2], rs2, rest[e][2]), __builtin_fmaf(o[3], rs2, rest[e][3]))};
  }
  CFENCE();
#pragma unroll
  for (int i = 0; i < 2; i++) {
    const int r = 8 * i + (lane >> 3);
    const v4u o = *(LAS v4u*)(rl + r * 144 + (lane & 7) * 16);
    *(GAS v4u*)(a.out + (size_t)(64 * p + 16 * T + r) * DM + 64 * j + (lane & 7) * 8) = o;
  }
  if (w == 0 && lane < 2 && (lane ? j == 0 : p == 0)) {
    const u64 e1 = *(LAS u64*)(sm + FLG + F_EP + lane * 8) + 1;
    __hip_atomic_store(gflag(a, lane ? GF_ER + 16 * p : GF_EC + 16 * j), e1, __ATOMIC_RELAXED, __HIP_MEMORY_SCOPE_AGENT);
  }
  TR(14);
  if (w == 0) TR_DUMP(16, 15);
}

struct cpx { float r, i; };
DEV cpx cmul(cpx a, cpx b) { return cpx{__builtin_fmaf(a.r, b.r, -a.i * b.i), __builtin_fmaf(a.r, b.i, a.i * b.r)}; }
DEV cpx cexp_p(float zr2, float zi2, float pos) {
  const float e = __builtin_amdgcn_exp2f(zr2 * pos), th = __builtin_amdgcn_fractf(zi2 * pos);
  return cpx{e * __builtin_amdgcn_cosf(th), e * __builtin_amdgcn_sinf(th)};
}
DEV float exp_bf(float v) { return rbf(__builtin_amdgcn_exp2f(v * 1.44269504088896f)); }
DEV void pow2_scale(float mx, float& s, float& inv) {
  const int e = min(max((int)(f2u(mx) >> 23), 15), 254);
  s = u2f((unsigned)(268 - e) << 23);
  inv = u2f((unsigned)(e - 14) << 23);
}

DEV void role_conv(const Args& a, LAS char* sm, int cw, int lane, int p, int j, unsigned fa) {
  const int g = lane >> 4, tok = lane & 15;
  const int ch = cw >> 1, mh = cw & 1, d = 64 * j + 2 * p + ch;
  const unsigned fme = FLG + F_CW + 4 * cw, fpa = FLG + F_CW + 4 * (cw ^ 1);
  TR_DECL; TR(0);
  const unsigned lar = ((const GAS unsigned*)a.lar)[d * 64 + lane], aim = ((const GAS unsigned*)a.aim)[d * 64 + lane];
  const unsigned cre = ((const GAS unsigned*)a.cre)[d * 64 + lane], cim = ((const GAS unsigned*)a.cim)[d * 64 + lane];
  const float ldt = bf_lo(*(const GAS u16*)(a.ldt + d)), dsk = bf_lo(*(const GAS u16*)(a.dsk + d));
  const int x0 = 1024 * mh + 16 * lane;
  unsigned ga[18];
#pragma unroll
  for (int i = 0; i < 18; i += 2) {
    const int j0 = 2047 - (x0 + i), j1 = j0 - 1;
    const unsigned c = pk_bf((float)j0, (float)j1);
    const int i0 = j0 < 128 ? (j0 < 0 ? 703 : j0) : (int)(c & 0xffff) - 0x4300 + 128;
    const int i1 = j1 < 128 ? (j1 < 0 ? 703 : j1) : (int)(c >> 16) - 0x4300 + 128;
    ga[i] = 2 * i0 + L_TAB + ch * TABB; ga[i + 1] = 2 * i1 + L_TAB + ch * TABB;
    pin(ga[i]); pin(ga[i + 1]);
  }
  LAS char* xb[2];
#pragma unroll
  for (int s = 0; s < 2; s++) xb[s] = opq(sm + L_X + (16 * cw + tok) * 128 + (((4 * s + g) ^ ((tok >> 1) & 7)) * 16));
  v4u ones = {0x3f803f80u, 0x3f803f80u, 0x3f803f80u, 0x3f803f80u};
  v4u idm[2];
#pragma unroll
  for (int h = 0; h < 2; h++)
#pragma unroll
    for (int e = 0; e < 4; e++) {
      const int k0 = 8 * g + 2 * e - 16 * h;
      idm[h][e] = (k0 == tok ? 0x3f80u : 0u) | (k0 + 1 == tok ? 0x3f800000u : 0u);
    }
  pin(ones); pin(idm[0]); pin(idm[1]);
  unsigned pv[53];
  const float dt = exp_bf(ldt);
  const int sh = mh ? 0 : 16;
  const float zr2 = -exp_bf(bf_hi(lar << sh)) * dt * 1.44269504088896f, zi2 = bf_hi(aim << sh) * dt * 0.159154943091895f;
  {
    const cpx E8 = cexp_p(zr2, zi2, 8.f), E32 = cexp_p(zr2, zi2, 32.f);
    cpx Y[4]; Y[0] = cpx{512.f, 0.f}; Y[1] = cpx{512.f * E8.r, 512.f * E8.i}; Y[2] = cmul(Y[1], E8); Y[3] = cmul(Y[2], E8);
#pragma unroll
    for (int b = 0; b < 32; b++) { pv[21 + b] = pk_h(Y[b & 3].r, Y[b & 3].i); pin(pv[21 + b]); if (b < 28) Y[b & 3] = cmul(Y[b & 3], E32); }
  }
  TR(1);
  f32x4 sa = zero4(), gr = zero4();
  v4u xl[2];
  static_for<0, 2>([&](auto B) LAM {
    constexpr int c0 = B == 0 ? 0 : 5, c1 = B == 0 ? 5 : 8;
    lds_wait<F_FULL, B == 0 ? need_X(4) + 1 : need_X(7)>(fa);
    if constexpr (B == 1) { TR(2); __builtin_amdgcn_s_setprio(3); }
    v4u f[2 * (c1 - c0)];
#pragma unroll
    for (int c = c0; c < c1; c++) { f[2 * (c - c0)] = *(LAS v4u*)(xb[0] + ((c + 1) & 7) * 8192); f[2 * (c - c0) + 1] = *(LAS v4u*)(xb[1] + ((c + 1) & 7) * 8192); }
    SB();
#pragma unroll
    for (int i = 0; i < 2 * (c1 - c0); i++) { mfma_bf(sa, ones, f[i]); mfma_bf(gr, f[i], f[i]); }
    if constexpr (B == 1) { xl[0] = f[4]; xl[1] = f[5]; }
  });
  const u64 tagc = tag_of(sm, 0);
  MFMA_DONE();
  const float dg = (tok & 2) ? ((tok & 1) ? gr[3] : gr[2]) : ((tok & 1) ? gr[1] : gr[0]);
  const float mean = sa[0] * (1.f / DM), ex2 = xsum32(xsum16(g == (tok >> 2) ? dg : 0.f)) * (1.f / DM);
  float var = __builtin_fmaf(-mean, mean, ex2);
  if (wany(var < ex2 * (1.f / 1024.f))) {
    float q = 0;
#pragma nounroll
    for (int c = 0; c < 16; c++) {
      const v4u u = *(LAS v4u*)((c & 1 ? xb[1] : xb[0]) + (c >> 1) * 8192);
#pragma unroll
      for (int e = 0; e < 4; e++) { const unsigned uu = u[e]; const float d0 = bf_lo(uu) - mean, d1 = bf_hi(uu) - mean; q = __builtin_fmaf(d0, d0, __builtin_fmaf(d1, d1, q)); }
    }
    var = xsum32(xsum16(q)) * (1.f / DM);
  }
  const float m = rbf(mean), sd = rbf(__builtin_amdgcn_sqrtf(rbf(rbf(var) + 1e-5f))), rs = __builtin_amdgcn_rcpf(sd);
  const rsrc_t ru = mk_rsrc(a.ws + WS_U, SEQ * DM * 2);
  dma_flag<1>(srd(kSeq, 256), FLG + F_XD + 4 * cw);
  {
    const float c0 = -m * rs;
    v4u xn[2]; f32x4 t[4];
#pragma unroll
    for (int s = 0; s < 2; s++)
#pragma unroll
      for (int e = 0; e < 4; e++) { const unsigned uu = xl[s][e]; xn[s][e] = pk_bf(__builtin_fmaf(bf_lo(uu), rs, c0), __builtin_fmaf(bf_hi(uu), rs, c0)); }
#pragma unroll
    for (int i = 0; i < 4; i++) t[i] = zero4();
    pin(xn[0]); pin(xn[1]);
    NOP2();
#pragma unroll
    for (int i = 0; i < 4; i++) mfma_bf(t[i], xn[i >> 1], idm[i & 1]);
    MFMA_DONE();
#pragma unroll
    for (int s = 0; s < 2; s++) {
      unsigned h0 = pk_h(t[2 * s][0], t[2 * s][1]), h1 = pk_h(t[2 * s + 1][0], t[2 * s + 1][1]), h2 = pk_h(t[2 * s][2], t[2 * s][3]), h3 = pk_h(t[2 * s + 1][2], t[2 * s + 1][3]);
      auto r0 = __builtin_amdgcn_permlane16_swap(h0, h1, false, false);
      auto r1 = __builtin_amdgcn_permlane16_swap(h2, h3, false, false);
      st_sc1(v4u{r0[0], r1[0], r0[1], r1[1]}, ru, (8 * (g & 1) + (tok >> 1)) * 8192 + (g >> 1) * 32 + (tok & 1) * 16,
                                             (j * 32 + 16 * s) * 8192 + p * 256 + cw * 64);
    }
  }
  TR(3);
  v4u stat = {pk_bf(-m, -m), pk_bf(-m, sd), f2u(rs * rs), 0u};
  pin(stat);
  SB();
  publish(gflag(a, GF_U + (j * 32 + p) * 4 + cw), tagc, lane);
  __builtin_amdgcn_s_setprio(0);
  SB();
  TR(4);
  float kq, kd, kinv;
  {
    const float csum = wave_sum((__builtin_fabsf(bf_lo(cre)) + __builtin_fabsf(bf_hi(cre))) + (__builtin_fabsf(bf_lo(cim)) + __builtin_fabsf(bf_hi(cim))));
    float cs, cinv, ks;
    pow2_scale(csum, cs, cinv);
    pow2_scale(__builtin_fmaf(2.f * dt, csum, __builtin_fabsf(dsk)), ks, kinv);
    kq = cinv * dt * (2.f / 512.f) * ks; kd = (lane | mh) == 0 ? dsk * ks : 0.f;
    const cpx E1 = cexp_p(zr2, -zi2, 1.f), E256 = cexp_p(zr2, -zi2, 256.f);
    auto put = [&](int row, cpx v) LAM { pv[row] = pk_h(v.r, v.i); pin(pv[row]); };
    cpx X[8]; X[0] = cpx{bf_hi(cre << sh) * cs, bf_hi(cim << sh) * -cs};
#pragma unroll
    for (int o = 1; o < 8; o++) X[o] = cmul(X[o - 1], E1);
#pragma unroll
    for (int o = 0; o < 8; o++) put(o, X[o]);
    cpx A = cmul(X[0], E256), B = cmul(X[4], E256);
    put(8, A); put(9, cmul(X[2], E256)); put(10, B); put(11, cmul(X[6], E256));
    A = cmul(A, E256); B = cmul(B, E256); put(12, A); put(13, B);
    A = cmul(A, E256); B = cmul(B, E256); put(14, A); put(15, B);
#pragma unroll
    for (int o = 16; o < 21; o++) { A = cmul(A, E256); put(o, A); }
    pin(kq); pin(kd); pin(kinv);
  }
  LAS char* pa0 = opq(sm + L_V + ch * SCR + tok * SROW + g * 16);
  LAS char* pa1 = opq(sm + L_V + ch * SCR + min(16 + tok, 20) * SROW + g * 16);
  LAS char* pb = opq(sm + L_V + ch * SCR + (21 + 16 * mh + tok) * SROW + g * 16);
  f32x4 k0 = zero4(), k1 = zero4();
  const int bq = 16 * mh + tok;
  LAS char* tb = sm + L_TAB + ch * TABB;
  LAS char* t0a = opq(tb + 2 * (g < 2 ? 8 * bq + 4 * g : (g == 2 ? 256 + 4 * bq : 384 + 2 * bq)));
  LAS char* t0b = opq(t0a + (g == 3 ? 128 : 4));
  LAS char* t1a = opq(tb + 2 * (g == 0 ? 512 + bq : (g == 1 ? 640 + bq : 672 + bq)));
  const int t1s = g == 0 ? 64 : 0;
  TR(12);
  lds_wait<F_XD, 1>(fa);
  lds_wait<F_FULL, need_W(15)>(fa);
  if (g == 0) *(LAS v4u*)(sm + L_STAT + (16 * cw + tok) * 16) = stat;
  lds_put(FLG + F_XD + 4 * cw, 2);
  lds_wait<F_FREE, FREE_SCR>(fa);
  TR(5);
  set_m0(L_X + ch * SCR + mh * 256);
  static_for<0, 53>([&](auto R) LAM { constexpr int r = R; put_tid<8192 + r * SROW>(pv[r]); });
  lds_put(fme, 1);
  lds_wait<0, 1>(fpa);
  TR(6);
  {
    v4u f0[8], f1[8], fb[8];
#pragma unroll
    for (int s = 0; s < 8; s++) { fb[s] = *(LAS v4u*)(pb + 64 * s); f0[s] = *(LAS v4u*)(pa0 + 64 * s); f1[s] = *(LAS v4u*)(pa1 + 64 * s); }
    SB();
#pragma unroll
    for (int s = 0; s < 8; s++) { mfma_h(k0, f0[s], fb[s]); mfma_h(k1, f1[s], fb[s]); }
  }
  MFMA_DONE();
  {
    const float kz = g < 2 ? kq : 0.f;
    *(LAS unsigned*)t0a = pk_h(__builtin_fmaf(k0[0], kq, kd), k0[1] * kq);
    *(LAS unsigned*)t0b = pk_h(k0[2] * kq, k0[3] * kq);
    const unsigned q0 = pk_h(k1[0] * kz, k1[1] * kz), q1 = pk_h(k1[2] * kz, k1[3] * kz);
    *(LAS u16*)(t1a) = (u16)q0; *(LAS u16*)(t1a + t1s) = (u16)(q0 >> 16); *(LAS u16*)(t1a + 2 * t1s) = (u16)q1; *(LAS u16*)(t1a + 3 * t1s) = (u16)(q1 >> 16);
  }
  lds_put(fme, 2);
  TR(7);
  lds_wait<0, 2>(fpa);
  LAS char* cr = sm + ch * CH;
  {
    unsigned P[9], Q[8];
#pragma unroll
    for (int i = 0; i < 9; i++) P[i] = (unsigned)*(LAS u16*)(size_t)ga[2 * i] | ((unsigned)*(LAS u16*)(size_t)ga[2 * i + 1] << 16);
#pragma unroll
    for (int i = 0; i < 8; i++) Q[i] = __builtin_amdgcn_alignbit(P[i + 1], P[i], 16);
    LAS char* kc = cr + x0 * 2;
    *(LAS v4u*)(kc) = v4u{P[0], P[1], P[2], P[3]}; *(LAS v4u*)(kc + 16) = v4u{P[4], P[5], P[6], P[7]};
    *(LAS v4u*)(kc + KCOPY) = v4u{Q[0], Q[1], Q[2], Q[3]}; *(LAS v4u*)(kc + KCOPY + 16) = v4u{Q[4], Q[5], Q[6], Q[7]};
  }
  lds_put(fme, 3);
  LAS char* ab[4];
#pragma unroll
  for (int i = 0; i < 4; i++) ab[i] = opq(cr + ((15 - tok) & 1) * KCOPY + 2 * (((15 - tok) & ~1) + 8 * g) + 64 * (4 - 4 * mh) + 1024 * i);
  LAS char* bb = opq(cr + C_U + 448 + 32 * tok + 16 * g - 64 * (4 * mh + 3));
  f32x4 acc[8];
#pragma unroll
  for (int c = 0; c < 8; c++) acc[c] = zero4();
  auto lda = [&](int i, int r, int k) LAM { return ld2(ab[(7 - i) >> 1] + 64 * (59 - 8 * i - r) - 1024 * ((7 - i) >> 1) + 8 * k); };
  TR(13);
  __builtin_amdgcn_s_setprio(3);
  lds_wait<0, 3>(fpa);
  lds_wait<0, 1>((unsigned)(FLG + F_YRDY));
  TR(8);
  v2u A[8][2]; v4u B[2][8];
#pragma unroll
  for (int i = 0; i < 8; i++) {
    B[0][i] = *(LAS v4u*)(bb + 64 * (8 * i + 3));
    A[7 - i][0] = lda(7 - i, 0, 0); A[7 - i][1] = lda(7 - i, 0, 1);
  }
  static_for<0, 4>([&](auto R) LAM {
    constexpr int r = R;
    static_for<0, 8>([&](auto II) LAM {
      constexpr int i = 7 - II;
      static_for<0, 8 - i>([&](auto K) LAM {
        constexpr int k = K;
        SB();
        mfma_h(acc[i + k], v4u{A[i][0].x, A[i][0].y, A[i][1].x, A[i][1].y}, B[r & 1][k]);
        SB();
        if constexpr (r < 3) {
          if constexpr (i < 7 && k < 2) A[i + 1][k] = lda(i + 1, r + 1, k);
          if constexpr (i <= 4 && i >= 1 && (k == 2 || k == 3)) B[(r + 1) & 1][2 * (4 - i) + k - 2] = *(LAS v4u*)(bb + 64 * (8 * (2 * (4 - i) + k - 2) + 3 - (r + 1)));
        }
        if constexpr (r == 3 && i == 0 && k == 1) lds_put(fme, 4);
      });
    });
    if constexpr (r < 3) { A[0][0] = lda(0, r + 1, 0); A[0][1] = lda(0, r + 1, 1); }
  });
  MFMA_DONE();
  TR(9);
  lds_wait<0, 4>(fpa);
  LAS char* exm = cr + mh * 4096;
  LAS char* exo = cr + (mh ^ 1) * 4096;
#pragma unroll
  for (int c = 0; c < 4; c++) *(LAS f32x4*)(exm + c * 1024 + lane * 16) = sel4(mh, acc[c], acc[4 + c]);
  lds_put(fme, 5);
  lds_wait<0, 5>(fpa);
  LAS char* yb = cr + 8192 + mh * 2048;
#pragma unroll
  for (int c = 0; c < 4; c++) {
    const f32x4 o = *(LAS f32x4*)(exo + c * 1024 + lane * 16), mm = sel4(mh, acc[4 + c], acc[c]);
    *(LAS v2u*)(yb + 512 * c + 32 * tok + 8 * g) = v2u{pk_bf((mm[0] + o[0]) * kinv, (mm[1] + o[1]) * kinv), pk_bf((mm[2] + o[2]) * kinv, (mm[3] + o[3]) * kinv)};
  }
  CFENCE();
  const rsrc_t ry = mk_rsrc(a.ws + WS_Y, SEQ * DM * 2);
#pragma unroll
  for (int i = 0; i < 2; i++) st_sc1(*(LAS v4u*)(yb + i * 1024 + lane * 16), ry, lane * 16 + i * 1024, d * 4096 + mh * 2048);
  lds_put(fme, 6);
  TR(10);
  publish(gflag(a, GF_Y + (j * 2 + mh) * 64 + 2 * p + ch), tagc, lane);
  TR(11);
  if (cw == 0) TR_DUMP(48, 14);
}

__global__ __launch_bounds__(NTHR, 3) NOPK void k_fused(Args a) {
  __shared__ __attribute__((aligned(16))) char smem[LDS_BYTES];
  LAS char* sm = (LAS char*)smem;
  const int lane = threadIdx.x & 63, w = __builtin_amdgcn_readfirstlane(threadIdx.x >> 6);
  const int b = blockIdx.x, p = (b & 7) * 4 + (b >> 6), j = (b >> 3) & 7;
  unsigned fa = FLG + 4 * (lane & 3);
  pin(fa);
  if (w == 0) { ((LAS unsigned*)(sm + FLG))[lane] = 0; LGKM0(); }
  __builtin_amdgcn_s_barrier();
  if (w >= 8) { __builtin_amdgcn_s_setprio(1); role_loader(a, sm, w - 8, lane, p, j, fa); }
  else if (w < 4) { __builtin_amdgcn_s_setprio(2); role_gemm(a, sm, w, lane, p, j, fa); }
  else { role_conv(a, sm, w - 4, lane, p, j, fa); }
}

extern "C" void kernel_launch(void* const* d_in, const int* in_sizes, int n_in, void* d_out, int out_size, void* d_ws, size_t ws_size, hipStream_t stream) {
  Args a;
  a.x = (const u16*)d_in[0]; a.lar = (const u16*)d_in[1]; a.aim = (const u16*)d_in[2]; a.cre = (const u16*)d_in[3]; a.cim = (const u16*)d_in[4];
  a.ldt = (const u16*)d_in[5]; a.dsk = (const u16*)d_in[6]; a.w1 = (const u16*)d_in[8]; a.b1 = (const u16*)d_in[9]; a.w2 = (const u16*)d_in[10];
  a.b2 = (const u16*)d_in[11]; a.out = (u16*)d_out; a.ws = (char*)d_ws;
  k_fused<<<256, NTHR, 0, stream>>>(a);
}
```

```cpp
#include <hip/hip_runtime.h>
#include <stdint.h>
#include <type_traits>

#define LAS __attribute__((address_space(3)))
#define GAS __attribute__((address_space(1)))
#if defined(__HIP_DEVICE_COMPILE__) && __HIP_DEVICE_COMPILE__
#define NOPK __attribute__((target("no-packed-fp32-ops")))
#else
#define NOPK
#endif
#define DEV __device__ __forceinline__ NOPK
#define LAM __attribute__((always_inline)) NOPK
typedef unsigned short u16;
typedef unsigned long long u64;
typedef unsigned v4u __attribute__((ext_vector_type(4)));
typedef unsigned v2u __attribute__((ext_vector_type(2)));
typedef unsigned v2u4 __attribute__((ext_vector_type(2), aligned(4)));
typedef __bf16 bf16x8 __attribute__((ext_vector_type(8)));
typedef _Float16 f16x8 __attribute__((ext_vector_type(8)));
typedef float f32x4 __attribute__((ext_vector_type(4)));
typedef short s16x4 __attribute__((ext_vector_type(4)));
typedef short s16x2 __attribute__((ext_vector_type(2)));
typedef __amdgpu_buffer_rsrc_t rsrc_t;

constexpr int SEQ = 2048, DM = 512, NMODE = 128, FF = 1376, NKS = 43;
constexpr size_t WS_H = 0, WS_U = 6u << 20, WS_Y = 8u << 20, WS_FLAG = 10u << 20, WS_DIAG = 12u << 20;
constexpr int GF_U = 0, GF_H = 1024, GF_Y = 2048, GF_EC = 3072, GF_ER = 3200;
#define FSTR 1
#define PSLEEP 8
#define UWAIT 1
#define HDELAY 12
constexpr int NTHR = 768;
constexpr int POLL_CAP = 1 << 14;
constexpr int AUX_SC1 = 16;

struct Args {
  const u16 *x, *lar, *aim, *cre, *cim, *ldt, *dsk, *w1, *b1, *w2, *b2;
  u16* out;
  char* ws;
};

#define TR_DECL
#define TR(i)
#define TR_DUMP(base, n)

template <int I, int N, class Fn> DEV void static_for(Fn&& f) {
  if constexpr (I < N) { f(std::integral_constant<int, I>{}); static_for<I + 1, N>(f); }
}
#define CE(x) (std::integral_constant<int, (x)>::value)
template <class T> DEV void pin(T& v) { asm volatile("" : "+v"(v)); }
template <class T> DEV void pins(T& v) { asm volatile("" : "+s"(v)); }
DEV float u2f(unsigned u) { return __builtin_bit_cast(float, u); }
DEV unsigned f2u(float f) { return __builtin_bit_cast(unsigned, f); }
DEV bool wany(bool c) { return __builtin_amdgcn_ballot_w64(c) != 0; }
DEV rsrc_t mk_rsrc(const void* p, int bytes) { return __builtin_amdgcn_make_buffer_rsrc((void*)p, 0, bytes, 0x00020000); }
DEV float bf_lo(unsigned u) { return u2f(u << 16); }
DEV float bf_hi(unsigned u) { return u2f(u & 0xffff0000u); }
typedef float f32x2 __attribute__((ext_vector_type(2)));
typedef __bf16 bf16x2 __attribute__((ext_vector_type(2)));
typedef _Float16 f16x2 __attribute__((ext_vector_type(2)));
DEV unsigned pk_bf(float a, float b) { return __builtin_bit_cast(unsigned, __builtin_convertvector(f32x2{a, b}, bf16x2)); }
DEV unsigned pk_h(float a, float b) { return __builtin_bit_cast(unsigned, __builtin_convertvector(f32x2{a, b}, f16x2)); }
DEV unsigned pk_hn(float a, float b) { return pk_h(a, -b); }
DEV float rbf(float a) { return u2f(pk_bf(0.f, a)); }
DEV unsigned relu_pk(unsigned v) { return __builtin_bit_cast(unsigned, __builtin_elementwise_max(__builtin_bit_cast(s16x2, v), s16x2{0, 0})); }
DEV v2u tr_read(LAS char* p) { return __builtin_bit_cast(v2u, __builtin_amdgcn_ds_read_tr16_b64_v4i16((LAS s16x4*)p)); }
DEV v2u ld2(LAS char* p) { return *(LAS v2u4*)p; }
DEV float xsum16(float v) { auto r = __builtin_amdgcn_permlane16_swap(f2u(v), f2u(v), false, false); return u2f(r[0]) + u2f(r[1]); }
DEV float xsum32(float v) { auto r = __builtin_amdgcn_permlane32_swap(f2u(v), f2u(v), false, false); return u2f(r[0]) + u2f(r[1]); }
template <int CTRL> DEV float dpp_add(float v) { return v + u2f((unsigned)__builtin_amdgcn_update_dpp(0, (int)f2u(v), CTRL, 0xf, 0xf, false)); }
DEV float wave_sum(float v) { v = dpp_add<0x121>(v); v = dpp_add<0x122>(v); v = dpp_add<0x124>(v); v = dpp_add<0x128>(v); return xsum32(xsum16(v)); }
DEV f32x4 sel4(int c, f32x4 a, f32x4 b) { return f32x4{c ? a[0] : b[0], c ? a[1] : b[1], c ? a[2] : b[2], c ? a[3] : b[3]}; }
DEV LAS char* opq(LAS char* p) { unsigned v = (unsigned)(size_t)p; pin(v); return (LAS char*)(size_t)v; }
DEV f32x4 zero4() { f32x4 z = {0, 0, 0, 0}; pin(z); return z; }
#define VMCNT(n) asm volatile("s_waitcnt vmcnt(%0)" ::"i"(n) : "memory")
#define LGKM0() asm volatile("s_waitcnt lgkmcnt(0)" ::: "memory")
#define CFENCE() asm volatile("" ::: "memory")
#define SB() __builtin_amdgcn_sched_barrier(0)
DEV void mfma_bf(f32x4& c, v4u a, v4u b) { asm volatile("v_mfma_f32_16x16x32_bf16 %0, %1, %2, %0" : "+v"(c) : "v"(a), "v"(b)); }
DEV void mfma_h(f32x4& c, v4u a, v4u b) { asm volatile("v_mfma_f32_16x16x32_f16 %0, %1, %2, %0" : "+v"(c) : "v"(a), "v"(b)); }
#define NOP2() do { SB(); asm volatile("s_nop 1"); SB(); } while (0)
#define MFMA_DONE() do { SB(); asm volatile("s_nop 7"); SB(); } while (0)

DEV void st_sc1(v4u v, rsrc_t r, int voff, int soff) { __builtin_amdgcn_raw_buffer_store_b128(v, r, voff, soff, AUX_SC1); NOP2(); }
DEV void lds_put(unsigned addr, unsigned v) { asm volatile("s_mov_b64 exec, 1\n\tds_write_b32 %0, %1\n\ts_mov_b64 exec, -1" ::"v"(addr), "v"(v) : "memory"); }
template <int OFF, int WANT> DEV void lds_wait(unsigned fa) {
  unsigned t, n;
  asm volatile("s_mov_b32 %1, 0x20000\n"
               "1:\n\tds_read_b32 %0, %2 offset:%3\n\ts_waitcnt lgkmcnt(0)\n\tv_cmp_gt_u32 vcc, %4, %0\n\ts_cbranch_vccz 2f\n\t"
               "s_sleep 1\n\ts_sub_u32 %1, %1, 1\n\ts_cbranch_scc0 1b\n2:"
               : "=&v"(t), "=&s"(n) : "v"(fa), "i"(OFF), "i"(WANT) : "memory", "vcc", "scc");
}
template <int OFF> DEV void lds_wait_s(unsigned fa, int want) {
  unsigned t, n;
  asm volatile("s_mov_b32 %1, 0x20000\n"
               "1:\n\tds_read_b32 %0, %2 offset:%3\n\ts_waitcnt lgkmcnt(0)\n\tv_cmp_gt_u32 vcc, %4, %0\n\ts_cbranch_vccz 2f\n\t"
               "s_sleep 1\n\ts_sub_u32 %1, %1, 1\n\ts_cbranch_scc0 1b\n2:"
               : "=&v"(t), "=&s"(n) : "v"(fa), "i"(OFF), "s"(want) : "memory", "vcc", "scc");
}
static __device__ const unsigned kSeq[64] = {0,1,2,3,4,5,6,7,8,9,10,11,12,13,14,15,16,17,18,19,20,21,22,23,24,25,26,27,28,29,30,31,32,
  33,34,35,36,37,38,39,40,41,42,43,44,45,46,47,48,49,50,51,52,53,54,55,56,57,58,59,60,61,62,63};
DEV v4u srd(const void* p, unsigned bytes) { const u64 v = (u64)p; return v4u{(unsigned)v, (unsigned)(v >> 32) & 0xffffu, bytes, 0x00020000u}; }
DEV void dma16(v4u rs, unsigned lds, int voff, int soff) {
  asm volatile("s_mov_b32 m0, %0\n\ts_nop 0\n\tbuffer_load_dwordx4 %1, %2, %3 offen lds" ::"s"(lds), "v"(voff), "s"(rs), "s"(soff) : "memory");
}
template <int N> DEV void dma_flag(v4u rs, unsigned lds) {
  asm volatile("s_mov_b32 m0, %0\n\ts_mov_b64 exec, 1\n\tbuffer_load_dword off, %1, %2 lds\n\ts_mov_b64 exec, -1" ::"s"(lds), "s"(rs), "s"(N * 4) : "memory");
}
DEV void dma_ep(v4u rs, unsigned lds, int voff) {
  asm volatile("s_mov_b32 m0, %0\n\ts_mov_b64 exec, 3\n\tbuffer_load_dword %2, %1, 0 offen sc1 lds\n\ts_mov_b64 exec, -1" ::"s"(lds), "s"(rs), "v"(voff) : "memory");
}
template <int OFF> DEV void put_tid(unsigned v) { asm volatile("ds_write_addtid_b32 %0 offset:%1" ::"v"(v), "i"(OFF) : "memory"); }
DEV void set_m0(unsigned v) { asm volatile("s_mov_b32 m0, %0\n\ts_nop 0" ::"s"(v) : "memory"); }
DEV GAS u64* gflag(const Args& a, int line) { return (GAS u64*)(a.ws + WS_FLAG) + line * FSTR; }
DEV void publish(GAS u64* f, u64 tag, int lane) {
  VMCNT(0);
  if (lane == 0) __hip_atomic_store(f, tag, __ATOMIC_RELAXED, __HIP_MEMORY_SCOPE_AGENT);
}
DEV void poll(GAS u64* p, u64 tag) {
#pragma nounroll
  for (int it = 0; it < POLL_CAP; it++) {
    if (!wany(__hip_atomic_load(p, __ATOMIC_RELAXED, __HIP_MEMORY_SCOPE_AGENT) != tag)) break;
    __builtin_amdgcn_s_sleep(PSLEEP);
  }
  __builtin_amdgcn_fence(__ATOMIC_ACQUIRE, "wavefront");
}
DEV void poll2(GAS u64* p, GAS u64* q, u64 tag) {
#pragma nounroll
  for (int it = 0; it < POLL_CAP; it++) {
    const u64 x = __hip_atomic_load(p, __ATOMIC_RELAXED, __HIP_MEMORY_SCOPE_AGENT), y = __hip_atomic_load(q, __ATOMIC_RELAXED, __HIP_MEMORY_SCOPE_AGENT);
    if (!wany(x != tag || y != tag)) break;
    __builtin_amdgcn_s_sleep(PSLEEP);
  }
  __builtin_amdgcn_fence(__ATOMIC_ACQUIRE, "wavefront");
}
DEV int unit_start(int j) { return (j * 37 + 3) / 7; }

constexpr int KCOPY = 4160, C_U = 2 * KCOPY, CH = C_U + 4608, L_TAB = 28160, TABB = 1408, L_STAT = 31744;
constexpr int L_X = 32768, L_RING = L_X + 65536, SROW = 528, SCR = 53 * SROW;
constexpr int L_Y = 0, YROW = 160, L_EX = L_Y + 64 * YROW;
constexpr int LDS_BYTES = 163840, FLG = LDS_BYTES - 256;
static_assert(2 * SCR <= 7 * 8192 && 2 * CH <= L_TAB && L_TAB + 2 * TABB <= L_STAT && L_EX + 16384 <= L_STAT && L_RING + 61440 <= FLG, "lds");
constexpr int F_FULL = 0, F_FREE = 16, F_XD = 32, F_CW = 48, F_YRDY = 64, F_GX = 80, F_EP = 96;

constexpr int NSTEP1 = 24;
constexpr int ST_KIND[NSTEP1] = {0, 1, 1, 0, 0, 1, 0, 1, 0, 1, 0, 0, 1, 0, 1, 1, 1, 1, 1, 1, 1, 1, 1, 1};
constexpr int st_idx(int s) { int n = 0; for (int i = 0; i < s; i++) n += ST_KIND[i] == ST_KIND[s]; return n; }
constexpr int step_of(int kind, int idx) { for (int s = 0; s < NSTEP1; s++) if (ST_KIND[s] == kind && st_idx(s) == idx) return s; return -1; }
constexpr int need_X(int c) { return step_of(0, c) + 1; }
constexpr int need_W(int u) { return step_of(1, u) + 1; }
constexpr int need_V(int t) { return NSTEP1 + t + 1; }
static_assert(need_X(1) < need_W(2) && need_X(2) < need_W(4) && need_X(3) < need_W(6) && need_X(7) < need_W(14), "x before its units");
constexpr int w_addr(int u) { return u % 7 < 5 ? L_RING + (u % 7) * 12288 : (u % 7 - 5) * 12288; }
constexpr int FREE_SCR = 14;
constexpr bool free_sent(int n) { return n <= 9 || n == FREE_SCR || n == 16; }
constexpr int x_addr(int c) { return L_X + ((c + 1) & 7) * 8192; }
constexpr int NVB = 14, L_V = L_X + 8192, VH = 7;
constexpr int PD = 8, VR = 8;
#define VDEPTH 8

DEV u64 tag_of(LAS char* sm, int which);
DEV void role_loader(const Args& a, LAS char* sm, int l, int lane, int p, int j, unsigned fa) {
  const v4u rx = srd(a.x, SEQ * DM * 2), rw = srd(a.w1, DM * FF * 2), rq = srd(kSeq, 256);
  int vx[2], vw[3];
#pragma unroll
  for (int i = 0; i < 2; i++) vx[i] = (8 * i + (lane >> 3)) * 1024 + (((lane & 7) ^ (4 * i + (lane >> 4))) * 16);
  const int sx = p * 65536 + l * 16384, sw = unit_start(j) * 64;
  TR_DECL;
  static_for<0, NSTEP1>([&](auto S) LAM {
    constexpr int s = S, kind = ST_KIND[s], idx = st_idx(s);
    if constexpr (s % 4 == 0) TR(s / 4);
    if constexpr (kind == 0) {
      const int so = sx + ((idx + j + 1) & 7) * 128;
#pragma unroll
      for (int i = 0; i < 2; i++) dma16(rx, CE(x_addr(idx)) + l * 2048 + i * 1024, vx[i], so);
    } else {
      if constexpr (idx >= 7) lds_wait<F_FREE, idx - 6>(fa);
      const int so = sw + ((((idx >> 1) + j + 1) & 7) * 64 + (idx & 1) * 32) * (FF * 2);
#pragma unroll
      for (int n = 0; n < 3; n++) {
        if constexpr (idx == 0) {
          const int sg = 16 * (3 * l + n) + (lane >> 2), r = (sg * 171) >> 10, qp = sg - 6 * r;
          vw[n] = r * (FF * 2) + ((qp ^ ((r >> 1) & 1)) * 64) + (((lane & 3) ^ (2 * ((r >> 3) & 1))) * 16);
        }
        dma16(rw, CE(w_addr(idx)) + l * 3072 + n * 1024, vw[n], so);
      }
      if constexpr (idx == 0) {
        if (l < 2) {
          const v4u re = srd(a.ws + WS_FLAG + (size_t)(l ? GF_ER + 16 * p : GF_EC + 16 * j) * 8 * FSTR, 8);
          dma_ep(re, FLG + F_EP + 8 * l, lane * 4);
        }
      }
    }
    if constexpr (kind == 1 || idx == 7) dma_flag<s + 1>(rq, FLG + F_FULL + 4 * l);
  });
  TR(6);
  if (l < 3) {
    const rsrc_t rv = mk_rsrc(a.w2, DM * FF * 2);
    const int vv = (lane >> 3) * 1024 + (lane & 7) * 16 + j * 128;
    v4u wr[VR][4];
    auto fetch = [&](int n) LAM {
      const int v = min(3 * n + l, 42), so = ((v >> 1) + 22 * (v & 1)) * 32768;
#pragma unroll
      for (int r = 0; r < 4; r++) wr[n % VR][r] = __builtin_amdgcn_raw_buffer_load_b128(rv, vv, so + r * 8192, 0);
    };
#pragma unroll
    for (int n = 0; n < VR; n++) { fetch(n); SB(); VMCNT(VDEPTH); }
    SB();
    TR(7);
    LAS char* wa[2];
#pragma unroll
    for (int i = 0; i < 2; i++) wa[i] = sm + L_V + (lane >> 3) * 128 + (((lane & 7) ^ ((((lane >> 4) & 1) | (i << 1)) << 1)) * 16);
    lds_wait<F_FREE, 16>(fa);
    lds_wait<F_CW, 3>(fa);
    TR(8);
    static_for<0, 15>([&](auto N) LAM {
      constexpr int n = N;
      if constexpr (n == 9) TR(9);
      const int v = 3 * n + l;
      if (n < 14 || l == 0) {
        if constexpr (n >= 9) lds_wait_s<F_FREE>(fa, 17 + (v >> 1) - NVB);
        const int lo = (v >= 2 * NVB ? v - 2 * NVB : v) * 4096;
#pragma unroll
        for (int r = 0; r < 4; r++) *(LAS v4u*)(wa[r & 1] + lo + r * 1024) = wr[n % VR][r];
        lds_put(FLG + F_FULL + 4 * l, n >= 13 && v + 3 > 42 ? 63 : NSTEP1 + ((v + 3) >> 1));
        if constexpr (n + VR < 15) fetch(n + VR);
        SB();
      }
    });
    TR(10);
    if (l == 0) TR_DUMP(0, 11);
    return;
  }
  const u64 tagc = tag_of(sm, 0);
  v4u uv[8];
  lds_wait<F_CW, UWAIT>(fa);
  poll2(gflag(a, GF_U + j * 128 + lane), gflag(a, GF_U + j * 128 + 64 + lane), tagc);
  TR(7);
  const rsrc_t ru = mk_rsrc(a.ws + WS_U, SEQ * DM * 2), ry = mk_rsrc(a.ws + WS_Y, SEQ * DM * 2);
#pragma unroll
  for (int i = 0; i < 8; i++) uv[i] = __builtin_amdgcn_raw_buffer_load_b128(ru, lane * 16 + (i & 3) * 1024, (j * 32 + p) * 8192 + (i >> 2) * 4096, AUX_SC1);
  lds_wait<F_FREE, FREE_SCR>(fa);
  lds_put(FLG + F_FULL + 12, 63);
  if (lane < 60) *(LAS v4u*)(sm + (lane & 1) * CH + C_U + (lane >> 1) * 16) = v4u{0, 0, 0, 0};
  if (lane < 8) *(LAS v4u*)(sm + (lane & 1) * CH + ((lane >> 1) & 1) * KCOPY + 4096 + (lane >> 2) * 16) = v4u{0, 0, 0, 0};
#pragma unroll
  for (int i = 0; i < 8; i++) *(LAS v4u*)(sm + (lane & 1) * CH + C_U + 480 + i * 512 + (lane >> 1) * 16) = uv[i];
  lds_put(FLG + F_YRDY, 1);
  TR(8);
  GAS u64* pf = gflag(a, GF_Y + (j * 2 + (p >> 4)) * 64 + lane);
  pin(pf);
  lds_wait<F_CW, 6>(fa);
  TR(9);
  __builtin_amdgcn_s_sleep(24);
  poll(pf, tagc);
  TR(10);
#pragma unroll
  for (int i = 0; i < 8; i++) uv[i] = __builtin_amdgcn_raw_buffer_load_b128(ry, (lane >> 3) * 4096 + (lane & 7) * 16, (64 * j + 8 * i) * 4096 + p * 128, AUX_SC1);
#pragma unroll
  for (int i = 0; i < 8; i++) *(LAS v4u*)(sm + L_Y + (8 * i + (lane >> 3)) * YROW + (lane & 7) * 16) = uv[i];
  lds_put(FLG + F_YRDY, 2);
  TR(11);
  TR_DUMP(32, 12);
}

DEV u64 tag_of(LAS char* sm, int which) {
  const v2u e = *(LAS v2u*)(sm + FLG + F_EP + 8 * which);
  const u64 v = ((u64)(unsigned)__builtin_amdgcn_readfirstlane(e.y) << 32) | (unsigned)__builtin_amdgcn_readfirstlane(e.x);
  return (v + 1) * 0x9E3779B97F4A7C15ull;
}

DEV void role_gemm(const Args& a, LAS char* sm, int w, int lane, int p, int j, unsigned fa) {
  const int g = lane >> 4, tok = lane & 15, q4 = tok >> 2, pp = lane & 3;
  const v4u rq = srd(kSeq, 256);
  const unsigned fre = FLG + F_FREE + 4 * w;
  volatile LAS unsigned* full = (volatile LAS unsigned*)(size_t)(fa + F_FULL);
  const rsrc_t rh = mk_rsrc(a.ws + WS_H, 32 * NKS * 4096);
  const int ks = w >> 1, ah = w & 1;
  TR_DECL; TR(0);
  u64 tagr;
  {
    const int us = unit_start(j), nown = (j == 7 ? NKS : unit_start(j + 1)) - us;
    unsigned bias[3];
#pragma unroll
    for (int i = 0; i < 3; i++) bias[i] = *(const GAS u16*)(a.b1 + us * 32 + (3 * w + i) * 16 + tok);
    LAS char* wb[2][3];
#pragma unroll
    for (int i = 0; i < 3; i++) {
      const int ft = 3 * w + i;
      wb[0][i] = opq(sm + (6 * (8 * g + q4) + ((ft >> 1) ^ ((q4 >> 1) & 1))) * 64 + (((ft & 1) ^ (g & 1)) * 32) + 8 * pp);
      wb[1][i] = opq(wb[0][i] + L_RING);
    }
    LAS char* xb[2];
#pragma unroll
    for (int s = 0; s < 2; s++) xb[s] = opq(sm + L_X + tok * 128 + (((4 * s + g) ^ ((tok >> 1) & 7)) * 16));
    int hoffv = (g & 1) * 1024 + (g >> 1) * 256 + tok * 16; pin(hoffv);
    f32x4 acc[3][4], cs[3];
#pragma unroll
    for (int i = 0; i < 3; i++) { cs[i] = zero4();
#pragma unroll
      for (int T = 0; T < 4; T++) acc[i][T] = zero4(); }
    v4u ones = {0x3f803f80u, 0x3f803f80u, 0x3f803f80u, 0x3f803f80u};
    pin(ones);
#pragma unroll
    for (int i = 0; i < 3; i++) { bias[i] = g ? 0u : bias[i] << 16; pin(bias[i]); }
    v2u wfr[2][3][2]; v4u xfr[2][4];
    auto rd = [&](auto U, auto K) LAM {
      constexpr int u = U, k = K, wa = w_addr(u), hi = wa >= L_RING;
      if constexpr (k >= 2 && k < 6) xfr[u & 1][k - 2] = *(LAS v4u*)(xb[u & 1] + (CE(x_addr(u >> 1)) - L_X) + (k - 2) * 2048);
      else { constexpr int i = k < 2 ? 0 : (k - 4) >> 1; wfr[u & 1][i][k & 1] = tr_read(wb[hi][i] + (wa - hi * L_RING) + (k & 1) * 1536); }
    };
    lds_wait<F_FULL, need_W(0)>(fa);
    TR(1);
    static_for<0, 10>([&](auto K) LAM { rd(std::integral_constant<int, 0>{}, K); });
    tagr = tag_of(sm, 1);
    lds_wait<F_FULL, need_W(1)>(fa);
    dma_flag<1>(rq, fre);
    static_for<0, 16>([&](auto U) LAM {
      constexpr int u = U, b = u & 1;
      if constexpr (u == 4 || u == 8 || u == 12) TR(2 + (u - 4) / 4);
      unsigned fv; bool lack = false;
      if constexpr (u < 14) fv = *full;
      static_for<0, 15>([&](auto M) LAM {
        constexpr int m = M, i = m / 5, T = m % 5;
        const v4u wv = {wfr[b][i][0].x, wfr[b][i][0].y, wfr[b][i][1].x, wfr[b][i][1].y};
        SB();
        if constexpr (T < 4) mfma_bf(acc[i][T], wv, xfr[b][T]); else mfma_bf(cs[i], ones, wv);
        SB();
        if constexpr (u < 15 && m < 10) rd(std::integral_constant<int, u + 1>{}, M);
        if constexpr (u < 14 && m == 11) lack = wany(fv < CE(need_W(u + 2)));
        if constexpr (u + 2 <= 9 && m == 13) { LGKM0(); dma_flag<u + 2>(rq, fre); }
        if constexpr ((u + 2 == FREE_SCR || u + 2 == 16) && m == 10) { VMCNT(0); lds_put(fre, u + 2); }
      });
      SB();
      if constexpr (u < 14) { if (__builtin_expect(lack, 0)) lds_wait<F_FULL, need_W(u + 2)>(fa); CFENCE(); }
    });
    MFMA_DONE();
    TR(5);
    lds_wait<F_XD, 2>(fa);
    v4u sb[4];
#pragma unroll
    for (int T = 0; T < 4; T++) sb[T] = *(LAS v4u*)(sm + L_STAT + (16 * T + tok) * 16);
    v4u av[3];
#pragma unroll
    for (int i = 0; i < 3; i++) {
      const float c = g ? 0.f : cs[i][0];
      const float r1 = c - rbf(c), r2 = r1 - rbf(r1);
      av[i] = v4u{pk_bf(c, r1), pk_bf(r2, u2f(bias[i])), 0u, 0u};
    }
#pragma unroll
    for (int T = 0; T < 4; T++) sb[T].z = 0;
    pin(av[0]); pin(av[1]); pin(av[2]); pin(sb[0]); pin(sb[1]); pin(sb[2]); pin(sb[3]);
    NOP2();
#pragma unroll
    for (int i = 0; i < 3; i++)
#pragma unroll
      for (int T = 0; T < 4; T++) mfma_bf(acc[i][T], av[i], sb[T]);
    SB();
#pragma unroll
    for (int i = 0; i < 3; i++) {
      const int ft = 3 * w + i;
      const int so = (p * NKS + us + (ft >> 1)) * 4096 + (ft & 1) * 512;
      const int vo = hoffv | ((ft >> 1) < nown ? 0 : 0x40000000);
#pragma unroll
      for (int T = 0; T < 4; T += 2) {
        unsigned pk[2][2];
#pragma unroll
        for (int t = 0; t < 2; t++)
#pragma unroll
          for (int r = 0; r < 2; r++) {
            const float t0 = acc[i][T + t][2 * r], t1 = acc[i][T + t][2 * r + 1];
            pk[t][r] = relu_pk(pk_bf(t0 * __builtin_fabsf(t0), t1 * __builtin_fabsf(t1)));
          }
        auto r0 = __builtin_amdgcn_permlane16_swap(pk[0][0], pk[1][0], false, false);
        auto r1 = __builtin_amdgcn_permlane16_swap(pk[0][1], pk[1][1], false, false);
        st_sc1(v4u{r0[0], r1[0], r0[1], r1[1]}, rh, vo + T * 1024, so);
        SB();
      }
    }
  }
  TR(6);
  GAS u64* pf = gflag(a, GF_H + p * 32 + (ks ? 16 + (lane & 15) : min(lane, 19)));
  LAS char* wb[2][4];
  const int phi = ((q4 >> 1) & 1) | ((g & 1) << 1);
#pragma unroll
  for (int e = 0; e < 4; e++) { wb[0][e] = opq(sm + L_V + ks * 4096 + (8 * g + q4) * 128 + ((e ^ phi) * 32) + 8 * pp); wb[1][e] = opq(wb[0][e] + VH * 8192); }
  f32x4 acc[4][2];
#pragma unroll
  for (int e = 0; e < 4; e++) { acc[e][0] = zero4(); acc[e][1] = zero4(); }
  pin(pf);
  publish(gflag(a, GF_H + (p * 8 + j) * 4 + w), tagr, lane);
  TR(7);
  v2u bias2[4];
#pragma unroll
  for (int e = 0; e < 4; e++) bias2[e] = *(const GAS v2u*)(a.b2 + 64 * j + 16 * e + 4 * g);
  v2u wfr[2][4][2];
  auto rd2 = [&](auto I, auto K) LAM {
    constexpr int i = I, k = K, t = i % NVB;
    wfr[i & 1][k >> 1][k & 1] = tr_read(wb[t / VH][k >> 1] + (t % VH) * 8192 + (k & 1) * 512);
  };
  __builtin_amdgcn_s_sleep(HDELAY);
  poll(pf, tagr);
  TR(8);
  const int hoff = (p * NKS + 22 * ks) * 4096 + ah * 2048;
  v4u hf[PD][2];
#pragma unroll
  for (int i = 0; i < PD; i++)
#pragma unroll
    for (int T = 0; T < 2; T++) hf[i][T] = __builtin_amdgcn_raw_buffer_load_b128(rh, lane * 16 + T * 1024, hoff + i * 4096, AUX_SC1);
  lds_wait<F_FULL, need_V(1)>(fa);
  static_for<0, 8>([&](auto K) LAM { rd2(std::integral_constant<int, 0>{}, K); });
  static_for<0, 22>([&](auto I) LAM {
    constexpr int i = I, b = i & 1;
    if constexpr (i == 1 || i == 8 || i == 15) TR(9 + (i - 1) / 7);
    if (i < 21 || ks == 0) {
      constexpr bool chk = i + 2 < 22;
      unsigned fv; bool lack = false;
      if constexpr (chk) fv = *full;
      const v4u h0 = hf[i % PD][0], h1 = hf[i % PD][1];
      static_for<0, 8>([&](auto M) LAM {
        constexpr int m = M, e = m >> 1;
        const v4u wv = {wfr[b][e][0].x, wfr[b][e][0].y, wfr[b][e][1].x, wfr[b][e][1].y};
        SB();
        mfma_bf(acc[e][m & 1], wv, (m & 1) ? h1 : h0);
        SB();
        if constexpr (i < 21) rd2(std::integral_constant<int, i + 1>{}, M);
        if constexpr (chk && m == 6) lack = wany(fv < CE(need_V(i + 2)));
      });
      SB();
      if constexpr (i + NVB < 22) lds_put(fre, 17 + i);
      if constexpr (i + PD < 22) {
        const int so = hoff + (i + PD < 21 ? i + PD : 21 - ks) * 4096;
#pragma unroll
        for (int T = 0; T < 2; T++) hf[i % PD][T] = __builtin_amdgcn_raw_buffer_load_b128(rh, lane * 16 + T * 1024, so, AUX_SC1);
      }
      if constexpr (chk) { if (__builtin_expect(lack, 0)) lds_wait<F_FULL, need_V(i + 2)>(fa); CFENCE(); }
    }
  });
  MFMA_DONE();
  TR(12);
  lds_wait<0, 2>((unsigned)(FLG + F_YRDY));
  TR(13);
  LAS char* ex = sm + L_EX;
#pragma unroll
  for (int e = 0; e < 4; e++) *(LAS f32x4*)(ex + w * 4096 + e * 1024 + lane * 16) = sel4(ks, acc[e][0], acc[e][1]);
  lds_put(FLG + F_GX + 4 * w, 1);
  const int T = 2 * ah + ks;
  v2u yv[4], xv[4];
#pragma unroll
  for (int e = 0; e < 4; e++) {
    yv[e] = tr_read(sm + L_Y + (16 * e + 4 * g + q4) * YROW + T * 32 + 8 * pp);
    xv[e] = *(LAS v2u*)(sm + L_X + (16 * T + tok) * 128 + (((2 * e + (g >> 1)) ^ ((tok >> 1) & 7)) * 16) + (g & 1) * 8);
  }
  const float rs2 = *(LAS float*)(sm + L_STAT + (16 * T + tok) * 16 + 8);
  float rest[4][4];
#pragma unroll
  for (int e = 0; e < 4; e++) {
    const f32x4 m = sel4(ks, acc[e][1], acc[e][0]);
    rest[e][0] = __builtin_fmaf(m[0], rs2, bf_lo(bias2[e].x) + (bf_lo(yv[e].x) + bf_lo(xv[e].x)));
    rest[e][1] = __builtin_fmaf(m[1], rs2, bf_hi(bias2[e].x) + (bf_hi(yv[e].x) + bf_hi(xv[e].x)));
    rest[e][2] = __builtin_fmaf(m[2], rs2, bf_lo(bias2[e].y) + (bf_lo(yv[e].y) + bf_lo(xv[e].y)));
    rest[e][3] = __builtin_fmaf(m[3], rs2, bf_hi(bias2[e].y) + (bf_hi(yv[e].y) + bf_hi(xv[e].y)));
#pragma unroll
    for (int r = 0; r < 4; r++) pin(rest[e][r]);
  }
  lds_wait<0, 1>(FLG + F_GX + 4 * (w ^ 2));
  LAS char* rl = ex + (w ^ 2) * 4096;
  f32x4 ov[4];
#pragma unroll
  for (int e = 0; e < 4; e++) ov[e] = *(LAS f32x4*)(rl + e * 1024 + lane * 16);
  CFENCE();
#pragma unroll
  for (int e = 0; e < 4; e++) {
    const f32x4 o = ov[e];
    *(LAS v2u*)(rl + tok * 144 + e * 32 + g * 8) = v2u{pk_bf(__builtin_fmaf(o[0], rs2, rest[e][0]), __builtin_fmaf(o[1], rs2, rest[e][1])),
                                                       pk_bf(__builtin_fmaf(o[2], rs2, rest[e][2]), __builtin_fmaf(o[3], rs2, rest[e][3]))};
  }
  CFENCE();
#pragma unroll
  for (int i = 0; i < 2; i++) {
    const int r = 8 * i + (lane >> 3);
    const v4u o = *(LAS v4u*)(rl + r * 144 + (lane & 7) * 16);
    *(GAS v4u*)(a.out + (size_t)(64 * p + 16 * T + r) * DM + 64 * j + (lane & 7) * 8) = o;
  }
  if (w == 0 && lane < 2 && (lane ? j == 0 : p == 0)) {
    const u64 e1 = *(LAS u64*)(sm + FLG + F_EP + lane * 8) + 1;
    __hip_atomic_store(gflag(a, lane ? GF_ER + 16 * p : GF_EC + 16 * j), e1, __ATOMIC_RELAXED, __HIP_MEMORY_SCOPE_AGENT);
  }
  TR(14);
  if (w == 0) TR_DUMP(16, 15);
}

struct cpx { float r, i; };
DEV cpx cmul(cpx a, cpx b) { return cpx{__builtin_fmaf(a.r, b.r, -a.i * b.i), __builtin_fmaf(a.r, b.i, a.i * b.r)}; }
DEV cpx cexp_p(float zr2, float zi2, float pos) {
  const float e = __builtin_amdgcn_exp2f(zr2 * pos), th = __builtin_amdgcn_fractf(zi2 * pos);
  return cpx{e * __builtin_amdgcn_cosf(th), e * __builtin_amdgcn_sinf(th)};
}
DEV float exp_bf(float v) { return rbf(__builtin_amdgcn_exp2f(v * 1.44269504088896f)); }
DEV void pow2_scale(float mx, float& s, float& inv) {
  const int e = min(max((int)(f2u(mx) >> 23), 15), 254);
  s = u2f((unsigned)(268 - e) << 23);
  inv = u2f((unsigned)(e - 14) << 23);
}

DEV void role_conv(const Args& a, LAS char* sm, int cw, int lane, int p, int j, unsigned fa) {
  const int g = lane >> 4, tok = lane & 15;
  const int ch = cw >> 1, mh = cw & 1, d = 64 * j + 2 * p + ch;
  const unsigned fme = FLG + F_CW + 4 * cw, fpa = FLG + F_CW + 4 * (cw ^ 1);
  TR_DECL; TR(0);
  const unsigned lar = ((const GAS unsigned*)a.lar)[d * 64 + lane], aim = ((const GAS unsigned*)a.aim)[d * 64 + lane];
  const unsigned cre = ((const GAS unsigned*)a.cre)[d * 64 + lane], cim = ((const GAS unsigned*)a.cim)[d * 64 + lane];
  const float ldt = bf_lo(*(const GAS u16*)(a.ldt + d)), dsk = bf_lo(*(const GAS u16*)(a.dsk + d));
  const int x0 = 1024 * mh + 16 * lane;
  unsigned ga[18];
#pragma unroll
  for (int i = 0; i < 18; i += 2) {
    const int j0 = 2047 - (x0 + i), j1 = j0 - 1;
    const unsigned c = pk_bf((float)j0, (float)j1);
    const int i0 = j0 < 128 ? (j0 < 0 ? 703 : j0) : (int)(c & 0xffff) - 0x4300 + 128;
    const int i1 = j1 < 128 ? (j1 < 0 ? 703 : j1) : (int)(c >> 16) - 0x4300 + 128;
    ga[i] = 2 * i0 + L_TAB + ch * TABB; ga[i + 1] = 2 * i1 + L_TAB + ch * TABB;
    pin(ga[i]); pin(ga[i + 1]);
  }
  LAS char* xb[2];
#pragma unroll
  for (int s = 0; s < 2; s++) xb[s] = opq(sm + L_X + (16 * cw + tok) * 128 + (((4 * s + g) ^ ((tok >> 1) & 7)) * 16));
  v4u ones = {0x3f803f80u, 0x3f803f80u, 0x3f803f80u, 0x3f803f80u};
  v4u idm[2];
#pragma unroll
  for (int h = 0; h < 2; h++)
#pragma unroll
    for (int e = 0; e < 4; e++) {
      const int k0 = 8 * g + 2 * e - 16 * h;
      idm[h][e] = (k0 == tok ? 0x3f80u : 0u) | (k0 + 1 == tok ? 0x3f800000u : 0u);
    }
  pin(ones); pin(idm[0]); pin(idm[1]);
  unsigned pv[53];
  const float dt = exp_bf(ldt);
  const int sh = mh ? 0 : 16;
  const float zr2 = -exp_bf(bf_hi(lar << sh)) * dt * 1.44269504088896f, zi2 = bf_hi(aim << sh) * dt * 0.159154943091895f;
  {
    const cpx E8 = cexp_p(zr2, zi2, 8.f), E32 = cexp_p(zr2, zi2, 32.f);
    cpx Y[4]; Y[0] = cpx{512.f, 0.f}; Y[1] = cpx{512.f * E8.r, 512.f * E8.i}; Y[2] = cmul(Y[1], E8); Y[3] = cmul(Y[2], E8);
#pragma unroll
    for (int b = 0; b < 32; b++) { pv[21 + b] = pk_h(Y[b & 3].r, Y[b & 3].i); pin(pv[21 + b]); if (b < 28) Y[b & 3] = cmul(Y[b & 3], E32); }
  }
  TR(1);
  f32x4 sa = zero4(), gr = zero4();
  v4u xl[2];
  static_for<0, 2>([&](auto B) LAM {
    constexpr int c0 = B == 0 ? 0 : 5, c1 = B == 0 ? 5 : 8;
    lds_wait<F_FULL, B == 0 ? need_X(4) + 1 : need_X(7)>(fa);
    if constexpr (B == 1) { TR(2); __builtin_amdgcn_s_setprio(3); }
    v4u f[2 * (c1 - c0)];
#pragma unroll
    for (int c = c0; c < c1; c++) { f[2 * (c - c0)] = *(LAS v4u*)(xb[0] + ((c + 1) & 7) * 8192); f[2 * (c - c0) + 1] = *(LAS v4u*)(xb[1] + ((c + 1) & 7) * 8192); }
    SB();
#pragma unroll
    for (int i = 0; i < 2 * (c1 - c0); i++) { mfma_bf(sa, ones, f[i]); mfma_bf(gr, f[i], f[i]); }
    if constexpr (B == 1) { xl[0] = f[4]; xl[1] = f[5]; }
  });
  const u64 tagc = tag_of(sm, 0);
  MFMA_DONE();
  const float dg = (tok & 2) ? ((tok & 1) ? gr[3] : gr[2]) : ((tok & 1) ? gr[1] : gr[0]);
  const float mean = sa[0] * (1.f / DM), ex2 = xsum32(xsum16(g == (tok >> 2) ? dg : 0.f)) * (1.f / DM);
  float var = __builtin_fmaf(-mean, mean, ex2);
  if (wany(var < ex2 * (1.f / 1024.f))) {
    float q = 0;
#pragma nounroll
    for (int c = 0; c < 16; c++) {
      const v4u u = *(LAS v4u*)((c & 1 ? xb[1] : xb[0]) + (c >> 1) * 8192);
#pragma unroll
      for (int e = 0; e < 4; e++) { const unsigned uu = u[e]; const float d0 = bf_lo(uu) - mean, d1 = bf_hi(uu) - mean; q = __builtin_fmaf(d0, d0, __builtin_fmaf(d1, d1, q)); }
    }
    var = xsum32(xsum16(q)) * (1.f / DM);
  }
  const float m = rbf(mean), sd = rbf(__builtin_amdgcn_sqrtf(rbf(rbf(var) + 1e-5f))), rs = __builtin_amdgcn_rcpf(sd);
  const rsrc_t ru = mk_rsrc(a.ws + WS_U, SEQ * DM * 2);
  dma_flag<1>(srd(kSeq, 256), FLG + F_XD + 4 * cw);
  {
    const float c0 = -m * rs;
    v4u xn[2]; f32x4 t[4];
#pragma unroll
    for (int s = 0; s < 2; s++)
#pragma unroll
      for (int e = 0; e < 4; e++) { const unsigned uu = xl[s][e]; xn[s][e] = pk_bf(__builtin_fmaf(bf_lo(uu), rs, c0), __builtin_fmaf(bf_hi(uu), rs, c0)); }
#pragma unroll
    for (int i = 0; i < 4; i++) t[i] = zero4();
    pin(xn[0]); pin(xn[1]);
    NOP2();
#pragma unroll
    for (int i = 0; i < 4; i++) mfma_bf(t[i], xn[i >> 1], idm[i & 1]);
    MFMA_DONE();
#pragma unroll
    for (int s = 0; s < 2; s++) {
      unsigned h0 = pk_h(t[2 * s][0], t[2 * s][1]), h1 = pk_h(t[2 * s + 1][0], t[2 * s + 1][1]), h2 = pk_h(t[2 * s][2], t[2 * s][3]), h3 = pk_h(t[2 * s + 1][2], t[2 * s + 1][3]);
      auto r0 = __builtin_amdgcn_permlane16_swap(h0, h1, false, false);
      auto r1 = __builtin_amdgcn_permlane16_swap(h2, h3, false, false);
      st_sc1(v4u{r0[0], r1[0], r0[1], r1[1]}, ru, (8 * (g & 1) + (tok >> 1)) * 8192 + (g >> 1) * 32 + (tok & 1) * 16,
                                             (j * 32 + 16 * s) * 8192 + p * 256 + cw * 64);
    }
  }
  TR(3);
  v4u stat = {pk_bf(-m, -m), pk_bf(-m, sd), f2u(rs * rs), 0u};
  pin(stat);
  SB();
  publish(gflag(a, GF_U + (j * 32 + p) * 4 + cw), tagc, lane);
  __builtin_amdgcn_s_setprio(0);
  SB();
  TR(4);
  float kq, kd, kinv;
  {
    const float csum = wave_sum((__builtin_fabsf(bf_lo(cre)) + __builtin_fabsf(bf_hi(cre))) + (__builtin_fabsf(bf_lo(cim)) + __builtin_fabsf(bf_hi(cim))));
    float cs, cinv, ks;
    pow2_scale(csum, cs, cinv);
    pow2_scale(__builtin_fmaf(2.f * dt, csum, __builtin_fabsf(dsk)), ks, kinv);
    kq = cinv * dt * (2.f / 512.f) * ks; kd = (lane | mh) == 0 ? dsk * ks : 0.f;
    const cpx E1 = cexp_p(zr2, -zi2, 1.f), E256 = cexp_p(zr2, -zi2, 256.f);
    auto put = [&](int row, cpx v) LAM { pv[row] = pk_h(v.r, v.i); pin(pv[row]); };
    cpx X[8]; X[0] = cpx{bf_hi(cre << sh) * cs, bf_hi(cim << sh) * -cs};
#pragma unroll
    for (int o = 1; o < 8; o++) X[o] = cmul(X[o - 1], E1);
#pragma unroll
    for (int o = 0; o < 8; o++) put(o, X[o]);
    cpx A = cmul(X[0], E256), B = cmul(X[4], E256);
    put(8, A); put(9, cmul(X[2], E256)); put(10, B); put(11, cmul(X[6], E256));
    A = cmul(A, E256); B = cmul(B, E256); put(12, A); put(13, B);
    A = cmul(A, E256); B = cmul(B, E256); put(14, A); put(15, B);
#pragma unroll
    for (int o = 16; o < 21; o++) { A = cmul(A, E256); put(o, A); }
    pin(kq); pin(kd); pin(kinv);
  }
  LAS char* pa0 = opq(sm + L_V + ch * SCR + tok * SROW + g * 16);
  LAS char* pa1 = opq(sm + L_V + ch * SCR + min(16 + tok, 20) * SROW + g * 16);
  LAS char* pb = opq(sm + L_V + ch * SCR + (21 + 16 * mh + tok) * SROW + g * 16);
  f32x4 k0 = zero4(), k1 = zero4();
  const int bq = 16 * mh + tok;
  LAS char* tb = sm + L_TAB + ch * TABB;
  LAS char* t0a = opq(tb + 2 * (g < 2 ? 8 * bq + 4 * g : (g == 2 ? 256 + 4 * bq : 384 + 2 * bq)));
  LAS char* t0b = opq(t0a + (g == 3 ? 128 : 4));
  LAS char* t1a = opq(tb + 2 * (g == 0 ? 512 + bq : (g == 1 ? 640 + bq : 672 + bq)));
  const int t1s = g == 0 ? 64 : 0;
  TR(12);
  lds_wait<F_XD, 1>(fa);
  lds_wait<F_FULL, need_W(15)>(fa);
  if (g == 0) *(LAS v4u*)(sm + L_STAT + (16 * cw + tok) * 16) = stat;
  lds_put(FLG + F_XD + 4 * cw, 2);
  lds_wait<F_FREE, FREE_SCR>(fa);
  TR(5);
  set_m0(L_X + ch * SCR + mh * 256);
  static_for<0, 53>([&](auto R) LAM { constexpr int r = R; put_tid<8192 + r * SROW>(pv[r]); });
  lds_put(fme, 1);
  lds_wait<0, 1>(fpa);
  TR(6);
  {
    v4u f0[8], f1[8], fb[8];
#pragma unroll
    for (int s = 0; s < 8; s++) { fb[s] = *(LAS v4u*)(pb + 64 * s); f0[s] = *(LAS v4u*)(pa0 + 64 * s); f1[s] = *(LAS v4u*)(pa1 + 64 * s); }
    SB();
#pragma unroll
    for (int s = 0; s < 8; s++) { mfma_h(k0, f0[s], fb[s]); mfma_h(k1, f1[s], fb[s]); }
  }
  MFMA_DONE();
  {
    const float kz = g < 2 ? kq : 0.f;
    *(LAS unsigned*)t0a = pk_h(__builtin_fmaf(k0[0], kq, kd), k0[1] * kq);
    *(LAS unsigned*)t0b = pk_h(k0[2] * kq, k0[3] * kq);
    const unsigned q0 = pk_h(k1[0] * kz, k1[1] * kz), q1 = pk_h(k1[2] * kz, k1[3] * kz);
    *(LAS u16*)(t1a) = (u16)q0; *(LAS u16*)(t1a + t1s) = (u16)(q0 >> 16); *(LAS u16*)(t1a + 2 * t1s) = (u16)q1; *(LAS u16*)(t1a + 3 * t1s) = (u16)(q1 >> 16);
  }
  lds_put(fme, 2);
  TR(7);
  lds_wait<0, 2>(fpa);
  LAS char* cr = sm + ch * CH;
  {
    unsigned P[9], Q[8];
#pragma unroll
    for (int i = 0; i < 9; i++) P[i] = (unsigned)*(LAS u16*)(size_t)ga[2 * i] | ((unsigned)*(LAS u16*)(size_t)ga[2 * i + 1] << 16);
#pragma unroll
    for (int i = 0; i < 8; i++) Q[i] = __builtin_amdgcn_alignbit(P[i + 1], P[i], 16);
    LAS char* kc = cr + x0 * 2;
    *(LAS v4u*)(kc) = v4u{P[0], P[1], P[2], P[3]}; *(LAS v4u*)(kc + 16) = v4u{P[4], P[5], P[6], P[7]};
    *(LAS v4u*)(kc + KCOPY) = v4u{Q[0], Q[1], Q[2], Q[3]}; *(LAS v4u*)(kc + KCOPY + 16) = v4u{Q[4], Q[5], Q[6], Q[7]};
  }
  lds_put(fme, 3);
  LAS char* ab[4];
#pragma unroll
  for (int i = 0; i < 4; i++) ab[i] = opq(cr + ((15 - tok) & 1) * KCOPY + 2 * (((15 - tok) & ~1) + 8 * g) + 64 * (4 - 4 * mh) + 1024 * i);
  LAS char* bb = opq(cr + C_U + 448 + 32 * tok + 16 * g - 64 * (4 * mh + 3));
  f32x4 acc[8];
#pragma unroll
  for (int c = 0; c < 8; c++) acc[c] = zero4();
  auto lda = [&](int i, int r, int k) LAM { return ld2(ab[(7 - i) >> 1] + 64 * (59 - 8 * i - r) - 1024 * ((7 - i) >> 1) + 8 * k); };
  TR(13);
  __builtin_amdgcn_s_setprio(3);
  lds_wait<0, 3>(fpa);
  lds_wait<0, 1>((unsigned)(FLG + F_YRDY));
  TR(8);
  v2u A[8][2]; v4u B[2][8];
#pragma unroll
  for (int i = 0; i < 8; i++) {
    B[0][i] = *(LAS v4u*)(bb + 64 * (8 * i + 3));
    A[7 - i][0] = lda(7 - i, 0, 0); A[7 - i][1] = lda(7 - i, 0, 1);
  }
  static_for<0, 4>([&](auto R) LAM {
    constexpr int r = R;
    static_for<0, 8>([&](auto II) LAM {
      constexpr int i = 7 - II;
      static_for<0, 8 - i>([&](auto K) LAM {
        constexpr int k = K;
        SB();
        mfma_h(acc[i + k], v4u{A[i][0].x, A[i][0].y, A[i][1].x, A[i][1].y}, B[r & 1][k]);
        SB();
        if constexpr (r < 3) {
          if constexpr (i < 7 && k < 2) A[i + 1][k] = lda(i + 1, r + 1, k);
          if constexpr (i <= 4 && i >= 1 && (k == 2 || k == 3)) B[(r + 1) & 1][2 * (4 - i) + k - 2] = *(LAS v4u*)(bb + 64 * (8 * (2 * (4 - i) + k - 2) + 3 - (r + 1)));
        }
        if constexpr (r == 3 && i == 0 && k == 1) lds_put(fme, 4);
      });
    });
    if constexpr (r < 3) { A[0][0] = lda(0, r + 1, 0); A[0][1] = lda(0, r + 1, 1); }
  });
  MFMA_DONE();
  TR(9);
  lds_wait<0, 4>(fpa);
  LAS char* exm = cr + mh * 4096;
  LAS char* exo = cr + (mh ^ 1) * 4096;
#pragma unroll
  for (int c = 0; c < 4; c++) *(LAS f32x4*)(exm + c * 1024 + lane * 16) = sel4(mh, acc[c], acc[4 + c]);
  lds_put(fme, 5);
  lds_wait<0, 5>(fpa);
  LAS char* yb = cr + 8192 + mh * 2048;
#pragma unroll
  for (int c = 0; c < 4; c++) {
    const f32x4 o = *(LAS f32x4*)(exo + c * 1024 + lane * 16), mm = sel4(mh, acc[4 + c], acc[c]);
    *(LAS v2u*)(yb + 512 * c + 32 * tok + 8 * g) = v2u{pk_bf((mm[0] + o[0]) * kinv, (mm[1] + o[1]) * kinv), pk_bf((mm[2] + o[2]) * kinv, (mm[3] + o[3]) * kinv)};
  }
  CFENCE();
  const rsrc_t ry = mk_rsrc(a.ws + WS_Y, SEQ * DM * 2);
#pragma unroll
  for (int i = 0; i < 2; i++) st_sc1(*(LAS v4u*)(yb + i * 1024 + lane * 16), ry, lane * 16 + i * 1024, d * 4096 + mh * 2048);
  lds_put(fme, 6);
  TR(10);
  publish(gflag(a, GF_Y + (j * 2 + mh) * 64 + 2 * p + ch), tagc, lane);
  TR(11);
  if (cw == 0) TR_DUMP(48, 14);
}

__global__ __launch_bounds__(NTHR, 3) NOPK void k_fused(Args a) {
  __shared__ __attribute__((aligned(16))) char smem[LDS_BYTES];
  LAS char* sm = (LAS char*)smem;
  const int lane = threadIdx.x & 63, w = __builtin_amdgcn_readfirstlane(threadIdx.x >> 6);
  const int b = blockIdx.x, p = (b & 7) * 4 + (b >> 6), j = (b >> 3) & 7;
  unsigned fa = FLG + 4 * (lane & 3);
  pin(fa);
  if (w == 0) { ((LAS unsigned*)(sm + FLG))[lane] = 0; LGKM0(); }
  __builtin_amdgcn_s_barrier();
  if (w >= 8) { __builtin_amdgcn_s_setprio(1); role_loader(a, sm, w - 8, lane, p, j, fa); }
  else if (w < 4) { __builtin_amdgcn_s_setprio(2); role_gemm(a, sm, w, lane, p, j, fa); }
  else { role_conv(a, sm, w - 4, lane, p, j, fa); }
}

extern "C" void kernel_launch(void* const* d_in, const int* in_sizes, int n_in, void* d_out, int out_size, void* d_ws, size_t ws_size, hipStream_t stream) {
  Args a;
  a.x = (const u16*)d_in[0]; a.lar = (const u16*)d_in[1]; a.aim = (const u16*)d_in[2]; a.cre = (const u16*)d_in[3]; a.cim = (const u16*)d_in[4];
  a.ldt = (const u16*)d_in[5]; a.dsk = (const u16*)d_in[6]; a.w1 = (const u16*)d_in[8]; a.b1 = (const u16*)d_in[9]; a.w2 = (const u16*)d_in[10];
  a.b2 = (const u16*)d_in[11]; a.out = (u16*)d_out; a.ws = (char*)d_ws;
  hipMemsetAsync(a.ws + WS_DIAG, 0, 32768, stream);
  k_fused<<<256, NTHR, 0, stream>>>(a);
}
```

```cpp
#include <hip/hip_runtime.h>
#include <stdint.h>
#include <type_traits>

#define LAS __attribute__((address_space(3)))
#define GAS __attribute__((address_space(1)))
#if defined(__HIP_DEVICE_COMPILE__) && __HIP_DEVICE_COMPILE__
#define NOPK __attribute__((target("no-packed-fp32-ops")))
#else
#define NOPK
#endif
#define DEV __device__ __forceinline__ NOPK
#define LAM __attribute__((always_inline)) NOPK
typedef unsigned short u16;
typedef unsigned long long u64;
typedef unsigned v4u __attribute__((ext_vector_type(4)));
typedef unsigned v2u __attribute__((ext_vector_type(2)));
typedef unsigned v2u4 __attribute__((ext_vector_type(2), aligned(4)));
typedef __bf16 bf16x8 __attribute__((ext_vector_type(8)));
typedef _Float16 f16x8 __attribute__((ext_vector_type(8)));
typedef float f32x4 __attribute__((ext_vector_type(4)));
typedef short s16x4 __attribute__((ext_vector_type(4)));
typedef short s16x2 __attribute__((ext_vector_type(2)));
typedef __amdgpu_buffer_rsrc_t rsrc_t;

constexpr int SEQ = 2048, DM = 512, NMODE = 128, FF = 1376, NKS = 43;
constexpr size_t WS_H = 0, WS_U = 6u << 20, WS_Y = 10u << 20, WS_FLAG = 14u << 20, WS_DIAG = 16u << 20;
constexpr int GF_U = 0, GF_H = 1024, GF_Y = 2048, GF_EC = 3072, GF_ER = 3200;
#define FSTR 16
#define PSLEEP 2
#define UWAIT 10
#define YDELAY 8
#define CPRIO 1
#define TPRIO 2
#define PUBAT 1
#define W2PF 9
#define LPRIO 1
#define KCPRIO 3
#define SSLEEP 4
#define IPRIO 1
#define ND 6
#define SPRIO 1
#define YGATE 11
#define ROT 0
#define SKIP6 1
#define ROTF(j) (ROT == 1 ? unit_start(j) : ROT == 2 ? ((j) & 1) * 22 : ROT == 3 ? ((j) & 3) * 11 : 0)
#define HAUX AUX_SC1
#define DW 0
#define PF_W
#define PF_CODE
#define HDELAY 20
constexpr int NTHR = 768;
constexpr int POLL_CAP = 1 << 14;
constexpr int AUX_SC1 = 16;

struct Args {
  const u16 *x, *lar, *aim, *cre, *cim, *ldt, *dsk, *w1, *b1, *w2, *b2;
  u16* out;
  char* ws;
};

#define TR_DECL
#define TR(i)
#define TR_DUMP(base, n)

template <int I, int N, class Fn> DEV void static_for(Fn&& f) {
  if constexpr (I < N) { f(std::integral_constant<int, I>{}); static_for<I + 1, N>(f); }
}
#define CE(x) (std::integral_constant<int, (x)>::value)
template <class T> DEV void pin(T& v) { asm volatile("" : "+v"(v)); }
template <class T> DEV void pins(T& v) { asm volatile("" : "+s"(v)); }
DEV float u2f(unsigned u) { return __builtin_bit_cast(float, u); }
DEV unsigned f2u(float f) { return __builtin_bit_cast(unsigned, f); }
DEV bool wany(bool c) { return __builtin_amdgcn_ballot_w64(c) != 0; }
DEV rsrc_t mk_rsrc(const void* p, int bytes) { return __builtin_amdgcn_make_buffer_rsrc((void*)p, 0, bytes, 0x00020000); }
DEV float bf_lo(unsigned u) { return u2f(u << 16); }
DEV float bf_hi(unsigned u) { return u2f(u & 0xffff0000u); }
typedef float f32x2 __attribute__((ext_vector_type(2)));
typedef __bf16 bf16x2 __attribute__((ext_vector_type(2)));
typedef _Float16 f16x2 __attribute__((ext_vector_type(2)));
DEV unsigned pk_bf(float a, float b) { return __builtin_bit_cast(unsigned, __builtin_convertvector(f32x2{a, b}, bf16x2)); }
DEV unsigned pk_h(float a, float b) { return __builtin_bit_cast(unsigned, __builtin_convertvector(f32x2{a, b}, f16x2)); }
DEV unsigned pk_hn(float a, float b) { return pk_h(a, -b); }
DEV float rbf(float a) { return u2f(pk_bf(0.f, a)); }
DEV unsigned relu_pk(unsigned v) { return __builtin_bit_cast(unsigned, __builtin_elementwise_max(__builtin_bit_cast(s16x2, v), s16x2{0, 0})); }
DEV v2u tr_read(LAS char* p) { return __builtin_bit_cast(v2u, __builtin_amdgcn_ds_read_tr16_b64_v4i16((LAS s16x4*)p)); }
DEV v2u ld2(LAS char* p) { return *(LAS v2u4*)p; }
DEV float xsum16(float v) { auto r = __builtin_amdgcn_permlane16_swap(f2u(v), f2u(v), false, false); return u2f(r[0]) + u2f(r[1]); }
DEV float xsum32(float v) { auto r = __builtin_amdgcn_permlane32_swap(f2u(v), f2u(v), false, false); return u2f(r[0]) + u2f(r[1]); }
template <int CTRL> DEV float dpp_add(float v) { return v + u2f((unsigned)__builtin_amdgcn_update_dpp(0, (int)f2u(v), CTRL, 0xf, 0xf, false)); }
DEV float wave_sum(float v) { v = dpp_add<0x121>(v); v = dpp_add<0x122>(v); v = dpp_add<0x124>(v); v = dpp_add<0x128>(v); return xsum32(xsum16(v)); }
DEV f32x4 sel4(int c, f32x4 a, f32x4 b) { return f32x4{c ? a[0] : b[0], c ? a[1] : b[1], c ? a[2] : b[2], c ? a[3] : b[3]}; }
DEV LAS char* opq(LAS char* p) { unsigned v = (unsigned)(size_t)p; pin(v); return (LAS char*)(size_t)v; }
DEV f32x4 zero4() { f32x4 z = {0, 0, 0, 0}; pin(z); return z; }
#define VMCNT(n) asm volatile("s_waitcnt vmcnt(%0)" ::"i"(n) : "memory")
#define LGKM0() asm volatile("s_waitcnt lgkmcnt(0)" ::: "memory")
#define CFENCE() asm volatile("" ::: "memory")
#define SB() __builtin_amdgcn_sched_barrier(0)
DEV void mfma_bf(f32x4& c, v4u a, v4u b) { asm volatile("v_mfma_f32_16x16x32_bf16 %0, %1, %2, %0" : "+v"(c) : "v"(a), "v"(b)); }
DEV void mfma_bf0(f32x4& d, v4u a, v4u b) { asm volatile("v_mfma_f32_16x16x32_bf16 %0, %1, %2, 0" : "=&v"(d) : "v"(a), "v"(b)); }
DEV void mfma_bf16k(f32x4& c, v2u a, v2u b) { asm volatile("v_mfma_f32_16x16x16_bf16 %0, %1, %2, %0" : "+v"(c) : "v"(a), "v"(b)); }
DEV void mfma_h(f32x4& c, v4u a, v4u b) { asm volatile("v_mfma_f32_16x16x32_f16 %0, %1, %2, %0" : "+v"(c) : "v"(a), "v"(b)); }
#define NOP2() do { SB(); asm volatile("s_nop 1"); SB(); } while (0)
#define MFMA_DONE() do { SB(); asm volatile("s_nop 7"); SB(); } while (0)

DEV void st_sc1(v4u v, rsrc_t r, int voff, int soff) { __builtin_amdgcn_raw_buffer_store_b128(v, r, voff, soff, AUX_SC1); NOP2(); }
DEV void lds_put(unsigned addr, unsigned v) { asm volatile("s_mov_b64 exec, 1\n\tds_write_b32 %0, %1\n\ts_mov_b64 exec, -1" ::"v"(addr), "v"(v) : "memory"); }
template <int OFF, int WANT> DEV void lds_wait(unsigned fa) {
  unsigned t, n;
  asm volatile("s_mov_b32 %1, 0x20000\n"
               "1:\n\tds_read_b32 %0, %2 offset:%3\n\ts_waitcnt lgkmcnt(0)\n\tv_cmp_gt_u32 vcc, %4, %0\n\ts_cbranch_vccz 2f\n\t"
               "s_sleep 1\n\ts_sub_u32 %1, %1, 1\n\ts_cbranch_scc0 1b\n2:"
               : "=&v"(t), "=&s"(n) : "v"(fa), "i"(OFF), "i"(WANT) : "memory", "vcc", "scc");
}
template <int OFF> DEV void lds_wait_s(unsigned fa, int want) {
  unsigned t, n;
  asm volatile("s_mov_b32 %1, 0x20000\n"
               "1:\n\tds_read_b32 %0, %2 offset:%3\n\ts_waitcnt lgkmcnt(0)\n\tv_cmp_gt_u32 vcc, %4, %0\n\ts_cbranch_vccz 2f\n\t"
               "s_sleep 1\n\ts_sub_u32 %1, %1, 1\n\ts_cbranch_scc0 1b\n2:"
               : "=&v"(t), "=&s"(n) : "v"(fa), "i"(OFF), "s"(want) : "memory", "vcc", "scc");
}
static __device__ const unsigned kSeq[64] = {0,1,2,3,4,5,6,7,8,9,10,11,12,13,14,15,16,17,18,19,20,21,22,23,24,25,26,27,28,29,30,31,32,
  33,34,35,36,37,38,39,40,41,42,43,44,45,46,47,48,49,50,51,52,53,54,55,56,57,58,59,60,61,62,63};
DEV v4u srd(const void* p, unsigned bytes) { const u64 v = (u64)p; return v4u{(unsigned)v, (unsigned)(v >> 32) & 0xffffu, bytes, 0x00020000u}; }
DEV void dma16(v4u rs, unsigned lds, int voff, int soff) {
  asm volatile("s_mov_b32 m0, %0\n\ts_nop 0\n\tbuffer_load_dwordx4 %1, %2, %3 offen lds" ::"s"(lds), "v"(voff), "s"(rs), "s"(soff) : "memory");
}
template <int N> DEV void dma_flag(v4u rs, unsigned lds) {
  asm volatile("s_mov_b32 m0, %0\n\ts_mov_b64 exec, 1\n\tbuffer_load_dword off, %1, %2 lds\n\ts_mov_b64 exec, -1" ::"s"(lds), "s"(rs), "s"(N * 4) : "memory");
}
DEV void dma_ep(v4u rs, unsigned lds, int voff) {
  asm volatile("s_mov_b32 m0, %0\n\ts_mov_b64 exec, 3\n\tbuffer_load_dword %2, %1, 0 offen sc1 lds\n\ts_mov_b64 exec, -1" ::"s"(lds), "s"(rs), "v"(voff) : "memory");
}
constexpr int CODE_BYTES = 28 * 1024, L_JUNK = 163840 - 512, L_B2 = 163840 - 768;
DEV void dma_touch(v4u rs, int voff) {
  asm volatile("s_mov_b32 m0, %0\n\ts_nop 0\n\tbuffer_load_dword %2, %1, 0 offen lds" ::"s"(L_JUNK), "s"(rs), "v"(voff) : "memory");
}
DEV void dma_b2(v4u rs, int voff) {
  asm volatile("s_mov_b32 m0, %0\n\ts_mov_b64 exec, 0xffffffff\n\tbuffer_load_dword %2, %1, 0 offen lds\n\ts_mov_b64 exec, -1" ::"s"(L_B2), "s"(rs), "v"(voff) : "memory");
}
DEV void dma_touch1(v4u rs, int soff) {
  asm volatile("s_mov_b32 m0, %0\n\ts_mov_b64 exec, 1\n\tbuffer_load_dword off, %1, %2 lds\n\ts_mov_b64 exec, -1" ::"s"(L_JUNK), "s"(rs), "s"(soff) : "memory");
}
template <int OFF> DEV void put_tid(unsigned v) { asm volatile("ds_write_addtid_b32 %0 offset:%1" ::"v"(v), "i"(OFF) : "memory"); }
DEV void set_m0(unsigned v) { asm volatile("s_mov_b32 m0, %0\n\ts_nop 0" ::"s"(v) : "memory"); }
DEV GAS u64* gflag(const Args& a, int line) { return (GAS u64*)(a.ws + WS_FLAG) + line * FSTR; }
DEV void publish(GAS u64* f, u64 tag, int lane) {
  VMCNT(0);
  if (lane == 0) __hip_atomic_store(f, tag, __ATOMIC_RELAXED, __HIP_MEMORY_SCOPE_AGENT);
}
DEV void poll(GAS u64* p, u64 tag) {
#pragma nounroll
  for (int it = 0; it < POLL_CAP; it++) {
    if (!wany(__hip_atomic_load(p, __ATOMIC_RELAXED, __HIP_MEMORY_SCOPE_AGENT) != tag)) break;
    __builtin_amdgcn_s_sleep(PSLEEP);
  }
  __builtin_amdgcn_fence(__ATOMIC_ACQUIRE, "wavefront");
}
DEV void poll2(GAS u64* p, GAS u64* q, u64 tag) {
#pragma nounroll
  for (int it = 0; it < POLL_CAP; it++) {
    const u64 x = __hip_atomic_load(p, __ATOMIC_RELAXED, __HIP_MEMORY_SCOPE_AGENT), y = __hip_atomic_load(q, __ATOMIC_RELAXED, __HIP_MEMORY_SCOPE_AGENT);
    if (!wany(x != tag || y != tag)) break;
    __builtin_amdgcn_s_sleep(PSLEEP);
  }
  __builtin_amdgcn_fence(__ATOMIC_ACQUIRE, "wavefront");
}
DEV int unit_start(int j) { return (j * 37 + 3) / 7; }

constexpr int KCOPY = 4160, C_U = 2 * KCOPY, CH = C_U + 4608, TABB = 4352, L_STAT = 31744;
constexpr int L_X = 32768, L_RING = L_X + 65536, L_TAB = L_RING + 24576;
constexpr int L_Y = 0, YROW = 160, L_EX = L_Y + 64 * YROW;
constexpr int LDS_BYTES = 163840, FLG = LDS_BYTES - 256;
static_assert(4 * TABB <= 36864 && 2 * CH <= L_STAT && L_EX + 16384 <= L_STAT && L_RING + 61440 <= FLG, "lds");
constexpr int F_FULL = 0, F_FREE = 16, F_CW = 32, F_GX = 48, F_EP = 64, F_ST = 80, F_HOK = 96;

constexpr int NSTEP1 = 24;
#define KINDS 0, 0, 0, 0, 0, 0, 0, 0, 1, 1, 1, 1, 1, 1, 1, 1, 1, 1, 1, 1, 1, 1, 1, 1
constexpr int ST_KIND[NSTEP1] = {KINDS};
constexpr int st_idx(int s) { int n = 0; for (int i = 0; i < s; i++) n += ST_KIND[i] == ST_KIND[s]; return n; }
constexpr int step_of(int kind, int idx) { for (int s = 0; s < NSTEP1; s++) if (ST_KIND[s] == kind && st_idx(s) == idx) return s; return -1; }
constexpr int need_X(int c) { return step_of(0, c) + 1; }
constexpr int need_W(int u) { return step_of(1, u) + 1; }
constexpr int need_V(int t) { return NSTEP1 + t + 1; }
static_assert(need_X(0) < need_W(0) && need_X(1) < need_W(2) && need_X(2) < need_W(4) && need_X(3) < need_W(6) && need_X(7) < need_W(14), "x before its units");
constexpr int w_addr(int u) { return u % 7 < 5 ? L_RING + (u % 7) * 12288 : (u % 7 - 5) * 12288; }
constexpr int x_addr(int c) { return L_X + ((c + 1) & 7) * 8192; }
constexpr int FREE_C = 14;
constexpr int NVB = 14, L_V = L_X + 8192, VH = 7;
#define PD 6
#define VDEPTH 8

DEV int kstep(int v, int rot) { const int k = v + rot; return k >= NKS ? k - NKS : k; }
DEV void role_loader(const Args& a, LAS char* sm, int l, int lane, int p, int j, unsigned fa) {
  const v4u rx = srd(a.x, SEQ * DM * 2), rw = srd(a.w1, DM * FF * 2), rv = srd(a.w2, DM * FF * 2), rq = srd(kSeq, 256);
  int vx[2], vw[3];
  const int nown = SKIP6 ? (j == 7 ? NKS : unit_start(j + 1)) - unit_start(j) : 6;
#pragma unroll
  for (int i = 0; i < 2; i++) vx[i] = (8 * i + (lane >> 3)) * 1024 + (((lane & 7) ^ (4 * i + (lane >> 4))) * 16);
#pragma unroll
  for (int n = 0; n < 3; n++) {
    const int sg = 16 * (3 * l + n) + (lane >> 2), r = (sg * 171) >> 10, qp = sg - 6 * r;
    const int q = qp ^ ((r >> 1) & 1);
    vw[n] = q < nown ? r * (FF * 2) + q * 64 + (((lane & 3) ^ (2 * ((r >> 3) & 1))) * 16) : 0x40000000;
    pin(vw[n]);
  }
  int vt = lane < 43 ? (blockIdx.x * 43 + lane) * 128 : 0x40000000;
  int vv = (8 * l + (lane >> 3)) * 1024 + (((((lane & 7) >> 1) ^ (((lane >> 4) & 1) | ((l & 1) << 1))) * 2 + (lane & 1)) * 16) + j * 128;
  pin(vx[0]); pin(vx[1]); pin(vt); pin(vv);
  const int sx = p * 65536 + l * 16384, sw = unit_start(j) * 64;
  const int st = (((int)blockIdx.x >> 3) * 43 + l) * 1024, sc = ((3 - (int)((size_t)a.w2 >> 7)) & 7) * 128;
  __builtin_amdgcn_s_barrier();
  TR_DECL;
  if (l == 2) dma_touch(rw, vt);
  static_for<0, NSTEP1>([&](auto S) LAM {
    constexpr int s = S, kind = ST_KIND[s], idx = st_idx(s);
    if constexpr (s % 4 == 0) TR(s / 4);
    if constexpr (s == W2PF) { if (l == 3) dma_touch(rv, vt); }
    if constexpr (kind == 0) {
      const int so = sx + ((idx + j + 1) & 7) * 128;
#pragma unroll
      for (int i = 0; i < 2; i++) dma16(rx, CE(x_addr(idx)) + l * 2048 + i * 1024, vx[i], so);
      if constexpr (idx == 0) {
        if (l == 3) dma_b2(srd(a.b2 + 64 * j, 128), lane * 4);
        if (l < 2) dma_ep(srd(a.ws + WS_FLAG + (size_t)(l ? GF_ER + 16 * p : GF_EC + 16 * j) * 8 * FSTR, 8), FLG + F_EP + 8 * l, lane * 4);
      }
    } else {
      if constexpr (idx >= 7) lds_wait<F_FREE, idx - 6>(fa);
      const int so = sw + ((((idx >> 1) + j + 1) & 7) * 64 + (idx & 1) * 32) * (FF * 2);
#pragma unroll
      for (int n = 0; n < 3; n++) {
        dma16(rw, CE(w_addr(idx)) + l * 3072 + n * 1024, vw[n], so);
      }
    }
    if constexpr (kind == 1 || idx == 4 || idx == 7) dma_flag<s + 1>(rq, FLG + F_FULL + 4 * l);
    if constexpr (kind == 1 && idx >= 5) dma_touch1(rv, min(st + (idx - 5) * 4096, 1375 * 1024) + sc);
  });
  TR(6);
  const int rot = ROTF(j);
  v4u wr[22 - ND][2];
  {
    const rsrc_t rv2 = mk_rsrc(a.w2, DM * FF * 2);
#pragma unroll
    for (int t = ND; t < 22; t++) {
#pragma unroll
      for (int hf = 0; hf < (t < 21 ? 2 : 1); hf++) wr[t - ND][hf] = __builtin_amdgcn_raw_buffer_load_b128(rv2, vv, kstep(2 * t + hf, rot) * 32768, 0);
      SB(); VMCNT(VDEPTH);
    }
  }
  TR(7);
  lds_wait<F_FREE, 16>(fa);
  lds_wait<F_CW, 2>(fa);
  TR(8);
  static_for<0, ND>([&](auto TT) LAM {
    constexpr int t = TT;
#pragma unroll
    for (int hf = 0; hf < 2; hf++) dma16(rv, L_V + t * 8192 + hf * 4096 + l * 1024, vv, kstep(2 * t + hf, rot) * 32768);
    VMCNT(VDEPTH);
  });
  dma_flag<need_V(ND - 1)>(rq, FLG + F_FULL + 4 * l);
  LAS char* wa[2] = {opq(sm + L_V + l * 1024 + lane * 16), opq(sm + L_V + VH * 8192 + l * 1024 + lane * 16)};
  lds_wait<F_FULL, need_V(ND - 1)>(fa);
  TR(9);
  static_for<ND, 22>([&](auto TT) LAM {
    constexpr int t = TT, q = t % NVB;
    if constexpr (t == NVB) TR(11);
    if constexpr (t >= NVB) lds_wait<F_FREE, 17 + t - NVB>(fa);
#pragma unroll
    for (int hf = 0; hf < (t < 21 ? 2 : 1); hf++) *(LAS v4u*)(wa[q / VH] + (q % VH) * 8192 + hf * 4096) = wr[t - ND][hf];
    lds_put(FLG + F_FULL + 4 * l, CE(need_V(t)));
  });
  TR(10);
  if (l == 0) TR_DUMP(0, 12);
}

DEV u64 tag_of(LAS char* sm, int which) {
  const v2u e = *(LAS v2u*)(sm + FLG + F_EP + 8 * which);
  const u64 v = ((u64)(unsigned)__builtin_amdgcn_readfirstlane(e.y) << 32) | (unsigned)__builtin_amdgcn_readfirstlane(e.x);
  return (v + 1) * 0x9E3779B97F4A7C15ull;
}

struct cpx { float r, i; };
DEV cpx cmul(cpx a, cpx b) { return cpx{__builtin_fmaf(a.r, b.r, -a.i * b.i), __builtin_fmaf(a.r, b.i, a.i * b.r)}; }
DEV cpx cexp_p(float zr2, float zi2, float pos) {
  const float e = __builtin_amdgcn_exp2f(zr2 * pos), th = __builtin_amdgcn_fractf(zi2 * pos);
  return cpx{e * __builtin_amdgcn_cosf(th), e * __builtin_amdgcn_sinf(th)};
}
DEV float exp_bf(float v) { return rbf(__builtin_amdgcn_exp2f(v * 1.44269504088896f)); }
DEV void pow2_scale(float mx, float& s, float& inv) {
  const int e = min(max((int)(f2u(mx) >> 23), 15), 254);
  s = u2f((unsigned)(268 - e) << 23);
  inv = u2f((unsigned)(e - 14) << 23);
}

DEV void mfma_h0(f32x4& d, v4u a, v4u b) { asm volatile("v_mfma_f32_16x16x32_f16 %0, %1, %2, 0" : "=&v"(d) : "v"(a), "v"(b)); }
DEV cpx csq(cpx a) { return cpx{__builtin_fmaf(a.r, a.r, -a.i * a.i), 2.f * a.r * a.i}; }
DEV void cmul_h(unsigned re, unsigned im, unsigned m, unsigned& ore, unsigned& oim) {
  const f16x2 r = __builtin_bit_cast(f16x2, re), i = __builtin_bit_cast(f16x2, im), mm = __builtin_bit_cast(f16x2, m);
  const f16x2 mr = {mm.x, mm.x}, mi = {mm.y, mm.y};
  ore = __builtin_bit_cast(unsigned, __builtin_elementwise_fma(r, mr, -(i * mi)));
  oim = __builtin_bit_cast(unsigned, __builtin_elementwise_fma(r, mi, i * mr));
}

struct KIn { unsigned lar, aim, cre, cim, ldt, dsk; };
typedef __attribute__((address_space(4))) const unsigned* cptr;
DEV KIn kgen_load(const Args& a, int cw, int lane, int p, int j) {
  const int d = 64 * j + 2 * p + (cw >> 1);
  KIn k;
  k.lar = ((const GAS unsigned*)(a.lar + d * NMODE))[(unsigned)lane]; k.aim = ((const GAS unsigned*)(a.aim + d * NMODE))[(unsigned)lane];
  k.cre = ((const GAS unsigned*)(a.cre + d * NMODE))[(unsigned)lane]; k.cim = ((const GAS unsigned*)(a.cim + d * NMODE))[(unsigned)lane];
  k.ldt = ((cptr)(size_t)a.ldt)[d >> 1]; k.dsk = ((cptr)(size_t)a.dsk)[d >> 1];
  return k;
}
DEV cpx conj(cpx a) { return cpx{a.r, -a.i}; }
DEV void kgen(KIn k, int cw, int lane, f32x4 (&kt)[2][2], float& kinv, v4u (&idm)[2]) {
  v4u oh, sel[2][2];
  bool oct[8];
#pragma unroll
  for (int q = 0; q < 8; q++) oct[q] = (lane >> 3) == q;
#pragma unroll
  for (int e = 0; e < 4; e++) {
    oh[e] = ((lane >> 1) & 3) == e ? 0x3c00u << (16 * (lane & 1)) : 0u;
    sel[0][0][e] = oct[0] ? oh[e] : 0u; sel[0][1][e] = oct[1] ? oh[e] : 0u;
  }
  pin(oh); pin(sel[0][0]); pin(sel[0][1]);
#pragma unroll
  for (int i = 0; i < 4; i++) kt[i >> 1][i & 1] = zero4();
  SB();
  const int sh = (cw & 1) ? 0 : 16;
  const float ldt = (cw & 2) ? bf_hi(k.ldt) : bf_lo(k.ldt), dsk = (cw & 2) ? bf_hi(k.dsk) : bf_lo(k.dsk);
  const float dt = exp_bf(ldt);
  const float zr2 = -exp_bf(bf_hi(k.lar << sh)) * dt * 1.44269504088896f, zi2 = bf_hi(k.aim << sh) * dt * 0.159154943091895f;
  const cpx E1 = cexp_p(zr2, zi2, 1.f), E2 = csq(E1);
  v4u f[2][2][4];
  {
    const cpx E8 = csq(csq(E2)), M = cexp_p(zr2, zi2, 32.f);
    cpx S[8];
    S[0] = cpx{1.f, 0.f}; S[1] = E8; S[2] = csq(E8); S[3] = cmul(S[2], E8); S[4] = M;
#pragma unroll
    for (int i = 1; i < 4; i++) S[4 + i] = cmul(S[i], M);
    const cpx M2 = csq(M), M4 = csq(M2), M6 = cmul(M2, M4);
    const unsigned mm[3] = {pk_h(M2.r, M2.i), pk_h(M4.r, M4.i), pk_h(M6.r, M6.i)};
#pragma unroll
    for (int e = 0; e < 4; e++) { f[1][0][0][e] = pk_h(S[2 * e].r, S[2 * e + 1].r); f[1][1][0][e] = pk_h(S[2 * e].i, S[2 * e + 1].i); }
#pragma unroll
    for (int q = 1; q < 4; q++)
#pragma unroll
      for (int e = 0; e < 4; e++) { unsigned re, im; cmul_h(f[1][0][0][e], f[1][1][0][e], mm[q - 1], re, im); f[1][0][q][e] = re; f[1][1][q][e] = im; }
  }
  float ks;
  {
    const float csum = wave_sum((__builtin_fabsf(bf_lo(k.cre)) + __builtin_fabsf(bf_hi(k.cre))) + (__builtin_fabsf(bf_lo(k.cim)) + __builtin_fabsf(bf_hi(k.cim))));
    pow2_scale(__builtin_fmaf(2.f * dt, csum, __builtin_fabsf(dsk)), ks, kinv);
    const float cs = 2.f * dt * ks;
    const cpx M = cexp_p(zr2, -zi2, 256.f);
    cpx S[8];
    S[0] = cpx{bf_hi(k.cre << sh) * cs, bf_hi(k.cim << sh) * -cs};
#pragma unroll
    for (int i = 1; i < 4; i++) S[i] = cmul(S[i - 1], conj(E2));
#pragma unroll
    for (int i = 0; i < 4; i++) S[4 + i] = cmul(S[i], M);
    const cpx M2 = csq(M), M4 = csq(M2), M6 = cmul(M2, M4), M8 = csq(M4);
    const unsigned m2 = pk_h(M2.r, M2.i), m4 = pk_h(M4.r, M4.i), m6 = pk_h(M6.r, M6.i), m1 = pk_h(E1.r, -E1.i), m8 = pk_h(M8.r, M8.i);
#pragma unroll
    for (int e = 0; e < 4; e++) { f[0][0][0][e] = pk_h(S[2 * e].r, S[2 * e + 1].r); f[0][1][0][e] = pk_h(S[2 * e].i, S[2 * e + 1].i); }
#pragma unroll
    for (int q = 1; q < 4; q++)
#pragma unroll
      for (int e = 0; e < 4; e++) {
        const int src = q >= 2 && (e & 1) ? e >> 1 : e;
        unsigned re = 0, im = 0;
        if (q < 3 || e < 3) cmul_h(f[0][0][0][src], f[0][1][0][src], q == 1 ? m2 : q == 2 ? ((e & 1) ? m1 : m4) : ((e & 1) ? m8 : m6), re, im);
        f[0][0][q][e] = re; f[0][1][q][e] = im;
      }
  }
#pragma unroll
  for (int q = 0; q < 16; q++) pin(f[q >> 3][(q >> 2) & 1][q & 3]);
  NOP2();
  f32x4 t[2][2][2];
  v4u o[2][2];
  static_for<0, 9>([&](auto H) LAM {
    constexpr int h = H, y = h & 1, gs = h >> 1, py = y ^ 1;
    static_for<0, 8>([&](auto I) LAM {
      constexpr int i = I;
      SB();
      if constexpr (h < 8) {
        if constexpr (i < 4) mfma_h0(t[y][i >> 1][i & 1], f[y][i & 1][2 * (i >> 1)], sel[gs & 1][0]);
        else mfma_h(t[y][(i - 4) >> 1][i & 1], f[y][i & 1][2 * ((i - 4) >> 1) + 1], sel[gs & 1][1]);
      } else if constexpr (i < 4) asm volatile("s_nop 1");
      SB();
      if constexpr (y == 1 && gs < 3 && i < 4) { sel[(gs + 1) & 1][0][i] = oct[2 * gs + 2] ? oh[i] : 0u; sel[(gs + 1) & 1][1][i] = oct[2 * gs + 3] ? oh[i] : 0u; }
      if constexpr (y == 1 && gs == 3 && i < 4) { idm[0][i] = oct[0] || oct[3] ? oh[i] : 0u; idm[1][i] = oct[4] || oct[7] ? oh[i] : 0u; }
      if constexpr (y == 1 && i == 4) { if constexpr (gs < 3) { pin(sel[(gs + 1) & 1][0]); pin(sel[(gs + 1) & 1][1]); } else { pin(idm[0]); pin(idm[1]); } }
      if constexpr (h > 0 && i >= 4) {
        const f32x4 v = t[py][(i - 4) >> 1][i & 1];
        o[py][(i - 4) >> 1][2 * (i & 1)] = pk_h(v[0], v[1]); o[py][(i - 4) >> 1][2 * (i & 1) + 1] = pk_h(v[2], v[3]);
      }
    });
    if constexpr (h > 0 && y == 0) {
      pin(o[0][0]); pin(o[0][1]); pin(o[1][0]); pin(o[1][1]);
      NOP2();
#pragma unroll
      for (int i = 0; i < 4; i++) mfma_h(kt[i >> 1][i & 1], o[0][i >> 1], o[1][i & 1]);
    }
  });
  MFMA_DONE();
  if (!(cw & 1) && lane == 0) kt[0][0][0] += dsk * ks;
  pin(kinv);
}
DEV bool fresh(v4u v, u64 tag) { return v.y == (unsigned)tag && v.w == (unsigned)(tag >> 32); }
template <int STEP> DEV void sweep4(v4u (&v)[4], rsrc_t r, int voff, int soff, u64 tag) {
#pragma nounroll
  for (int it = 0; it < POLL_CAP; it++) {
#pragma unroll
    for (int i = 0; i < 4; i++) v[i] = __builtin_amdgcn_raw_buffer_load_b128(r, voff, soff + i * STEP, AUX_SC1);
    if (!wany(!(fresh(v[0], tag) && fresh(v[1], tag) && fresh(v[2], tag) && fresh(v[3], tag)))) break;
    __builtin_amdgcn_s_sleep(SSLEEP);
    CFENCE();
  }
}

#define TA1 "s_load_dword %0, %1, 0x0\n\t" "s_load_dword %0, %1, 0x80\n\t" "s_load_dword %0, %1, 0x100\n\t" "s_load_dword %0, %1, 0x180\n\t" "s_load_dword %0, %1, 0x200\n\t" "s_load_dword %0, %1, 0x280\n\t" "s_load_dword %0, %1, 0x300\n\t" "s_load_dword %0, %1, 0x380\n\t" "s_load_dword %0, %1, 0x400\n\t" "s_load_dword %0, %1, 0x480\n\t" "s_load_dword %0, %1, 0x500\n\t"
#define TA2 "s_load_dword %0, %1, 0x0\n\t" "s_load_dword %0, %1, 0x40\n\t" "s_load_dword %0, %1, 0x80\n\t" "s_load_dword %0, %1, 0xc0\n\t" "s_load_dword %0, %1, 0x100\n\t" "s_load_dword %0, %1, 0x140\n\t" "s_load_dword %0, %1, 0x180\n\t" "s_load_dword %0, %1, 0x1c0\n\t" "s_load_dword %0, %1, 0x200\n\t" "s_load_dword %0, %1, 0x240\n\t" "s_load_dword %0, %1, 0x280\n\t" "s_load_dword %0, %1, 0x2c0\n\t" "s_load_dword %0, %1, 0x300\n\t" "s_load_dword %0, %1, 0x340\n\t" "s_load_dword %0, %1, 0x380\n\t" "s_load_dword %0, %1, 0x3c0\n\t" "s_load_dword %0, %1, 0x400\n\t" "s_load_dword %0, %1, 0x440\n\t" "s_load_dword %0, %1, 0x480\n\t" "s_load_dword %0, %1, 0x4c0\n\t" "s_load_dword %0, %1, 0x500\n\t" "s_load_dword %0, %1, 0x540\n\t"
#define TB1 "s_load_dword %0, %2, 0x0\n\t" "s_load_dword %0, %2, 0x400\n\t" "s_load_dword %0, %2, 0x800\n\t" "s_load_dword %0, %2, 0xc00\n\t" "s_load_dword %0, %2, 0x1000\n\t" "s_load_dword %0, %2, 0x1400\n\t" "s_load_dword %0, %2, 0x1800\n\t" "s_load_dword %0, %2, 0x1c00\n\t" "s_load_dword %0, %2, 0x2000\n\t" "s_load_dword %0, %2, 0x2400\n\t" "s_load_dword %0, %2, 0x2800\n\t"
#define TB2 "s_load_dword %0, %2, 0x0\n\t" "s_load_dword %0, %2, 0x40\n\t" "s_load_dword %0, %2, 0x400\n\t" "s_load_dword %0, %2, 0x440\n\t" "s_load_dword %0, %2, 0x800\n\t" "s_load_dword %0, %2, 0x840\n\t" "s_load_dword %0, %2, 0xc00\n\t" "s_load_dword %0, %2, 0xc40\n\t" "s_load_dword %0, %2, 0x1000\n\t" "s_load_dword %0, %2, 0x1040\n\t" "s_load_dword %0, %2, 0x1400\n\t" "s_load_dword %0, %2, 0x1440\n\t" "s_load_dword %0, %2, 0x1800\n\t" "s_load_dword %0, %2, 0x1840\n\t" "s_load_dword %0, %2, 0x1c00\n\t" "s_load_dword %0, %2, 0x1c40\n\t" "s_load_dword %0, %2, 0x2000\n\t" "s_load_dword %0, %2, 0x2040\n\t" "s_load_dword %0, %2, 0x2400\n\t" "s_load_dword %0, %2, 0x2440\n\t" "s_load_dword %0, %2, 0x2800\n\t" "s_load_dword %0, %2, 0x2840\n\t"
DEV void role_gemm(const Args& a, LAS char* sm, int w, int lane, int p, int j, unsigned fa) {
  const int g = lane >> 4, tok = lane & 15, q4 = tok >> 2, pp = lane & 3;
  const v4u rq = srd(kSeq, 256);
  const unsigned fre = FLG + F_FREE + 4 * w;
  volatile LAS unsigned* full = (volatile LAS unsigned*)(size_t)(fa + F_FULL);
  const rsrc_t rh = mk_rsrc(a.ws + WS_H, 32 * NKS * 4096);
  const int ks = w >> 1, ah = w & 1;
  TR_DECL; TR(0);
  u64 tagr;
  {
    const int us = unit_start(j), nown = (j == 7 ? NKS : unit_start(j + 1)) - us;
    unsigned bias[3];
#pragma unroll
    for (int i = 0; i < 3; i++) bias[i] = *(const GAS u16*)(a.b1 + us * 32 + (3 * w + i) * 16 + tok);
    LAS char* wb[2][3];
#pragma unroll
    for (int i = 0; i < 3; i++) {
      const int ft = 3 * w + i;
      wb[0][i] = opq(sm + (6 * (8 * g + q4) + ((ft >> 1) ^ ((q4 >> 1) & 1))) * 64 + (((ft & 1) ^ (g & 1)) * 32) + 8 * pp);
      wb[1][i] = opq(wb[0][i] + L_RING);
    }
    LAS char* xb[2];
#pragma unroll
    for (int s = 0; s < 2; s++) xb[s] = opq(sm + L_X + tok * 128 + (((4 * s + g) ^ ((tok >> 1) & 7)) * 16));
    int hoffv = (g & 1) * 1024 + (g >> 1) * 256 + tok * 16; pin(hoffv);
    f32x4 acc[3][4], cs[3];
#pragma unroll
    for (int i = 0; i < 3; i++) { cs[i] = zero4();
#pragma unroll
      for (int T = 0; T < 4; T++) acc[i][T] = zero4(); }
    v4u ones = {0x3f803f80u, 0x3f803f80u, 0x3f803f80u, 0x3f803f80u};
    pin(ones);
#pragma unroll
    for (int i = 0; i < 3; i++) { bias[i] = g ? 0u : bias[i] << 16; pin(bias[i]); }
    v2u wfr[2][3][2]; v4u xfr[2][4];
    auto rd = [&](auto U, auto K) LAM {
      constexpr int u = U, k = K, wa = w_addr(u), hi = wa >= L_RING;
      if constexpr (k >= 2 && k < 6) xfr[u & 1][k - 2] = *(LAS v4u*)(xb[u & 1] + (CE(x_addr(u >> 1)) - L_X) + (k - 2) * 2048);
      else { constexpr int i = k < 2 ? 0 : (k - 4) >> 1; wfr[u & 1][i][k & 1] = tr_read(wb[hi][i] + (wa - hi * L_RING) + (k & 1) * 1536); }
    };
    lds_wait<F_FULL, need_W(0)>(fa);
    TR(1);
    static_for<0, 10>([&](auto K) LAM { rd(std::integral_constant<int, 0>{}, K); });
    tagr = tag_of(sm, 1); pins(tagr);
    lds_wait<F_FULL, need_W(1)>(fa);
    dma_flag<1>(rq, fre);
    int vo[3], so[3];
#pragma unroll
    for (int i = 0; i < 3; i++) {
      const int ft = 3 * w + i;
      so[i] = (p * NKS + us + (ft >> 1)) * 4096 + (ft & 1) * 512; pins(so[i]);
    }
    LAS char* sr = opq(sm + L_STAT + tok * 16);
    static_for<0, 15>([&](auto U) LAM {
      constexpr int u = U, b = u & 1;
      if constexpr (u == 4 || u == 8 || u == 12) TR(2 + (u - 4) / 4);
      unsigned fv; bool lack = false;
      if constexpr (u < 14) fv = *full;
      static_for<0, 15>([&](auto M) LAM {
        constexpr int m = M, i = m / 5, T = m % 5;
        const v4u wv = {wfr[b][i][0].x, wfr[b][i][0].y, wfr[b][i][1].x, wfr[b][i][1].y};
        SB();
        if constexpr (T < 4) mfma_bf(acc[i][T], wv, xfr[b][T]); else mfma_bf(cs[i], ones, wv);
        SB();
        if constexpr (u < 15 && m < 10) rd(std::integral_constant<int, u + 1>{}, M);
        if constexpr (u < 14 && m == 11) lack = wany(fv < CE(need_W(u + 2)));
        if constexpr (u < 14 && m == 13) __builtin_amdgcn_s_waitcnt(0xc07f);
        if constexpr (u + 2 <= 9 && m == 13) dma_flag<u + 2>(rq, fre);
        if constexpr (u + 2 == FREE_C && m == 10) { VMCNT(0); lds_put(fre, u + 2); }
        if constexpr (u == 14 && m == 10) lds_put(fre, 16);
        if constexpr (u == 14 && m == 11) fv = full[F_ST / 4];
        if constexpr (u == 14 && m == 14) { __builtin_amdgcn_s_waitcnt(0xc07f); lack = wany(fv < 1); }
      });
      SB();
      if constexpr (u < 14) { if (__builtin_expect(lack, 0)) lds_wait<F_FULL, need_W(u + 2)>(fa); CFENCE(); }
      else { if (__builtin_expect(lack, 0)) lds_wait<F_ST, 1>(fa); CFENCE(); }
    });
    TR(5);
    v2u sb[4], av[3];
    float cr[3][3];
    auto wv = [&](int i) LAM { return v4u{wfr[1][i][0].x, wfr[1][i][0].y, wfr[1][i][1].x, wfr[1][i][1].y}; };
    auto epi = [&](int i, int T) LAM {
      unsigned pk[2][2];
#pragma unroll
      for (int t = 0; t < 2; t++)
#pragma unroll
        for (int r = 0; r < 2; r++) {
          const float t0 = acc[i][T + t][2 * r], t1 = acc[i][T + t][2 * r + 1];
          pk[t][r] = relu_pk(pk_bf(t0 * __builtin_fabsf(t0), t1 * __builtin_fabsf(t1)));
        }
      auto r0 = __builtin_amdgcn_permlane16_swap(pk[0][0], pk[1][0], false, false);
      auto r1 = __builtin_amdgcn_permlane16_swap(pk[0][1], pk[1][1], false, false);
      st_sc1(v4u{r0[0], r1[0], r0[1], r1[1]}, rh, vo[i] + T * 1024, so[i]);
    };
    static_for<0, 27>([&](auto M) LAM {
      constexpr int m = M;
      SB();
      if constexpr (m < 3) mfma_bf(cs[m], ones, wv(m));
      else if constexpr (m < 15) mfma_bf(acc[(m - 3) >> 2][(m - 3) & 3], wv((m - 3) >> 2), xfr[1][(m - 3) & 3]);
      else mfma_bf16k(acc[(m - 15) >> 2][(m - 15) & 3], av[(m - 15) >> 2], sb[(m - 15) & 3]);
      SB();
      if constexpr (m < 4) sb[m] = *(LAS v2u*)(sr + m * 256);
      if constexpr (m < 3) { vo[m] = hoffv | (((3 * w + m) >> 1) < nown ? 0 : 0x40000000); pin(vo[m]); }
      if constexpr (m >= 5 && m < 14) {
        constexpr int i = (m - 5) / 3, k = (m - 5) % 3;
        if constexpr (k == 0) { pin(cs[i]); cr[i][0] = g ? 0.f : cs[i][0]; cr[i][1] = rbf(cr[i][0]); pin(cr[i][0]); pin(cr[i][1]); }
        if constexpr (k == 1) { cr[i][1] = cr[i][0] - cr[i][1]; cr[i][2] = cr[i][1] - rbf(cr[i][1]); pin(cr[i][1]); pin(cr[i][2]); }
        if constexpr (k == 2) { av[i] = v2u{pk_bf(cr[i][0], cr[i][1]), pk_bf(cr[i][2], u2f(bias[i]))}; pin(av[i]); }
      }
    });
    SB();
#pragma unroll
    for (int i = 0; i < 6; i++) { epi(i >> 1, 2 * (i & 1)); SB(); }
  }
  lds_put(FLG + F_GX + 4 * w, 1);
  TR(6);
  GAS u64* pf = gflag(a, GF_H + p * 32 + (lane & 31));
  LAS char* wb[2][4];
  const int phi = ((q4 >> 1) & 1) | ((g & 1) << 1);
#pragma unroll
  for (int e = 0; e < 4; e++) { wb[0][e] = opq(sm + L_V + ks * 4096 + (8 * g + q4) * 128 + ((e ^ phi) * 32) + 8 * pp); wb[1][e] = opq(wb[0][e] + VH * 8192); }
  f32x4 acc[4][2];
#pragma unroll
  for (int e = 0; e < 4; e++) { acc[e][0] = zero4(); acc[e][1] = zero4(); }
  v2u wfr[2][4][2];
  auto rd2 = [&](auto I, auto K) LAM {
    constexpr int i = I, k = K, t = i % NVB;
    wfr[i & 1][k >> 1][k & 1] = tr_read(wb[t / VH][k >> 1] + (t % VH) * 8192 + (k & 1) * 512);
  };
  const int T = 2 * ah + ks;
  float rest[4][4];
#pragma unroll
  for (int e = 0; e < 4; e++) {
    const v2u b2 = *(LAS v2u*)(sm + L_B2 + 32 * e + 8 * g);
    const v2u xv = *(LAS v2u*)(sm + L_X + (16 * T + tok) * 128 + (((2 * e + (g >> 1)) ^ ((tok >> 1) & 7)) * 16) + (g & 1) * 8);
    rest[e][0] = bf_lo(b2.x) + bf_lo(xv.x); rest[e][1] = bf_hi(b2.x) + bf_hi(xv.x); rest[e][2] = bf_lo(b2.y) + bf_lo(xv.y); rest[e][3] = bf_hi(b2.y) + bf_hi(xv.y);
#pragma unroll
    for (int r = 0; r < 4; r++) pin(rest[e][r]);
  }
  float rs2 = *(LAS float*)(sm + L_STAT + (16 * T + tok) * 16 + 8);
  int hv0 = lane * 16 + ks * 1024, hv1 = hv0 ^ 1024;
  volatile LAS unsigned* frw = (volatile LAS unsigned*)opq(sm + (lane ? L_JUNK + lane * 4 : (int)fre));
  GAS u64* fh = gflag(a, GF_H + (p * 8 + j) * 4 + w);
  int hso = p * NKS * 4096 + ah * 2048, rot = ROTF(j) + ks;
  pin(rs2); pin(hv0); pin(hv1); pin(pf); pins(fh); pins(hso); pins(rot);
  SB();
  publish(fh, tagr, lane);
  TR(7);
  if (w == 0) {
    __builtin_amdgcn_s_sleep(HDELAY);
    poll(pf, tagr);
    lds_put(FLG + F_HOK, 1);
  } else lds_wait<0, 1>((unsigned)(FLG + F_HOK));
  TR(8);
  v4u hf[PD][2];
#pragma unroll
  for (int i = 0; i < PD - 1; i++) {
    hf[i][0] = __builtin_amdgcn_raw_buffer_load_b128(rh, hv0, hso + kstep(2 * i, rot) * 4096, HAUX);
    hf[i][1] = __builtin_amdgcn_raw_buffer_load_b128(rh, hv1, hso + kstep(2 * i, rot) * 4096, HAUX);
  }
  lds_wait<F_FULL, need_V(ND - 1)>(fa);
  static_for<0, 8>([&](auto K) LAM { rd2(std::integral_constant<int, 0>{}, K); });
  static_for<0, 22>([&](auto I) LAM {
    constexpr int i = I, b = i & 1, nx = i + PD - 1;
    if constexpr (i == 1 || i == 8 || i == 15) TR(9 + (i - 1) / 7);
    if (i < 21 || ks == 0) {
      constexpr bool chk = !(i & 1) && i + 3 >= ND && i + 2 < 22;
      constexpr int want = need_V(i + 3 < 22 ? i + 3 : 21), out = 2 * ((nx < 22 ? nx : 22) - 1 - i);
      unsigned fv; bool lack = false;
      SB();
      __builtin_amdgcn_s_waitcnt((out & 15) | ((out >> 4) << 14) | 0x70);
      static_for<0, 8>([&](auto M) LAM {
        constexpr int m = M, e = m >> 1;
        const v4u wv = {wfr[b][e][0].x, wfr[b][e][0].y, wfr[b][e][1].x, wfr[b][e][1].y};
        SB();
        mfma_bf(acc[e][m & 1], wv, hf[i % PD][m & 1]);
        SB();
        if constexpr (chk && m == 0) fv = *full;
        if constexpr (i < 21 && m < 4) { rd2(std::integral_constant<int, i + 1>{}, std::integral_constant<int, 2 * m>{}); rd2(std::integral_constant<int, i + 1>{}, std::integral_constant<int, 2 * m + 1>{}); }
        if constexpr (nx < 22 && m == 4) hf[nx % PD][0] = __builtin_amdgcn_raw_buffer_load_b128(rh, hv0, hso + kstep(2 * nx, rot) * 4096, HAUX);
        if constexpr (nx < 22 && m == 5) hf[nx % PD][1] = __builtin_amdgcn_raw_buffer_load_b128(rh, hv1, hso + kstep(2 * nx, rot) * 4096, HAUX);
        if constexpr (m == 6 && (i & 1) && (i + NVB < 23 || i == YGATE)) { CFENCE(); *frw = 18 + i; }
        if constexpr (chk && m == 6) lack = wany(fv < want);
      });
      SB();
      if constexpr (chk) { if (__builtin_expect(lack, 0)) lds_wait<F_FULL, want>(fa); CFENCE(); }
    }
  });
  LAS char* exw = opq(sm + L_EX + w * 4096 + lane * 16);
  LAS char* rl = sm + L_EX + (w ^ 2) * 4096;
  LAS char* exr = opq(rl + lane * 16);
  LAS char* ya = opq(sm + L_Y + (4 * g + q4) * YROW + T * 32 + 8 * pp);
  LAS char* rlw = opq(rl + tok * 144 + g * 8);
  LAS char* rlr = opq(rl + (lane >> 3) * 144 + (lane & 7) * 16);
  int ov = (lane >> 3) * 1024 + (lane & 7) * 16; pin(ov);
  MFMA_DONE();
  TR(12);
  lds_wait<F_CW, 5>(fa);
#pragma unroll
  for (int e = 0; e < 4; e++) *(LAS f32x4*)(exw + e * 1024) = acc[e][1];
  lds_put(FLG + F_GX + 4 * w, 2);
#pragma unroll
  for (int e = 0; e < 4; e++)
#pragma unroll
    for (int r = 0; r < 4; r++) { rest[e][r] = __builtin_fmaf(acc[e][0][r], rs2, rest[e][r]); pin(rest[e][r]); }
  lds_wait<0, 2>(FLG + F_GX + 4 * (w ^ 2));
  f32x4 po[4];
#pragma unroll
  for (int e = 0; e < 4; e++) po[e] = *(LAS f32x4*)(exr + e * 1024);
#pragma unroll
  for (int e = 0; e < 4; e++)
#pragma unroll
    for (int r = 0; r < 4; r++) { rest[e][r] = __builtin_fmaf(po[e][r], rs2, rest[e][r]); pin(rest[e][r]); }
  TR(13);
  lds_wait<F_CW, 6>(fa);
  TR(15);
  v2u yv[4];
#pragma unroll
  for (int e = 0; e < 4; e++) yv[e] = tr_read(ya + 16 * e * YROW);
#pragma unroll
  for (int e = 0; e < 4; e++)
    *(LAS v2u*)(rlw + e * 32) = v2u{pk_bf(rest[e][0] + bf_lo(yv[e].x), rest[e][1] + bf_hi(yv[e].x)), pk_bf(rest[e][2] + bf_lo(yv[e].y), rest[e][3] + bf_hi(yv[e].y))};
  CFENCE();
  {
    const rsrc_t ro = mk_rsrc(a.out, SEQ * DM * 2);
    const v4u o0 = *(LAS v4u*)(rlr), o1 = *(LAS v4u*)(rlr + 8 * 144);
    st_sc1(o0, ro, ov, (64 * p + 16 * T) * 1024 + j * 128);
    st_sc1(o1, ro, ov, (64 * p + 16 * T + 8) * 1024 + j * 128);
  }
  if (w == 0 && lane < 2 && (lane ? j == 0 : p == 0)) {
    const u64 e1 = *(LAS u64*)(sm + FLG + F_EP + lane * 8) + 1;
    __hip_atomic_store(gflag(a, lane ? GF_ER + 16 * p : GF_EC + 16 * j), e1, __ATOMIC_RELAXED, __HIP_MEMORY_SCOPE_AGENT);
  }
  TR(14);
  if (w == DW) TR_DUMP(16, 16);
}

DEV void role_conv(const Args& a, LAS char* sm, int cw, int lane, int p, int j, unsigned fa, const KIn& kin) {
  const int g = lane >> 4, tok = lane & 15;
  const int ch = cw >> 1, mh = cw & 1, d = 64 * j + 2 * p + ch;
  const unsigned fme = FLG + F_CW + 4 * cw, fpa = FLG + F_CW + 4 * (cw ^ 1);
  TR_DECL; TR(0);
  __builtin_amdgcn_s_setprio(KCPRIO);
  v4u idm[2];
  f32x4 dm;
#pragma unroll
  for (int r = 0; r < 4; r++) dm[r] = 4 * g + r == tok ? 1.f / DM : 0.f;
  int uoff = (8 * (g & 1) + (tok >> 1)) * 8192 + (g >> 1) * 32 + (tok & 1) * 16;
  LAS char* xs[2];
#pragma unroll
  for (int s = 0; s < 2; s++) xs[s] = opq(sm + L_X + (16 * cw + tok) * 128 + (((4 * s + g) ^ ((tok >> 1) & 7)) * 16));
  pin(dm); pin(uoff);
  const rsrc_t ru = mk_rsrc(a.ws + WS_U, SEQ * DM * 2);
  int uso = j * 262144 + p * 256 + cw * 64; pins(uso);
  f32x4 kt[2][2]; float kinv;
  kgen(kin, cw, lane, kt, kinv, idm);
  TR(1);
  u64 tagc;
  v4u stat;
  {
    v4u xl[2];
    f32x4 sa = zero4(), gr = zero4();
    v4u ones = {0x3f803f80u, 0x3f803f80u, 0x3f803f80u, 0x3f803f80u};
    pin(ones);
    __builtin_amdgcn_s_setprio(3);
    static_for<0, 2>([&](auto B) LAM {
      constexpr int c0 = B == 0 ? 0 : 5, c1 = B == 0 ? 5 : 8;
      lds_wait<F_FULL, need_X(c1 - 1)>(fa);
      if constexpr (B == 1) TR(4);
      v4u f[2 * (c1 - c0)];
#pragma unroll
      for (int c = c0; c < c1; c++) { f[2 * (c - c0)] = *(LAS v4u*)(xs[0] + ((c + 1) & 7) * 8192); f[2 * (c - c0) + 1] = *(LAS v4u*)(xs[1] + ((c + 1) & 7) * 8192); }
      SB();
#pragma unroll
      for (int i = 0; i < 2 * (c1 - c0); i++) { mfma_bf(gr, f[i], f[i]); mfma_bf(sa, ones, f[i]); }
            if constexpr (B == 1) { xl[0] = f[4]; xl[1] = f[5]; }
    });
    __builtin_amdgcn_s_setprio(SPRIO);
    tagc = tag_of(sm, 0); pins(tagc);
    float xf[2][8];
#pragma unroll
    for (int s = 0; s < 2; s++)
#pragma unroll
      for (int e = 0; e < 4; e++) { const unsigned uu = xl[s][e]; xf[s][2 * e] = bf_lo(uu); xf[s][2 * e + 1] = bf_hi(uu); pin(xf[s][2 * e]); pin(xf[s][2 * e + 1]); }
    pin(sa); pin(gr);
    SB();
    const float mean = sa[0] * (1.f / DM);
    const float ex2 = xsum32(xsum16(__builtin_fmaf(gr[0], dm[0], gr[1] * dm[1]) + __builtin_fmaf(gr[2], dm[2], gr[3] * dm[3])));
    float var = __builtin_fmaf(-mean, mean, ex2);
    if (__builtin_expect(wany(!(var >= ex2 * (1.f / 1024.f))), 0)) {
      float q = 0;
#pragma nounroll
      for (int c = 0; c < 16; c++) {
        const v4u u = *(LAS v4u*)((c & 1 ? xs[1] : xs[0]) + (c >> 1) * 8192);
#pragma unroll
        for (int e = 0; e < 4; e++) { const unsigned uu = u[e]; const float d0 = bf_lo(uu) - mean, d1 = bf_hi(uu) - mean; q = __builtin_fmaf(d0, d0, __builtin_fmaf(d1, d1, q)); }
      }
      var = xsum32(xsum16(q)) * (1.f / DM);
    }
    const float m = rbf(mean), sd = rbf(__builtin_amdgcn_sqrtf(rbf(rbf(var) + 1e-5f))), rs = __builtin_amdgcn_rcpf(sd);
    const float c0 = -m * rs;
    v4u xn[2]; f32x4 t[4];
#pragma unroll
    for (int s = 0; s < 2; s++) {
#pragma unroll
      for (int e = 0; e < 4; e++) xn[s][e] = pk_h(__builtin_fmaf(xf[s][2 * e], rs, c0), __builtin_fmaf(xf[s][2 * e + 1], rs, c0));
      pin(xn[s]);
      NOP2();
      mfma_h0(t[2 * s], xn[s], idm[0]); mfma_h0(t[2 * s + 1], xn[s], idm[1]);
      SB();
    }
#pragma unroll
    for (int s = 0; s < 2; s++) {
      auto r0 = __builtin_amdgcn_permlane16_swap(pk_h(t[2 * s][0], t[2 * s][1]), pk_h(t[2 * s + 1][0], t[2 * s + 1][1]), false, false);
      auto r1 = __builtin_amdgcn_permlane16_swap(pk_h(t[2 * s][2], t[2 * s][3]), pk_h(t[2 * s + 1][2], t[2 * s + 1][3]), false, false);
      st_sc1(v4u{r0[0], r1[0], r0[1], r1[1]}, ru, uoff, uso + s * 131072);
    }
    stat = v4u{pk_bf(-m, -m), pk_bf(-m, sd), f2u(rs * rs), 0u};
    pin(stat);
  }
  LAS char* sw = opq(sm + L_STAT + (16 * cw + tok) * 16);
  TR(5);
  __builtin_amdgcn_s_setprio(0);
  LAS char* tw = opq(sm + L_TAB + cw * TABB + g * 512 + tok * 16);
  LAS char* tb = sm + L_TAB + ch * 2 * TABB;
  const int nh = 127 - lane, nl = 63 - lane;
  auto odd = [&](int q) LAM { return 2056 + (q >> 3) * 16 + ((q >> 2) & 1) * 512 + (q & 2) * 2; };
  LAS char* e0 = opq(tb + (mh ? 8 * nh : 32 * nh));
  LAS char* e1 = opq(tb + (mh ? 8 * nl : 16 * nh));
  LAS char* e2 = opq(tb + (lane == 63 ? 2056 + 31 * 16 + 516 : 8 * nh));
  LAS char* e3 = opq(tb + odd(4 * nl + 3));
  LAS char* e4 = opq(tb + odd(4 * nl + 1));
  LAS char* e5 = opq(tb + odd(max(4 * nl - 1, 1)));
  LAS char* cr = sm + ch * CH;
  LAS char* kc0 = opq(cr + (mh ? 3072 + 8 * lane : 32 * lane));
  LAS char* kc1 = opq(cr + (mh ? 3584 + 8 * lane : 2048 + 16 * lane));
  LAS char* uw = opq(sm + (lane & 1) * CH + C_U + 480 + cw * 1024 + (lane >> 1) * 16);
  LAS char* zw = opq(cr + (lane < 30 ? C_U + lane * 16 : ((lane >> 1) & 1) * KCOPY + 4096 + (lane & 1) * 16));
  LAS char* ab[4];
#pragma unroll
  for (int i = 0; i < 4; i++) ab[i] = opq(cr + ((15 - tok) & 1) * KCOPY + 2 * (((15 - tok) & ~1) + 8 * g) + 64 * (4 - 4 * mh) + 1024 * i);
  LAS char* bb = opq(cr + C_U + 448 + 32 * tok + 16 * g - 64 * (4 * mh + 3));
  f32x4 acc[8];
#pragma unroll
  for (int c = 0; c < 8; c++) acc[c] = zero4();
  SB();
  publish(gflag(a, GF_U + (j * 32 + p) * 4 + cw), tagc, lane);
  TR(2);
  lds_wait<F_FULL, need_W(UWAIT)>(fa);
  TR(3);
  v4u uv[4];
  poll(gflag(a, GF_U + (j * 32 + 8 * cw) * 4 + (lane & 31)), tagc);
#pragma unroll
  for (int i = 0; i < 2; i++) uv[i] = __builtin_amdgcn_raw_buffer_load_b128(ru, lane * 16 + i * 1024, (j * 32 + p) * 8192 + cw * 2048, AUX_SC1);
  TR(7);
  __builtin_amdgcn_s_setprio(IPRIO);
  lds_wait<F_FULL, need_W(15)>(fa);
  if (g == 0) *(LAS v4u*)sw = stat;
  lds_put(FLG + F_ST + 4 * cw, 1);
  lds_wait<F_FREE, FREE_C>(fa);
  TR(8);
#pragma unroll
  for (int i = 0; i < 4; i++) *(LAS f32x4*)(tw + (i >> 1) * 2048 + (i & 1) * 256) = kt[i >> 1][i & 1];
  if (lane == 32) *(LAS float*)(tw + 4096 - 1024) = kt[1][0][2];
  lds_put(fme, 1);
#pragma unroll
  for (int i = 0; i < 2; i++) *(LAS v4u*)(uw + i * 512) = uv[i];
  if (mh == 1 && lane < 34) *(LAS v4u*)zw = v4u{0, 0, 0, 0};
  lds_wait<0, 1>(fpa);
  TR(15);
  auto tv = [&](LAS char* q) LAM { return *(LAS float*)q + *(LAS float*)(q + TABB); };
  if (mh == 0) {
    const float A = tv(opq(e0 + 32)), B = tv(opq(e0 + 16)), C = tv(e0), A4 = tv(opq(e1 + 16)), B4 = tv(opq(e1 + 8)), C4 = tv(e1);
    const unsigned aa = pk_h(A, A), bb2 = pk_h(B, B), cc = pk_h(C, C), b4 = pk_h(B4, B4), c4 = pk_h(C4, C4);
    *(LAS v4u*)(kc0) = v4u{aa, aa, bb2, bb2}; *(LAS v4u*)(kc0 + 16) = v4u{bb2, pk_h(B, C), cc, cc};
    *(LAS v4u*)(kc0 + KCOPY) = v4u{aa, pk_h(A, B), bb2, bb2}; *(LAS v4u*)(kc0 + KCOPY + 16) = v4u{bb2, cc, cc, cc};
    *(LAS v4u*)(kc1) = v4u{pk_h(A4, A4), b4, pk_h(B4, C4), c4}; *(LAS v4u*)(kc1 + KCOPY) = v4u{pk_h(A4, B4), b4, c4, c4};
  } else {
    const float A = tv(opq(e0 + 8)), B = tv(opq(e0 + 4)), C = tv(e0), Dd = tv(e2);
    const float g3 = tv(e3), g2 = tv(opq(e1 + 4)), g1 = tv(e4), g0 = tv(e1), gm = lane == 63 ? 0.f : tv(e5);
    *(LAS v2u*)(kc0) = v2u{pk_h(A, B), pk_h(C, C)}; *(LAS v2u*)(kc0 + KCOPY) = v2u{pk_h(B, C), pk_h(C, Dd)};
    *(LAS v2u*)(kc1) = v2u{pk_h(g3, g2), pk_h(g1, g0)}; *(LAS v2u*)(kc1 + KCOPY) = v2u{pk_h(g2, g1), pk_h(g0, gm)};
  }
  lds_put(fme, 2);
  auto lda = [&](int i, int r, int k) LAM { return ld2(ab[(7 - i) >> 1] + 64 * (59 - 8 * i - r) - 1024 * ((7 - i) >> 1) + 8 * k); };
  lds_wait<F_CW, 2>(fa);
  TR(6);
  lds_wait<0, 1>(FLG + F_GX + 4 * cw);
  __builtin_amdgcn_s_setprio(CPRIO);
  TR(9);
  v2u A[8][2]; v4u B[2][8];
#pragma unroll
  for (int i = 0; i < 8; i++) {
    B[0][i] = *(LAS v4u*)(bb + 64 * (8 * i + 3));
    A[7 - i][0] = lda(7 - i, 0, 0); A[7 - i][1] = lda(7 - i, 0, 1);
  }
  static_for<0, 4>([&](auto R) LAM {
    constexpr int r = R;
    static_for<0, 8>([&](auto II) LAM {
      constexpr int i = 7 - II;
      static_for<0, 8 - i>([&](auto K) LAM {
        constexpr int k = K;
        SB();
        mfma_h(acc[i + k], v4u{A[i][0].x, A[i][0].y, A[i][1].x, A[i][1].y}, B[r & 1][k]);
        SB();
        if constexpr (r < 3) {
          if constexpr (i < 7 && k < 2) A[i + 1][k] = lda(i + 1, r + 1, k);
          if constexpr (i <= 4 && i >= 1 && (k == 2 || k == 3)) B[(r + 1) & 1][2 * (4 - i) + k - 2] = *(LAS v4u*)(bb + 64 * (8 * (2 * (4 - i) + k - 2) + 3 - (r + 1)));
        }
        if constexpr (r == 3 && i == 4 && k == 0) lds_put(fme, 3);
      });
    });
    if constexpr (r < 3) { A[0][0] = lda(0, r + 1, 0); A[0][1] = lda(0, r + 1, 1); }
  });
  LAS char* exm = opq(cr + mh * 4096 + lane * 16);
  LAS char* exo = opq(cr + (mh ^ 1) * 4096 + lane * 16);
  int yoff = (tok >> 2) * 256 + g * 64 + (tok & 3) * 16; pin(yoff);
  const rsrc_t ry = mk_rsrc(a.ws + WS_Y, SEQ * DM * 4);
  unsigned tlo = (unsigned)tagc, thi = (unsigned)(tagc >> 32); pin(tlo); pin(thi);
  MFMA_DONE();
  TR(10);
  lds_wait<0, 3>(fpa);
  auto finish = [&](auto MH) LAM {
    constexpr int own = 4 * MH;
#pragma unroll
    for (int c = 0; c < 4; c++) *(LAS f32x4*)(exm + c * 1024) = acc[(own ^ 4) + c];
    lds_put(fme, 4);
#pragma unroll
    for (int c = 0; c < 4; c++) { acc[own + c] *= kinv; pin(acc[own + c]); }
    lds_wait<0, 4>(fpa);
    f32x4 o[4];
#pragma unroll
    for (int c = 0; c < 4; c++) o[c] = *(LAS f32x4*)(exo + c * 1024);
#pragma unroll
    for (int c = 0; c < 4; c++)
      st_sc1(v4u{pk_bf(__builtin_fmaf(o[c][0], kinv, acc[own + c][0]), __builtin_fmaf(o[c][1], kinv, acc[own + c][1])), tlo,
                 pk_bf(__builtin_fmaf(o[c][2], kinv, acc[own + c][2]), __builtin_fmaf(o[c][3], kinv, acc[own + c][3])), thi}, ry, yoff + c * 1024, d * 8192 + MH * 4096);
  };
  if (mh) finish(std::integral_constant<int, 1>{}); else finish(std::integral_constant<int, 0>{});
  lds_put(fme, 5);
  TR(11);
  LAS char* yw = opq(sm + L_Y + (16 * cw + g) * YROW + (lane & 3) * 32 + ((lane >> 2) & 3) * 8);
  lds_wait<F_CW, 5>(fa);
  lds_wait<F_FREE, 17 + YGATE>(fa);
  TR(12);
  sweep4<32768>(uv, ry, g * 8192 + tok * 16, (64 * j + 16 * cw) * 8192 + p * 256, tagc);
  TR(13);
#pragma unroll
  for (int i = 0; i < 4; i++) *(LAS v2u*)(yw + 4 * i * YROW) = v2u{uv[i].x, uv[i].z};
  lds_put(fme, 6);
  TR(14);
  if (cw == 0) TR_DUMP(48, 16);
}

__global__ __launch_bounds__(NTHR, 3) NOPK void k_fused(Args a) {
  __shared__ __attribute__((aligned(16))) char smem[LDS_BYTES];
  LAS char* sm = (LAS char*)smem;
  const int lane = threadIdx.x & 63, w = __builtin_amdgcn_readfirstlane(threadIdx.x >> 6);
  const int b = blockIdx.x, p = (b & 7) * 4 + (b >> 6), j = (b >> 3) & 7;
  if (w == 11) {
    u64 pc; asm volatile("s_getpc_b64 %0" : "=s"(pc));
    dma_touch(srd((const void*)pc, CODE_BYTES), lane < 8 ? ((b >> 3) * 8 + lane) * 128 : 0x40000000);
  }
  unsigned fa = FLG + 4 * (lane & 3);
  pin(fa);
  if (w >= 8) {
    if (w == 8) { ((LAS unsigned*)(sm + FLG))[lane] = 0; LGKM0(); }
    __builtin_amdgcn_s_setprio(LPRIO); role_loader(a, sm, w - 8, lane, p, j, fa);
  } else {
    if (w < 4) { __builtin_amdgcn_s_barrier(); __builtin_amdgcn_s_setprio(2); role_gemm(a, sm, w, lane, p, j, fa); }
    else {
      const KIn kin = kgen_load(a, w - 4, lane, p, j);
      SB();
      __builtin_amdgcn_s_barrier();
      role_conv(a, sm, w - 4, lane, p, j, fa, kin);
    }
  }
}

extern "C" void kernel_launch(void* const* d_in, const int* in_sizes, int n_in, void* d_out, int out_size, void* d_ws, size_t ws_size, hipStream_t stream) {
  Args a;
  a.x = (const u16*)d_in[0]; a.lar = (const u16*)d_in[1]; a.aim = (const u16*)d_in[2]; a.cre = (const u16*)d_in[3]; a.cim = (const u16*)d_in[4];
  a.ldt = (const u16*)d_in[5]; a.dsk = (const u16*)d_in[6]; a.w1 = (const u16*)d_in[8]; a.b1 = (const u16*)d_in[9]; a.w2 = (const u16*)d_in[10];
  a.b2 = (const u16*)d_in[11]; a.out = (u16*)d_out; a.ws = (char*)d_ws;
  k_fused<<<256, NTHR, 0, stream>>>(a);
}
```

```cpp
#include <hip/hip_runtime.h>
#include <stdint.h>
#include <type_traits>

#define LAS __attribute__((address_space(3)))
#define GAS __attribute__((address_space(1)))
#if defined(__HIP_DEVICE_COMPILE__) && __HIP_DEVICE_COMPILE__
#define NOPK __attribute__((target("no-packed-fp32-ops")))
#else
#define NOPK
#endif
#define DEV __device__ __forceinline__ NOPK
#define LAM __attribute__((always_inline)) NOPK
typedef unsigned short u16;
typedef unsigned long long u64;
typedef unsigned v4u __attribute__((ext_vector_type(4)));
typedef unsigned v2u __attribute__((ext_vector_type(2)));
typedef unsigned v2u4 __attribute__((ext_vector_type(2), aligned(4)));
typedef __bf16 bf16x8 __attribute__((ext_vector_type(8)));
typedef _Float16 f16x8 __attribute__((ext_vector_type(8)));
typedef float f32x4 __attribute__((ext_vector_type(4)));
typedef short s16x4 __attribute__((ext_vector_type(4)));
typedef short s16x2 __attribute__((ext_vector_type(2)));
typedef __amdgpu_buffer_rsrc_t rsrc_t;

constexpr int SEQ = 2048, DM = 512, NMODE = 128, FF = 1376, NKS = 43;
constexpr size_t WS_H = 0, WS_U = 6u << 20, WS_Y = 10u << 20, WS_FLAG = 14u << 20, WS_DIAG = 16u << 20;
constexpr int GF_U = 0, GF_H = 1024, GF_Y = 2048, GF_EC = 3072, GF_ER = 3200;
#define FSTR 16
#define PSLEEP 2
#define UWAIT 9
#define YDELAY 8
#define CPRIO 3
#define TPRIO 3
#define PUBAT 1
#define W2PF 12
#define LPRIO 1
#define KCPRIO 3
#define SSLEEP 4
#define IPRIO 1
#define ND 6
#define SPRIO 3
#define YGATE 11
#define ROT 0
#define SKIP6 1
#define ROTF(j) (ROT == 1 ? unit_start(j) : ROT == 2 ? ((j) & 1) * 22 : ROT == 3 ? ((j) & 3) * 11 : 0)
#define HAUX AUX_SC1
#define DW 0
#define PREP 6
#define TFIRST 1
#define NPRE 11
#define VFIRST 9
#define PUTGATE 1
#define TDELAY 4
#define W1DELAY 6
#define VD1 12
#define XPOL ""
#define PPOL ""
#define EPRIO 0
#define PF_W
#define PF_CODE
#define HDELAY 14
constexpr int NTHR = 768;
constexpr int POLL_CAP = 1 << 14;
constexpr int AUX_SC1 = 16;

struct Args {
  const u16 *x, *w1, *lar, *aim, *cre, *cim, *ldt, *dsk, *b1, *w2, *b2;
  u16* out;
  char* ws;
};

#define TR_DECL
#define TR(i)
#define TR_DUMP(base, n)

template <int I, int N, class Fn> DEV void static_for(Fn&& f) {
  if constexpr (I < N) { f(std::integral_constant<int, I>{}); static_for<I + 1, N>(f); }
}
#define CE(x) (std::integral_constant<int, (x)>::value)
template <class T> DEV void pin(T& v) { asm volatile("" : "+v"(v)); }
template <class T> DEV void pins(T& v) { asm volatile("" : "+s"(v)); }
DEV float u2f(unsigned u) { return __builtin_bit_cast(float, u); }
DEV unsigned f2u(float f) { return __builtin_bit_cast(unsigned, f); }
DEV bool wany(bool c) { return __builtin_amdgcn_ballot_w64(c) != 0; }
DEV rsrc_t mk_rsrc(const void* p, int bytes) { return __builtin_amdgcn_make_buffer_rsrc((void*)p, 0, bytes, 0x00020000); }
DEV float bf_lo(unsigned u) { return u2f(u << 16); }
DEV float bf_hi(unsigned u) { return u2f(u & 0xffff0000u); }
typedef float f32x2 __attribute__((ext_vector_type(2)));
typedef __bf16 bf16x2 __attribute__((ext_vector_type(2)));
typedef _Float16 f16x2 __attribute__((ext_vector_type(2)));
DEV unsigned pk_bf(float a, float b) { return __builtin_bit_cast(unsigned, __builtin_convertvector(f32x2{a, b}, bf16x2)); }
DEV unsigned pk_h(float a, float b) { return __builtin_bit_cast(unsigned, __builtin_convertvector(f32x2{a, b}, f16x2)); }
DEV unsigned pk_hn(float a, float b) { return pk_h(a, -b); }
DEV float rbf(float a) { return u2f(pk_bf(0.f, a)); }
DEV unsigned relu_pk(unsigned v) { return __builtin_bit_cast(unsigned, __builtin_elementwise_max(__builtin_bit_cast(s16x2, v), s16x2{0, 0})); }
DEV v2u tr_read(LAS char* p) { return __builtin_bit_cast(v2u, __builtin_amdgcn_ds_read_tr16_b64_v4i16((LAS s16x4*)p)); }
DEV v2u ld2(LAS char* p) { return *(LAS v2u4*)p; }
DEV float xsum16(float v) { auto r = __builtin_amdgcn_permlane16_swap(f2u(v), f2u(v), false, false); return u2f(r[0]) + u2f(r[1]); }
DEV float xsum32(float v) { auto r = __builtin_amdgcn_permlane32_swap(f2u(v), f2u(v), false, false); return u2f(r[0]) + u2f(r[1]); }
template <int CTRL> DEV float dpp_add(float v) { return v + u2f((unsigned)__builtin_amdgcn_update_dpp(0, (int)f2u(v), CTRL, 0xf, 0xf, false)); }
DEV float wave_sum(float v) { v = dpp_add<0x121>(v); v = dpp_add<0x122>(v); v = dpp_add<0x124>(v); v = dpp_add<0x128>(v); return xsum32(xsum16(v)); }
DEV f32x4 sel4(int c, f32x4 a, f32x4 b) { return f32x4{c ? a[0] : b[0], c ? a[1] : b[1], c ? a[2] : b[2], c ? a[3] : b[3]}; }
DEV LAS char* opq(LAS char* p) { unsigned v = (unsigned)(size_t)p; pin(v); return (LAS char*)(size_t)v; }
DEV f32x4 zero4() { f32x4 z = {0, 0, 0, 0}; pin(z); return z; }
#define VMCNT(n) asm volatile("s_waitcnt vmcnt(%0)" ::"i"(n) : "memory")
#define LGKM0() asm volatile("s_waitcnt lgkmcnt(0)" ::: "memory")
#define CFENCE() asm volatile("" ::: "memory")
#define SB() __builtin_amdgcn_sched_barrier(0)
DEV void mfma_bf(f32x4& c, v4u a, v4u b) { asm volatile("v_mfma_f32_16x16x32_bf16 %0, %1, %2, %0" : "+v"(c) : "v"(a), "v"(b)); }
DEV void mfma_bf0(f32x4& d, v4u a, v4u b) { asm volatile("v_mfma_f32_16x16x32_bf16 %0, %1, %2, 0" : "=&v"(d) : "v"(a), "v"(b)); }
DEV void mfma_bf16k(f32x4& c, v2u a, v2u b) { asm volatile("v_mfma_f32_16x16x16_bf16 %0, %1, %2, %0" : "+v"(c) : "v"(a), "v"(b)); }
DEV void mfma_h(f32x4& c, v4u a, v4u b) { asm volatile("v_mfma_f32_16x16x32_f16 %0, %1, %2, %0" : "+v"(c) : "v"(a), "v"(b)); }
#define NOP2() do { SB(); asm volatile("s_nop 1"); SB(); } while (0)
#define MFMA_DONE() do { SB(); asm volatile("s_nop 7"); SB(); } while (0)

DEV void st_sc1(v4u v, rsrc_t r, int voff, int soff) { __builtin_amdgcn_raw_buffer_store_b128(v, r, voff, soff, AUX_SC1); NOP2(); }
DEV void lds_put(unsigned addr, unsigned v) { asm volatile("s_mov_b64 exec, 1\n\tds_write_b32 %0, %1\n\ts_mov_b64 exec, -1" ::"v"(addr), "v"(v) : "memory"); }
template <int OFF, int WANT> DEV void lds_wait(unsigned fa) {
  unsigned t, n;
  asm volatile("s_mov_b32 %1, 0x20000\n"
               "1:\n\tds_read_b32 %0, %2 offset:%3\n\ts_waitcnt lgkmcnt(0)\n\tv_cmp_gt_u32 vcc, %4, %0\n\ts_cbranch_vccz 2f\n\t"
               "s_sleep 1\n\ts_sub_u32 %1, %1, 1\n\ts_cbranch_scc0 1b\n2:"
               : "=&v"(t), "=&s"(n) : "v"(fa), "i"(OFF), "i"(WANT) : "memory", "vcc", "scc");
}
template <int OFF> DEV void lds_wait_s(unsigned fa, int want) {
  unsigned t, n;
  asm volatile("s_mov_b32 %1, 0x20000\n"
               "1:\n\tds_read_b32 %0, %2 offset:%3\n\ts_waitcnt lgkmcnt(0)\n\tv_cmp_gt_u32 vcc, %4, %0\n\ts_cbranch_vccz 2f\n\t"
               "s_sleep 1\n\ts_sub_u32 %1, %1, 1\n\ts_cbranch_scc0 1b\n2:"
               : "=&v"(t), "=&s"(n) : "v"(fa), "i"(OFF), "s"(want) : "memory", "vcc", "scc");
}
static __device__ const unsigned kSeq[64] = {0,1,2,3,4,5,6,7,8,9,10,11,12,13,14,15,16,17,18,19,20,21,22,23,24,25,26,27,28,29,30,31,32,
  33,34,35,36,37,38,39,40,41,42,43,44,45,46,47,48,49,50,51,52,53,54,55,56,57,58,59,60,61,62,63};
DEV v4u srd(const void* p, unsigned bytes) { const u64 v = (u64)p; return v4u{(unsigned)v, (unsigned)(v >> 32) & 0xffffu, bytes, 0x00020000u}; }
DEV void dma16(v4u rs, unsigned lds, int voff, int soff) {
  asm volatile("s_mov_b32 m0, %0\n\ts_nop 0\n\tbuffer_load_dwordx4 %1, %2, %3 offen lds" ::"s"(lds), "v"(voff), "s"(rs), "s"(soff) : "memory");
}
DEV void dma16x(v4u rs, unsigned lds, int voff, int soff) {
  asm volatile("s_mov_b32 m0, %0\n\ts_nop 0\n\tbuffer_load_dwordx4 %1, %2, %3 offen" XPOL " lds" ::"s"(lds), "v"(voff), "s"(rs), "s"(soff) : "memory");
}
template <int N> DEV void dma_flag(v4u rs, unsigned lds) {
  asm volatile("s_mov_b32 m0, %0\n\ts_mov_b64 exec, 1\n\tbuffer_load_dword off, %1, %2 lds\n\ts_mov_b64 exec, -1" ::"s"(lds), "s"(rs), "s"(N * 4) : "memory");
}
DEV void dma_ep(v4u rs, unsigned lds, int voff) {
  asm volatile("s_mov_b32 m0, %0\n\ts_mov_b64 exec, 3\n\tbuffer_load_dword %2, %1, 0 offen sc1 lds\n\ts_mov_b64 exec, -1" ::"s"(lds), "s"(rs), "v"(voff) : "memory");
}
constexpr int CODE_BYTES = 29 * 1024, L_JUNK = 163840 - 512, L_B2 = 163840 - 768;
DEV void dma_touch(v4u rs, int voff) {
  asm volatile("s_mov_b32 m0, %0\n\ts_nop 0\n\tbuffer_load_dword %2, %1, 0 offen lds" ::"s"(L_JUNK), "s"(rs), "v"(voff) : "memory");
}
DEV void dma_b2(v4u rs, int voff) {
  asm volatile("s_mov_b32 m0, %0\n\ts_mov_b64 exec, 0xffffffff\n\tbuffer_load_dword %2, %1, 0 offen lds\n\ts_mov_b64 exec, -1" ::"s"(L_B2), "s"(rs), "v"(voff) : "memory");
}
DEV void dma_touch1(v4u rs, int soff) {
  asm volatile("s_mov_b32 m0, %0\n\ts_mov_b64 exec, 1\n\tbuffer_load_dword off, %1, %2 lds\n\ts_mov_b64 exec, -1" ::"s"(L_JUNK), "s"(rs), "s"(soff) : "memory");
}
template <int OFF> DEV void put_tid(unsigned v) { asm volatile("ds_write_addtid_b32 %0 offset:%1" ::"v"(v), "i"(OFF) : "memory"); }
DEV void set_m0(unsigned v) { asm volatile("s_mov_b32 m0, %0\n\ts_nop 0" ::"s"(v) : "memory"); }
DEV GAS u64* gflag(const Args& a, int line) { return (GAS u64*)(a.ws + WS_FLAG) + line * FSTR; }
DEV void publish(GAS u64* f, u64 tag, int lane) {
  VMCNT(0);
  if (lane == 0) __hip_atomic_store(f, tag, __ATOMIC_RELAXED, __HIP_MEMORY_SCOPE_AGENT);
}
template <int N> DEV void flag_store(GAS u64* f, v2u tag) {
  int z = 0;
  asm volatile("s_waitcnt vmcnt(%3)\n\ts_mov_b64 exec, 1\n\tglobal_store_dwordx2 %0, %1, %2 sc1\n\ts_mov_b64 exec, -1" ::"v"(z), "v"(tag), "s"(f), "i"(N) : "memory");
}
DEV void poll(GAS u64* p, u64 tag) {
#pragma nounroll
  for (int it = 0; it < POLL_CAP; it++) {
    if (!wany(__hip_atomic_load(p, __ATOMIC_RELAXED, __HIP_MEMORY_SCOPE_AGENT) != tag)) break;
    __builtin_amdgcn_s_sleep(PSLEEP);
  }
  __builtin_amdgcn_fence(__ATOMIC_ACQUIRE, "wavefront");
}
DEV void poll2(GAS u64* p, GAS u64* q, u64 tag) {
#pragma nounroll
  for (int it = 0; it < POLL_CAP; it++) {
    const u64 x = __hip_atomic_load(p, __ATOMIC_RELAXED, __HIP_MEMORY_SCOPE_AGENT), y = __hip_atomic_load(q, __ATOMIC_RELAXED, __HIP_MEMORY_SCOPE_AGENT);
    if (!wany(x != tag || y != tag)) break;
    __builtin_amdgcn_s_sleep(PSLEEP);
  }
  __builtin_amdgcn_fence(__ATOMIC_ACQUIRE, "wavefront");
}
DEV int unit_start(int j) { return (j * 37 + 3) / 7; }

constexpr int KCOPY = 4160, C_U = 2 * KCOPY, CH = C_U + 4608, TABB = 4352, L_STAT = 31744;
constexpr int L_X = 32768, L_RING = L_X + 65536, L_TAB = L_RING + 24576;
constexpr int L_Y = 0, YROW = 160, L_EX = L_Y + 64 * YROW, L_YS = L_EX + 16384;
constexpr int LDS_BYTES = 163840, FLG = LDS_BYTES - 256;
static_assert(4 * TABB <= 36864 && 2 * CH <= L_STAT && L_YS + 256 <= L_STAT && L_RING + 61440 <= FLG, "lds");
constexpr int F_FULL = 0, F_FREE = 16, F_CW = 32, F_GX = 48, F_EP = 64, F_ST = 80, F_HOK = 96, F_GP = 112;

constexpr int NSTEP1 = 24;
#define KINDS 0, 0, 0, 0, 0, 0, 0, 0, 1, 1, 1, 1, 1, 1, 1, 1, 1, 1, 1, 1, 1, 1, 1, 1
constexpr int ST_KIND[NSTEP1] = {KINDS};
constexpr int st_idx(int s) { int n = 0; for (int i = 0; i < s; i++) n += ST_KIND[i] == ST_KIND[s]; return n; }
constexpr int step_of(int kind, int idx) { for (int s = 0; s < NSTEP1; s++) if (ST_KIND[s] == kind && st_idx(s) == idx) return s; return -1; }
constexpr int need_X(int c) { return step_of(0, c) + 1; }
constexpr int need_W(int u) { return step_of(1, u) + 1; }
constexpr int need_V(int t) { return NSTEP1 + t + 1; }
static_assert(need_X(0) < need_W(0) && need_X(1) < need_W(2) && need_X(2) < need_W(4) && need_X(3) < need_W(6) && need_X(7) < need_W(14), "x before its units");
constexpr int w_addr(int u) { return u % 7 < 5 ? L_RING + (u % 7) * 12288 : (u % 7 - 5) * 12288; }
constexpr int x_addr(int c) { return L_X + ((c + 1) & 7) * 8192; }
constexpr int FREE_C = 14;
constexpr int NVB = 14, L_V = L_X + 8192, VH = 7;
#define PD 6
#define VDEPTH 12

DEV int kstep(int v, int rot) { const int k = v + rot; return k >= NKS ? k - NKS : k; }
#define NENTRY 4
DEV void x_step(v4u rx, const int (&vx)[2], int sx, int idx, int l, int j, int lds) {
  const int so = sx + ((idx + j + 1) & 7) * 128;
#pragma unroll
  for (int i = 0; i < 2; i++) dma16x(rx, lds + l * 2048 + i * 1024, vx[i], so);
}
DEV void loader_entry(const Args& a, int l, int lane, int p, int j, int (&vx)[2]) {
  vx[0] = (lane >> 3) * 1024 + (((lane & 7) ^ (lane >> 4)) * 16); vx[1] = (vx[0] ^ 64) + 8192;
  const v4u rx = srd(a.x, SEQ * DM * 2);
  static_for<0, NENTRY>([&](auto S) LAM { constexpr int s = S; x_step(rx, vx, p * 65536 + l * 16384, s, l, j, CE(x_addr(s))); });
}
DEV void role_loader(const Args& a, LAS char* sm, int l, int lane, int p, int j, unsigned fa, const int (&vx)[2]) {
  const v4u rx = srd(a.x, SEQ * DM * 2), rw = srd(a.w1, DM * FF * 2), rv = srd(a.w2, DM * FF * 2), rq = srd(kSeq, 256);
  int vw[3], vt, vv;
  const int sx = p * 65536 + l * 16384, sw = unit_start(j) * 64;
  const int st = (((int)blockIdx.x >> 3) * 43 + l) * 1024, sc = ((3 - (int)((size_t)a.w2 >> 7)) & 7) * 128;
  TR_DECL; TR(0);
  if (l == 3) dma_b2(srd(a.b2 + 64 * j, 128), lane * 4);
  if (l < 2) dma_ep(srd(a.ws + WS_FLAG + (size_t)(l ? GF_ER + 16 * p : GF_EC + 16 * j) * 8 * FSTR, 8), FLG + F_EP + 8 * l, lane * 4);
  static_for<NENTRY, NSTEP1>([&](auto S) LAM {
    constexpr int s = S, kind = ST_KIND[s], idx = st_idx(s);
    if constexpr (s % 4 == 0) TR(s / 4);
    if constexpr (s == W2PF) { if (l == 3) dma_touch(rv, vt); }
    if constexpr (s == PREP) {
      const int nown = SKIP6 ? (j == 7 ? NKS : unit_start(j + 1)) - unit_start(j) : 6;
#pragma unroll
      for (int n = 0; n < 3; n++) {
        const int sg = 16 * (3 * l + n) + (lane >> 2), r = (sg * 171) >> 10, qp = sg - 6 * r, q = qp ^ ((r >> 1) & 1);
        vw[n] = q < nown ? r * (FF * 2) + q * 64 + (((lane & 3) ^ (2 * ((r >> 3) & 1))) * 16) : 0x40000000;
        pin(vw[n]);
      }
      vt = lane < 43 ? (blockIdx.x * 43 + lane) * 128 : 0x40000000;
      vv = (8 * l + (lane >> 3)) * 1024 + (((((lane & 7) >> 1) ^ (((lane >> 4) & 1) | ((l & 1) << 1))) * 2 + (lane & 1)) * 16) + j * 128;
      pin(vt); pin(vv);
      __builtin_amdgcn_s_setprio(LPRIO);
    }
    if constexpr (kind == 0) {
      x_step(rx, vx, sx, idx, l, j, CE(x_addr(idx)));
    } else {
      if constexpr (idx >= 7) lds_wait<F_FREE, idx - 6>(fa);
      const int so = sw + ((((idx >> 1) + j + 1) & 7) * 64 + (idx & 1) * 32) * (FF * 2);
#pragma unroll
      for (int n = 0; n < 3; n++) {
        dma16(rw, CE(w_addr(idx)) + l * 3072 + n * 1024, vw[n], so);
      }
    }
    if constexpr (kind == 1 || idx == 4 || idx == 7) dma_flag<s + 1>(rq, FLG + F_FULL + 4 * l);
    if constexpr (kind == 1 && idx >= 5) dma_touch1(rv, min(st + (idx - 5) * 4096, 1375 * 1024) + sc);
  });
  TR(6);
  const int rot = ROTF(j);
  const rsrc_t rv2 = mk_rsrc(a.w2, DM * FF * 2);
  v4u wr[22][2];
  LAS char* wa[2] = {opq(sm + L_V + l * 1024 + lane * 16), opq(sm + L_V + VH * 8192 + l * 1024 + lane * 16)};
  volatile LAS unsigned* flw = (volatile LAS unsigned*)opq(sm + (lane ? L_JUNK + lane * 4 : FLG + F_FULL + 4 * l));
  auto get = [&](auto TT) LAM {
    constexpr int t = TT;
#pragma unroll
    for (int hf = 0; hf < (t < 21 ? 2 : 1); hf++) wr[t][hf] = __builtin_amdgcn_raw_buffer_load_b128(rv2, vv, kstep(2 * t + hf, rot) * 32768, 0);
    SB(); VMCNT(t < NPRE ? VD1 : VDEPTH);
  };
  auto put = [&](auto TT) LAM {
    constexpr int t = TT, q = t % NVB;
    if constexpr (t >= NVB) lds_wait<F_FREE, 17 + t - NVB>(fa);
#pragma unroll
    for (int hf = 0; hf < (t < 21 ? 2 : 1); hf++) *(LAS v4u*)(wa[q / VH] + (q % VH) * 8192 + hf * 4096) = wr[t][hf];
    if constexpr (((t & 1) && t >= VFIRST) || t >= NVB) { CFENCE(); *flw = CE(need_V(t)); }
    SB();
  };
  static_for<0, NPRE>(get);
  TR(7);
  lds_wait<F_FREE, 16>(fa);
  lds_wait<F_CW, PUTGATE>(fa);
  static_for<0, NPRE>(put);
  TR(8);
  lds_wait<F_GP, 1>(fa);
  TR(9);
  static_for<NPRE, 22>(get);
  TR(11);
  static_for<NPRE, 22>(put);
  TR(10);
  if (l == 0) TR_DUMP(0, 12);
}

DEV u64 tag_of(LAS char* sm, int which) {
  const v2u e = *(LAS v2u*)(sm + FLG + F_EP + 8 * which);
  const u64 v = ((u64)(unsigned)__builtin_amdgcn_readfirstlane(e.y) << 32) | (unsigned)__builtin_amdgcn_readfirstlane(e.x);
  return (v + 1) * 0x9E3779B97F4A7C15ull;
}

struct cpx { float r, i; };
DEV cpx cmul(cpx a, cpx b) { return cpx{__builtin_fmaf(a.r, b.r, -a.i * b.i), __builtin_fmaf(a.r, b.i, a.i * b.r)}; }
DEV cpx cexp_p(float zr2, float zi2, float pos) {
  const float e = __builtin_amdgcn_exp2f(zr2 * pos), th = __builtin_amdgcn_fractf(zi2 * pos);
  return cpx{e * __builtin_amdgcn_cosf(th), e * __builtin_amdgcn_sinf(th)};
}
DEV float exp_bf(float v) { return rbf(__builtin_amdgcn_exp2f(v * 1.44269504088896f)); }
DEV void pow2_scale(float mx, float& s, float& inv) {
  const int e = min(max((int)(f2u(mx) >> 23), 15), 254);
  s = u2f((unsigned)(268 - e) << 23);
  inv = u2f((unsigned)(e - 14) << 23);
}

DEV void mfma_h0(f32x4& d, v4u a, v4u b) { asm volatile("v_mfma_f32_16x16x32_f16 %0, %1, %2, 0" : "=&v"(d) : "v"(a), "v"(b)); }
DEV cpx csq(cpx a) { return cpx{__builtin_fmaf(a.r, a.r, -a.i * a.i), 2.f * a.r * a.i}; }
DEV void cmul_h(unsigned re, unsigned im, unsigned m, unsigned& ore, unsigned& oim) {
  const f16x2 r = __builtin_bit_cast(f16x2, re), i = __builtin_bit_cast(f16x2, im), mm = __builtin_bit_cast(f16x2, m);
  const f16x2 mr = {mm.x, mm.x}, mi = {mm.y, mm.y};
  ore = __builtin_bit_cast(unsigned, __builtin_elementwise_fma(r, mr, -(i * mi)));
  oim = __builtin_bit_cast(unsigned, __builtin_elementwise_fma(r, mi, i * mr));
}

struct KIn { unsigned lar, aim, cre, cim, ldt, dsk; };
DEV int c_priv(int cw) { return cw < 3 ? 24576 + cw * 2048 : L_RING + 61440; }
static_assert(24576 + 3 * 2048 <= L_STAT && L_RING + 61440 + 2048 <= L_B2, "lds");
DEV void dma4(const void* base, unsigned lds, int voff) {
  asm volatile("s_mov_b32 m0, %0\n\ts_nop 0\n\tglobal_load_lds_dword %1, %2" PPOL ::"s"(lds), "v"(voff), "s"(base) : "memory");
}
DEV void kgen_fetch(const Args& a, int cw, int lane, int p, int j) {
  const int d = 64 * j + 2 * p + (cw >> 1), vo = d * 256 + lane * 4, lds = c_priv(cw);
  dma4(a.lar, lds, vo); dma4(a.aim, lds + 256, vo); dma4(a.cre, lds + 512, vo); dma4(a.cim, lds + 768, vo);
  int vs = (d >> 1) * 4; pin(vs);
  asm volatile("s_mov_b64 exec, 1" ::: "memory");
  dma4(a.ldt, lds + 1024, vs); dma4(a.dsk, lds + 1028, vs);
  asm volatile("s_mov_b64 exec, -1" ::: "memory");
}
DEV KIn kgen_read(LAS char* sm, int cw, int lane) {
  LAS char* q = sm + c_priv(cw);
  VMCNT(0);
  KIn k;
  k.lar = *(LAS unsigned*)(q + lane * 4); k.aim = *(LAS unsigned*)(q + 256 + lane * 4); k.cre = *(LAS unsigned*)(q + 512 + lane * 4); k.cim = *(LAS unsigned*)(q + 768 + lane * 4);
  k.ldt = *(LAS u16*)(q + 1024 + (cw & 2)); k.dsk = *(LAS u16*)(q + 1028 + (cw & 2));
  return k;
}
DEV cpx conj(cpx a) { return cpx{a.r, -a.i}; }
DEV void kgen_factors(KIn k, int cw, int lane, v4u (&f)[2][2][4], float& kinv, float& kd) {
  const int sh = (cw & 1) ? 0 : 16;
  const float ldt = bf_lo(k.ldt), dsk = bf_lo(k.dsk);
  const float dt = exp_bf(ldt);
  const float zr2 = -exp_bf(bf_hi(k.lar << sh)) * dt * 1.44269504088896f, zi2 = bf_hi(k.aim << sh) * dt * 0.159154943091895f;
  const cpx E1 = cexp_p(zr2, zi2, 1.f), E2 = csq(E1);
  {
    const cpx E8 = csq(csq(E2)), M = cexp_p(zr2, zi2, 32.f);
    cpx S[8];
    S[0] = cpx{1.f, 0.f}; S[1] = E8; S[2] = csq(E8); S[3] = cmul(S[2], E8); S[4] = M;
#pragma unroll
    for (int i = 1; i < 4; i++) S[4 + i] = cmul(S[i], M);
    const cpx M2 = csq(M), M4 = csq(M2), M6 = cmul(M2, M4);
    const unsigned mm[3] = {pk_h(M2.r, M2.i), pk_h(M4.r, M4.i), pk_h(M6.r, M6.i)};
#pragma unroll
    for (int e = 0; e < 4; e++) { f[1][0][0][e] = pk_h(S[2 * e].r, S[2 * e + 1].r); f[1][1][0][e] = pk_h(S[2 * e].i, S[2 * e + 1].i); }
#pragma unroll
    for (int q = 1; q < 4; q++)
#pragma unroll
      for (int e = 0; e < 4; e++) { unsigned re, im; cmul_h(f[1][0][0][e], f[1][1][0][e], mm[q - 1], re, im); f[1][0][q][e] = re; f[1][1][q][e] = im; }
  }
  float ks;
  {
    const float csum = wave_sum((__builtin_fabsf(bf_lo(k.cre)) + __builtin_fabsf(bf_hi(k.cre))) + (__builtin_fabsf(bf_lo(k.cim)) + __builtin_fabsf(bf_hi(k.cim))));
    pow2_scale(__builtin_fmaf(2.f * dt, csum, __builtin_fabsf(dsk)), ks, kinv);
    const float cs = 2.f * dt * ks;
    const cpx M = cexp_p(zr2, -zi2, 256.f);
    cpx S[8];
    S[0] = cpx{bf_hi(k.cre << sh) * cs, bf_hi(k.cim << sh) * -cs};
#pragma unroll
    for (int i = 1; i < 4; i++) S[i] = cmul(S[i - 1], conj(E2));
#pragma unroll
    for (int i = 0; i < 4; i++) S[4 + i] = cmul(S[i], M);
    const cpx M2 = csq(M), M4 = csq(M2), M6 = cmul(M2, M4), M8 = csq(M4);
    const unsigned m2 = pk_h(M2.r, M2.i), m4 = pk_h(M4.r, M4.i), m6 = pk_h(M6.r, M6.i), m1 = pk_h(E1.r, -E1.i), m8 = pk_h(M8.r, M8.i);
#pragma unroll
    for (int e = 0; e < 4; e++) { f[0][0][0][e] = pk_h(S[2 * e].r, S[2 * e + 1].r); f[0][1][0][e] = pk_h(S[2 * e].i, S[2 * e + 1].i); }
#pragma unroll
    for (int q = 1; q < 4; q++)
#pragma unroll
      for (int e = 0; e < 4; e++) {
        const int src = q >= 2 && (e & 1) ? e >> 1 : e;
        unsigned re = 0, im = 0;
        if (q < 3 || e < 3) cmul_h(f[0][0][0][src], f[0][1][0][src], q == 1 ? m2 : q == 2 ? ((e & 1) ? m1 : m4) : ((e & 1) ? m8 : m6), re, im);
        f[0][0][q][e] = re; f[0][1][q][e] = im;
      }
  }
#pragma unroll
  for (int q = 0; q < 16; q++) pin(f[q >> 3][(q >> 2) & 1][q & 3]);
  kd = ((cw & 1) | lane) == 0 ? dsk * ks : 0.f;
  pin(kinv); pin(kd);
}
DEV void kgen_table(const v4u (&f)[2][2][4], v4u oh, int lane, float kd, f32x4 (&kt)[2][2]) {
  v4u sel[2][2];
  bool oct[8];
#pragma unroll
  for (int q = 0; q < 8; q++) oct[q] = (lane >> 3) == q;
#pragma unroll
  for (int e = 0; e < 4; e++) { sel[0][0][e] = oct[0] ? oh[e] : 0u; sel[0][1][e] = oct[1] ? oh[e] : 0u; }
  pin(sel[0][0]); pin(sel[0][1]);
#pragma unroll
  for (int i = 0; i < 4; i++) kt[i >> 1][i & 1] = zero4();
  kt[0][0][0] = kd; pin(kt[0][0]);
  NOP2();
  f32x4 t[2][2][2];
  v4u o[2][2];
  static_for<0, 9>([&](auto H) LAM {
    constexpr int h = H, y = h & 1, gs = h >> 1, py = y ^ 1;
    static_for<0, 8>([&](auto I) LAM {
      constexpr int i = I;
      SB();
      if constexpr (h < 8) {
        if constexpr (i < 4) mfma_h0(t[y][i >> 1][i & 1], f[y][i & 1][2 * (i >> 1)], sel[gs & 1][0]);
        else mfma_h(t[y][(i - 4) >> 1][i & 1], f[y][i & 1][2 * ((i - 4) >> 1) + 1], sel[gs & 1][1]);
      } else if constexpr (i < 4) asm volatile("s_nop 1");
      SB();
      if constexpr (y == 1 && gs < 3 && i < 4) { sel[(gs + 1) & 1][0][i] = oct[2 * gs + 2] ? oh[i] : 0u; sel[(gs + 1) & 1][1][i] = oct[2 * gs + 3] ? oh[i] : 0u; }
      if constexpr (y == 1 && i == 4 && gs < 3) { pin(sel[(gs + 1) & 1][0]); pin(sel[(gs + 1) & 1][1]); }
      if constexpr (h > 0 && i >= 4) {
        const f32x4 v = t[py][(i - 4) >> 1][i & 1];
        o[py][(i - 4) >> 1][2 * (i & 1)] = pk_h(v[0], v[1]); o[py][(i - 4) >> 1][2 * (i & 1) + 1] = pk_h(v[2], v[3]);
      }
    });
    if constexpr (h > 0 && y == 0) {
      pin(o[0][0]); pin(o[0][1]); pin(o[1][0]); pin(o[1][1]);
      NOP2();
#pragma unroll
      for (int i = 0; i < 4; i++) mfma_h(kt[i >> 1][i & 1], o[0][i >> 1], o[1][i & 1]);
    }
  });
  MFMA_DONE();
}
constexpr unsigned TMASK = 0x00070007u;
DEV v4u seal(v4u v, v4u t) { for (int e = 0; e < 4; e++) v[e] = ((v[e] + 0x00040004u) & ~TMASK) | t[e]; return v; }
DEV unsigned unseal(v4u& v, v4u t) { v ^= t; return (v.x | v.y | v.z | v.w) & TMASK; }

#define TA1 "s_load_dword %0, %1, 0x0\n\t" "s_load_dword %0, %1, 0x80\n\t" "s_load_dword %0, %1, 0x100\n\t" "s_load_dword %0, %1, 0x180\n\t" "s_load_dword %0, %1, 0x200\n\t" "s_load_dword %0, %1, 0x280\n\t" "s_load_dword %0, %1, 0x300\n\t" "s_load_dword %0, %1, 0x380\n\t" "s_load_dword %0, %1, 0x400\n\t" "s_load_dword %0, %1, 0x480\n\t" "s_load_dword %0, %1, 0x500\n\t"
#define TA2 "s_load_dword %0, %1, 0x0\n\t" "s_load_dword %0, %1, 0x40\n\t" "s_load_dword %0, %1, 0x80\n\t" "s_load_dword %0, %1, 0xc0\n\t" "s_load_dword %0, %1, 0x100\n\t" "s_load_dword %0, %1, 0x140\n\t" "s_load_dword %0, %1, 0x180\n\t" "s_load_dword %0, %1, 0x1c0\n\t" "s_load_dword %0, %1, 0x200\n\t" "s_load_dword %0, %1, 0x240\n\t" "s_load_dword %0, %1, 0x280\n\t" "s_load_dword %0, %1, 0x2c0\n\t" "s_load_dword %0, %1, 0x300\n\t" "s_load_dword %0, %1, 0x340\n\t" "s_load_dword %0, %1, 0x380\n\t" "s_load_dword %0, %1, 0x3c0\n\t" "s_load_dword %0, %1, 0x400\n\t" "s_load_dword %0, %1, 0x440\n\t" "s_load_dword %0, %1, 0x480\n\t" "s_load_dword %0, %1, 0x4c0\n\t" "s_load_dword %0, %1, 0x500\n\t" "s_load_dword %0, %1, 0x540\n\t"
#define TB1 "s_load_dword %0, %2, 0x0\n\t" "s_load_dword %0, %2, 0x400\n\t" "s_load_dword %0, %2, 0x800\n\t" "s_load_dword %0, %2, 0xc00\n\t" "s_load_dword %0, %2, 0x1000\n\t" "s_load_dword %0, %2, 0x1400\n\t" "s_load_dword %0, %2, 0x1800\n\t" "s_load_dword %0, %2, 0x1c00\n\t" "s_load_dword %0, %2, 0x2000\n\t" "s_load_dword %0, %2, 0x2400\n\t" "s_load_dword %0, %2, 0x2800\n\t"
#define TB2 "s_load_dword %0, %2, 0x0\n\t" "s_load_dword %0, %2, 0x40\n\t" "s_load_dword %0, %2, 0x400\n\t" "s_load_dword %0, %2, 0x440\n\t" "s_load_dword %0, %2, 0x800\n\t" "s_load_dword %0, %2, 0x840\n\t" "s_load_dword %0, %2, 0xc00\n\t" "s_load_dword %0, %2, 0xc40\n\t" "s_load_dword %0, %2, 0x1000\n\t" "s_load_dword %0, %2, 0x1040\n\t" "s_load_dword %0, %2, 0x1400\n\t" "s_load_dword %0, %2, 0x1440\n\t" "s_load_dword %0, %2, 0x1800\n\t" "s_load_dword %0, %2, 0x1840\n\t" "s_load_dword %0, %2, 0x1c00\n\t" "s_load_dword %0, %2, 0x1c40\n\t" "s_load_dword %0, %2, 0x2000\n\t" "s_load_dword %0, %2, 0x2040\n\t" "s_load_dword %0, %2, 0x2400\n\t" "s_load_dword %0, %2, 0x2440\n\t" "s_load_dword %0, %2, 0x2800\n\t" "s_load_dword %0, %2, 0x2840\n\t"
DEV void role_gemm(const Args& a, LAS char* sm, int w, int lane, int p, int j, unsigned fa) {
  const int g = lane >> 4, tok = lane & 15, q4 = tok >> 2, pp = lane & 3;
  const v4u rq = srd(kSeq, 256);
  const unsigned fre = FLG + F_FREE + 4 * w;
  volatile LAS unsigned* full = (volatile LAS unsigned*)(size_t)(fa + F_FULL);
  const rsrc_t rh = mk_rsrc(a.ws + WS_H, 32 * NKS * 4096);
  const int ks = w >> 1, ah = w & 1;
  TR_DECL; TR(0);
  u64 tagr;
  v2u tgv;
  GAS u64* fh = gflag(a, GF_H + (p * 8 + j) * 4 + w); pins(fh);
  {
    const int us = unit_start(j), nown = (j == 7 ? NKS : unit_start(j + 1)) - us;
    unsigned bias[3];
#pragma unroll
    for (int i = 0; i < 3; i++) {
      bias[i] = *(const GAS u16*)(a.b1 + us * 32 + (3 * w + i) * 16 + tok);
    }
    LAS char* wb[2][3];
#pragma unroll
    for (int i = 0; i < 3; i++) {
      const int ft = 3 * w + i;
      wb[0][i] = opq(sm + (6 * (8 * g + q4) + ((ft >> 1) ^ ((q4 >> 1) & 1))) * 64 + (((ft & 1) ^ (g & 1)) * 32) + 8 * pp);
      wb[1][i] = opq(wb[0][i] + L_RING);
    }
    LAS char* xb[2];
#pragma unroll
    for (int s = 0; s < 2; s++) xb[s] = opq(sm + L_X + tok * 128 + (((4 * s + g) ^ ((tok >> 1) & 7)) * 16));
    int hoffv = (g & 1) * 1024 + (g >> 1) * 256 + tok * 16; pin(hoffv);
    f32x4 acc[3][4], cs[3];
#pragma unroll
    for (int i = 0; i < 3; i++) { cs[i] = zero4();
#pragma unroll
      for (int T = 0; T < 4; T++) acc[i][T] = zero4(); }
    v4u ones = {0x3f803f80u, 0x3f803f80u, 0x3f803f80u, 0x3f803f80u};
    pin(ones);
#pragma unroll
    for (int i = 0; i < 3; i++) { bias[i] = g ? 0u : bias[i] << 16; pin(bias[i]); }
    v2u wfr[2][3][2]; v4u xfr[2][4];
    auto rd = [&](auto U, auto K) LAM {
      constexpr int u = U, k = K, wa = w_addr(u), hi = wa >= L_RING;
      if constexpr (k >= 2 && k < 6) xfr[u & 1][k - 2] = *(LAS v4u*)(xb[u & 1] + (CE(x_addr(u >> 1)) - L_X) + (k - 2) * 2048);
      else { constexpr int i = k < 2 ? 0 : (k - 4) >> 1; wfr[u & 1][i][k & 1] = tr_read(wb[hi][i] + (wa - hi * L_RING) + (k & 1) * 1536); }
    };
    lds_wait<F_FULL, need_W(0)>(fa);
    TR(1);
    static_for<0, 10>([&](auto K) LAM { rd(std::integral_constant<int, 0>{}, K); });
    tagr = tag_of(sm, 1); pins(tagr);
    tgv = v2u{(unsigned)tagr, (unsigned)(tagr >> 32)}; pin(tgv);
    lds_wait<F_FULL, need_W(1)>(fa);
    dma_flag<1>(rq, fre);
    int vo[3], so[3];
#pragma unroll
    for (int i = 0; i < 3; i++) {
      const int ft = 3 * w + i;
      so[i] = (p * NKS + us + (ft >> 1)) * 4096 + (ft & 1) * 512; pins(so[i]);
    }
    LAS char* sr = opq(sm + L_STAT + tok * 16);
    static_for<0, 15>([&](auto U) LAM {
      constexpr int u = U, b = u & 1;
      if constexpr (u == 4 || u == 8 || u == 12) TR(2 + (u - 4) / 4);
      unsigned fv; bool lack = false;
      if constexpr (u < 14) fv = *full;
      static_for<0, 15>([&](auto M) LAM {
        constexpr int m = M, i = m / 5, T = m % 5;
        const v4u wv = {wfr[b][i][0].x, wfr[b][i][0].y, wfr[b][i][1].x, wfr[b][i][1].y};
        SB();
        if constexpr (T < 4) mfma_bf(acc[i][T], wv, xfr[b][T]); else mfma_bf(cs[i], ones, wv);
        SB();
        if constexpr (u < 15 && m < 10) rd(std::integral_constant<int, u + 1>{}, M);
        if constexpr (u < 14 && m == 11) lack = wany(fv < CE(need_W(u + 2)));
        if constexpr (u < 14 && m == 13) __builtin_amdgcn_s_waitcnt(0xc07f);
        if constexpr (u + 2 <= 9 && m == 13) dma_flag<u + 2>(rq, fre);
        if constexpr (u + 2 == FREE_C && m == 10) { VMCNT(0); lds_put(fre, u + 2); }
        if constexpr (u == 14 && m == 10) lds_put(fre, 16);
        if constexpr (u == 14 && m == 11) fv = full[F_CW / 4];
        if constexpr (u == 14 && m == 14) { __builtin_amdgcn_s_waitcnt(0xc07f); lack = wany(fv < 1); }
      });
      SB();
      if constexpr (u < 14) { if (__builtin_expect(lack, 0)) lds_wait<F_FULL, need_W(u + 2)>(fa); CFENCE(); }
      else { if (__builtin_expect(lack, 0)) lds_wait<F_CW, 1>(fa); CFENCE(); }
    });
    TR(5);
    v2u sb[4], av[3];
    float cr[3][3];
    auto wv = [&](int i) LAM { return v4u{wfr[1][i][0].x, wfr[1][i][0].y, wfr[1][i][1].x, wfr[1][i][1].y}; };
    auto epi = [&](int i, int T) LAM {
      unsigned pk[2][2];
#pragma unroll
      for (int t = 0; t < 2; t++)
#pragma unroll
        for (int r = 0; r < 2; r++) {
          const float t0 = acc[i][T + t][2 * r], t1 = acc[i][T + t][2 * r + 1];
          pk[t][r] = relu_pk(pk_bf(t0 * __builtin_fabsf(t0), t1 * __builtin_fabsf(t1)));
        }
      auto r0 = __builtin_amdgcn_permlane16_swap(pk[0][0], pk[1][0], false, false);
      auto r1 = __builtin_amdgcn_permlane16_swap(pk[0][1], pk[1][1], false, false);
      st_sc1(v4u{r0[0], r1[0], r0[1], r1[1]}, rh, vo[i] + T * 1024, so[i]);
    };
    static_for<0, 27>([&](auto M) LAM {
      constexpr int m = M;
      SB();
      if constexpr (m < 3) mfma_bf(cs[m], ones, wv(m));
      else if constexpr (m < 15) mfma_bf(acc[(m - 3) >> 2][(m - 3) & 3], wv((m - 3) >> 2), xfr[1][(m - 3) & 3]);
      else mfma_bf16k(acc[(m - 15) >> 2][(m - 15) & 3], av[(m - 15) >> 2], sb[(m - 15) & 3]);
      SB();
      if constexpr (m < 4) sb[m] = *(LAS v2u*)(sr + m * 256);
      if constexpr (m < 3) { vo[m] = hoffv | (((3 * w + m) >> 1) < nown ? 0 : 0x40000000); pin(vo[m]); }
      if constexpr (m >= 5 && m < 14) {
        constexpr int i = (m - 5) / 3, k = (m - 5) % 3;
        if constexpr (k == 0) { pin(cs[i]); cr[i][0] = g ? 0.f : cs[i][0]; cr[i][1] = rbf(cr[i][0]); pin(cr[i][0]); pin(cr[i][1]); }
        if constexpr (k == 1) { cr[i][1] = cr[i][0] - cr[i][1]; cr[i][2] = cr[i][1] - rbf(cr[i][1]); pin(cr[i][1]); pin(cr[i][2]); }
        if constexpr (k == 2) { av[i] = v2u{pk_bf(cr[i][0], cr[i][1]), pk_bf(cr[i][2], u2f(bias[i]))}; pin(av[i]); }
      }
    });
    SB();
#pragma unroll
    for (int i = 0; i < 6; i++) { epi(i >> 1, 2 * (i & 1)); SB(); }
  }
  lds_put(FLG + F_GX + 4 * w, 1);
  TR(6);
  __builtin_amdgcn_s_setprio(3);
  GAS u64* pf = gflag(a, GF_H + p * 32 + (lane & 31));
  LAS char* wb[2][4];
  const int phi = ((q4 >> 1) & 1) | ((g & 1) << 1);
#pragma unroll
  for (int e = 0; e < 4; e++) { wb[0][e] = opq(sm + L_V + ks * 4096 + (8 * g + q4) * 128 + ((e ^ phi) * 32) + 8 * pp); wb[1][e] = opq(wb[0][e] + VH * 8192); }
  f32x4 acc[4][2];
#pragma unroll
  for (int e = 0; e < 4; e++) { acc[e][0] = zero4(); acc[e][1] = zero4(); }
  v2u wfr[2][4][2];
  auto rd2 = [&](auto I, auto K) LAM {
    constexpr int i = I, k = K, t = i % NVB;
    wfr[i & 1][k >> 1][k & 1] = tr_read(wb[t / VH][k >> 1] + (t % VH) * 8192 + (k & 1) * 512);
  };
  const int T = 2 * ah + ks;
  int hv0 = lane * 16 + ks * 1024, hv1 = hv0 ^ 1024;
  volatile LAS unsigned* frw = (volatile LAS unsigned*)opq(sm + (lane ? L_JUNK + lane * 4 : (int)fre));
  int hso = p * NKS * 4096 + ah * 2048, rot = ROTF(j) + ks;
  pin(hv0); pin(hv1); pin(pf); pins(hso); pins(rot);
  SB();
  TR(16);
  flag_store<0>(fh, tgv);
  lds_put(FLG + F_GP + 4 * w, 1);
  TR(7);
  if (w == 0) {
    __builtin_amdgcn_s_sleep(HDELAY);
    poll(pf, tagr);
    lds_put(FLG + F_HOK, 1);
  } else lds_wait<0, 1>((unsigned)(FLG + F_HOK));
  TR(8);
  v4u hf[PD][2];
#pragma unroll
  for (int i = 0; i < PD - 1; i++) {
    hf[i][0] = __builtin_amdgcn_raw_buffer_load_b128(rh, hv0, hso + kstep(2 * i, rot) * 4096, HAUX);
    hf[i][1] = __builtin_amdgcn_raw_buffer_load_b128(rh, hv1, hso + kstep(2 * i, rot) * 4096, HAUX);
  }
  SB();
  float rest[4][4];
#pragma unroll
  for (int e = 0; e < 4; e++) {
    const v2u b2 = *(LAS v2u*)(sm + L_B2 + 32 * e + 8 * g);
    const v2u xv = *(LAS v2u*)(sm + L_X + (16 * T + tok) * 128 + (((2 * e + (g >> 1)) ^ ((tok >> 1) & 7)) * 16) + (g & 1) * 8);
    rest[e][0] = bf_lo(b2.x) + bf_lo(xv.x); rest[e][1] = bf_hi(b2.x) + bf_hi(xv.x); rest[e][2] = bf_lo(b2.y) + bf_lo(xv.y); rest[e][3] = bf_hi(b2.y) + bf_hi(xv.y);
#pragma unroll
    for (int r = 0; r < 4; r++) pin(rest[e][r]);
  }
  float rs2 = *(LAS float*)(sm + L_STAT + (16 * T + tok) * 16 + 8);
  pin(rs2);
  __builtin_amdgcn_s_setprio(2);
  lds_wait<F_FULL, need_V(VFIRST)>(fa);
  static_for<0, 8>([&](auto K) LAM { rd2(std::integral_constant<int, 0>{}, K); });
  static_for<0, 22>([&](auto I) LAM {
    constexpr int i = I, b = i & 1, nx = i + PD - 1;
    if constexpr (i == 1 || i == 8 || i == 15) TR(9 + (i - 1) / 7);
    if (i < 21 || ks == 0) {
      constexpr bool chk = !(i & 1) && i + 3 > VFIRST && i + 2 < 22;
      constexpr int want = need_V(i + 3 < 22 ? i + 3 : 21), out = 2 * ((nx < 22 ? nx : 22) - 1 - i);
      unsigned fv; bool lack = false;
      SB();
      __builtin_amdgcn_s_waitcnt((out & 15) | ((out >> 4) << 14) | 0x70);
      static_for<0, 8>([&](auto M) LAM {
        constexpr int m = M, e = m >> 1;
        const v4u wv = {wfr[b][e][0].x, wfr[b][e][0].y, wfr[b][e][1].x, wfr[b][e][1].y};
        SB();
        mfma_bf(acc[e][m & 1], wv, hf[i % PD][m & 1]);
        SB();
        if constexpr (chk && m == 0) fv = *full;
        if constexpr (i < 21 && m < 4) { rd2(std::integral_constant<int, i + 1>{}, std::integral_constant<int, 2 * m>{}); rd2(std::integral_constant<int, i + 1>{}, std::integral_constant<int, 2 * m + 1>{}); }
        if constexpr (nx < 22 && m == 4) hf[nx % PD][0] = __builtin_amdgcn_raw_buffer_load_b128(rh, hv0, hso + kstep(2 * nx, rot) * 4096, HAUX);
        if constexpr (nx < 22 && m == 5) hf[nx % PD][1] = __builtin_amdgcn_raw_buffer_load_b128(rh, hv1, hso + kstep(2 * nx, rot) * 4096, HAUX);
        if constexpr (m == 6 && (i & 1) && (i + NVB < 23 || i == YGATE)) { CFENCE(); *frw = 18 + i; }
        if constexpr (chk && m == 6) lack = wany(fv < want);
      });
      SB();
      if constexpr (chk) { if (__builtin_expect(lack, 0)) lds_wait<F_FULL, want>(fa); CFENCE(); }
    }
  });
  LAS char* exw = opq(sm + L_EX + w * 4096 + lane * 16);
  LAS char* rl = sm + L_EX + (w ^ 2) * 4096;
  LAS char* exr = opq(rl + lane * 16);
  LAS char* ya = opq(sm + L_Y + (4 * g + q4) * YROW + T * 32 + 8 * pp);
  LAS char* sa = opq(sm + L_YS + 16 * g);
  LAS char* rlw = opq(rl + tok * 144 + g * 8);
  LAS char* rlr = opq(rl + (lane >> 3) * 144 + (lane & 7) * 16);
  int ov = (lane >> 3) * 1024 + (lane & 7) * 16; pin(ov);
  MFMA_DONE();
  TR(12);
  lds_wait<F_CW, 5>(fa);
#pragma unroll
  for (int e = 0; e < 4; e++) *(LAS f32x4*)(exw + e * 1024) = acc[e][1];
  lds_put(FLG + F_GX + 4 * w, 2);
#pragma unroll
  for (int e = 0; e < 4; e++)
#pragma unroll
    for (int r = 0; r < 4; r++) { rest[e][r] = __builtin_fmaf(acc[e][0][r], rs2, rest[e][r]); pin(rest[e][r]); }
  lds_wait<0, 2>(FLG + F_GX + 4 * (w ^ 2));
  f32x4 po[4];
#pragma unroll
  for (int e = 0; e < 4; e++) po[e] = *(LAS f32x4*)(exr + e * 1024);
#pragma unroll
  for (int e = 0; e < 4; e++)
#pragma unroll
    for (int r = 0; r < 4; r++) { rest[e][r] = __builtin_fmaf(po[e][r], rs2, rest[e][r]); pin(rest[e][r]); }
  TR(13);
  lds_wait<F_CW, 6>(fa);
  TR(15);
  v2u yv[4]; f32x4 ys[4];
#pragma unroll
  for (int e = 0; e < 4; e++) { yv[e] = tr_read(ya + 16 * e * YROW); ys[e] = *(LAS f32x4*)(sa + 64 * e); }
#pragma unroll
  for (int e = 0; e < 4; e++) {
    const unsigned y0 = yv[e].x, y1 = yv[e].y;
    const f16x2 h0 = __builtin_bit_cast(f16x2, y0), h1 = __builtin_bit_cast(f16x2, y1);
    *(LAS v2u*)(rlw + e * 32) = v2u{pk_bf(__builtin_fmaf((float)h0.x, ys[e][0], rest[e][0]), __builtin_fmaf((float)h0.y, ys[e][1], rest[e][1])),
                                    pk_bf(__builtin_fmaf((float)h1.x, ys[e][2], rest[e][2]), __builtin_fmaf((float)h1.y, ys[e][3], rest[e][3]))};
  }
  CFENCE();
  {
    const rsrc_t ro = mk_rsrc(a.out, SEQ * DM * 2);
    const v4u o0 = *(LAS v4u*)(rlr), o1 = *(LAS v4u*)(rlr + 8 * 144);
    st_sc1(o0, ro, ov, (64 * p + 16 * T) * 1024 + j * 128);
    st_sc1(o1, ro, ov, (64 * p + 16 * T + 8) * 1024 + j * 128);
  }
  if (w == 0 && lane < 2 && (lane ? j == 0 : p == 0)) {
    const u64 e1 = *(LAS u64*)(sm + FLG + F_EP + lane * 8) + 1;
    __hip_atomic_store(gflag(a, lane ? GF_ER + 16 * p : GF_EC + 16 * j), e1, __ATOMIC_RELAXED, __HIP_MEMORY_SCOPE_AGENT);
  }
  TR(14);
  if (w == DW) TR_DUMP(16, 17);
}

DEV void role_conv(const Args& a, LAS char* sm, int cw, int lane, int p, int j, unsigned fa) {
  const int g = lane >> 4, tok = lane & 15;
  const int ch = cw >> 1, mh = cw & 1, d = 64 * j + 2 * p + ch;
  const unsigned fme = FLG + F_CW + 4 * cw, fpa = FLG + F_CW + 4 * (cw ^ 1);
  TR_DECL; TR(0);
  __builtin_amdgcn_s_setprio(KCPRIO);
  v4u oh, idm[2];
#pragma unroll
  for (int e = 0; e < 4; e++) {
    oh[e] = ((lane >> 1) & 3) == e ? 0x3c00u << (16 * (lane & 1)) : 0u;
    idm[0][e] = g == (tok >> 3) ? oh[e] : 0u; idm[1][e] = g - 2 == (tok >> 3) ? oh[e] : 0u;
  }
  pin(oh); pin(idm[0]); pin(idm[1]);
  f32x4 dm;
#pragma unroll
  for (int r = 0; r < 4; r++) dm[r] = 4 * g + r == tok ? 1.f / DM : 0.f;
  int uoff = (8 * (g & 1) + (tok >> 1)) * 8192 + (g >> 1) * 32 + (tok & 1) * 16;
  LAS char* xs[2];
#pragma unroll
  for (int s = 0; s < 2; s++) xs[s] = opq(sm + L_X + (16 * cw + tok) * 128 + (((4 * s + g) ^ ((tok >> 1) & 7)) * 16));
  pin(dm); pin(uoff);
  const rsrc_t ru = mk_rsrc(a.ws + WS_U, SEQ * DM * 2);
  int uso = j * 262144 + p * 256 + cw * 64; pins(uso);
  v4u kf[2][2][4];
  float kinv, kd;
  SB();
  kgen_factors(kgen_read(sm, cw, lane), cw, lane, kf, kinv, kd);
  TR(1);
  __builtin_amdgcn_s_setprio(TPRIO);
  v2u kh[4];
  {
    f32x4 kt[2][2];
    kgen_table(kf, oh, lane, kd, kt);
#pragma unroll
    for (int i = 0; i < 4; i++) { const f32x4 v = kt[i >> 1][i & 1]; kh[i] = v2u{pk_h(v[0], v[1]), pk_h(v[2], v[3])}; pin(kh[i]); }
  }
  LAS char* tw = opq(sm + c_priv(cw) + g * 256 + tok * 8);
  TR(17);
  u64 tagc;
  v4u stat, pat;
  {
    v4u xl[2];
    f32x4 sa = zero4(), gr = zero4();
    v4u ones = {0x3f803f80u, 0x3f803f80u, 0x3f803f80u, 0x3f803f80u};
    pin(ones);
    __builtin_amdgcn_s_setprio(3);
    static_for<0, 2>([&](auto B) LAM {
      constexpr int c0 = B == 0 ? 0 : 5, c1 = B == 0 ? 5 : 8;
      lds_wait<F_FULL, need_X(c1 - 1)>(fa);
      if constexpr (B == 1) TR(4);
      v4u f[2 * (c1 - c0)];
      unsigned en = 0;
      if constexpr (B == 0) en = *(LAS unsigned*)(sm + FLG + F_EP);
#pragma unroll
      for (int c = c0; c < c1; c++) { f[2 * (c - c0)] = *(LAS v4u*)(xs[0] + ((c + 1) & 7) * 8192); f[2 * (c - c0) + 1] = *(LAS v4u*)(xs[1] + ((c + 1) & 7) * 8192); }
      SB();
#pragma unroll
      for (int i = 0; i < 2 * (c1 - c0); i++) {
        mfma_bf(gr, f[i], f[i]);
        if (B == 0 && i >= 2 && i < 6) { SB(); unsigned t = ((en + (i - 2)) & 7) | (((en + 2 * (i - 2) + 1) & 7) << 16); pin(t); pat[i - 2] = t; SB(); }
        mfma_bf(sa, ones, f[i]);
      }
            if constexpr (B == 1) { xl[0] = f[4]; xl[1] = f[5]; }
    });
    __builtin_amdgcn_s_setprio(SPRIO);
    float xf[2][8];
#pragma unroll
    for (int s = 0; s < 2; s++)
#pragma unroll
      for (int e = 0; e < 4; e++) { const unsigned uu = xl[s][e]; xf[s][2 * e] = bf_lo(uu); xf[s][2 * e + 1] = bf_hi(uu); pin(xf[s][2 * e]); pin(xf[s][2 * e + 1]); }
    pin(sa); pin(gr);
    SB();
    const float mean = sa[0] * (1.f / DM);
    const float ex2 = xsum32(xsum16(__builtin_fmaf(gr[0], dm[0], gr[1] * dm[1]) + __builtin_fmaf(gr[2], dm[2], gr[3] * dm[3])));
    float var = __builtin_fmaf(-mean, mean, ex2);
    if (__builtin_expect(wany(!(var >= ex2 * (1.f / 1024.f))), 0)) {
      float q = 0;
#pragma nounroll
      for (int c = 0; c < 16; c++) {
        const v4u u = *(LAS v4u*)((c & 1 ? xs[1] : xs[0]) + (c >> 1) * 8192);
#pragma unroll
        for (int e = 0; e < 4; e++) { const unsigned uu = u[e]; const float d0 = bf_lo(uu) - mean, d1 = bf_hi(uu) - mean; q = __builtin_fmaf(d0, d0, __builtin_fmaf(d1, d1, q)); }
      }
      var = xsum32(xsum16(q)) * (1.f / DM);
    }
    const float m = rbf(mean), sd = rbf(__builtin_amdgcn_sqrtf(rbf(rbf(var) + 1e-5f))), rs = __builtin_amdgcn_rcpf(sd);
    const float c0 = -m * rs;
    v4u xn[2]; f32x4 t[4];
#pragma unroll
    for (int s = 0; s < 2; s++) {
#pragma unroll
      for (int e = 0; e < 4; e++) xn[s][e] = pk_h(__builtin_fmaf(xf[s][2 * e], rs, c0), __builtin_fmaf(xf[s][2 * e + 1], rs, c0));
      pin(xn[s]);
      NOP2();
      mfma_h0(t[2 * s], xn[s], idm[0]); mfma_h0(t[2 * s + 1], xn[s], idm[1]);
      SB();
    }
#pragma unroll
    for (int s = 0; s < 2; s++) {
      auto r0 = __builtin_amdgcn_permlane16_swap(pk_h(t[2 * s][0], t[2 * s][1]), pk_h(t[2 * s + 1][0], t[2 * s + 1][1]), false, false);
      auto r1 = __builtin_amdgcn_permlane16_swap(pk_h(t[2 * s][2], t[2 * s][3]), pk_h(t[2 * s + 1][2], t[2 * s + 1][3]), false, false);
      st_sc1(seal(v4u{r0[0], r1[0], r0[1], r1[1]}, pat), ru, uoff, uso + s * 131072);
    }
    stat = v4u{pk_bf(-m, -m), pk_bf(-m, sd), f2u(rs * rs), 0u};
    pin(stat);
  }
  LAS char* sw = opq(sm + L_STAT + (16 * cw + tok) * 16);
  TR(5);
  __builtin_amdgcn_s_sleep(TDELAY);
#pragma unroll
  for (int i = 0; i < 4; i++) *(LAS v2u*)(tw + (i >> 1) * 1024 + (i & 1) * 128) = kh[i];
  if (g == 0) *(LAS v4u*)sw = stat;
  lds_put(fme, 1);
  __builtin_amdgcn_s_setprio(EPRIO);
  tagc = tag_of(sm, 0); pins(tagc);
  if (mh == 0 && lane == 0) __hip_atomic_store(gflag(a, GF_Y) + d, (tagc & 0xffffffff00000000ull) | f2u(kinv * 65536.f), __ATOMIC_RELAXED, __HIP_MEMORY_SCOPE_AGENT);
  TR(16);
  v4u im[6];
  {
    const int nh = 127 - lane, nl = 63 - lane, to = c_priv(cw ^ 1) - c_priv(cw);
    LAS char* tb = sm + c_priv(cw);
    auto odd = [&](int q) LAM { return 1028 + (q >> 3) * 8 + ((q >> 2) & 1) * 256 + (q & 2); };
    auto tv = [&](int off) LAM { return *(LAS _Float16*)(tb + off) + *(LAS _Float16*)(tb + to + off); };
    auto pk = [&](_Float16 x, _Float16 y) LAM { return __builtin_bit_cast(unsigned, f16x2{x, y}); };
    lds_wait<0, 1>(fpa);
    if (mh == 0) {
      const _Float16 A = tv(lane ? 16 * nh + 16 : 1540), B = tv(16 * nh + 8), C = tv(16 * nh), A4 = tv(8 * nh + 8), B4 = tv(8 * nh + 4), C4 = tv(8 * nh);
      const unsigned aa = pk(A, A), bb2 = pk(B, B), cc = pk(C, C), b4 = pk(B4, B4), c4 = pk(C4, C4);
      im[0] = v4u{aa, aa, bb2, bb2}; im[1] = v4u{bb2, pk(B, C), cc, cc}; im[2] = v4u{aa, pk(A, B), bb2, bb2}; im[3] = v4u{bb2, cc, cc, cc};
      im[4] = v4u{pk(A4, A4), b4, pk(B4, C4), c4}; im[5] = v4u{pk(A4, B4), b4, c4, c4};
    } else {
      const _Float16 A = tv(4 * nh + 4), B = tv(4 * nh + 2), C = tv(4 * nh), Dd = tv(lane == 63 ? 1028 + 31 * 8 + 258 : 4 * nh);
      const _Float16 g3 = tv(odd(4 * nl + 3)), g2 = tv(4 * nl + 2), g1 = tv(odd(4 * nl + 1)), g0 = tv(4 * nl), gq = tv(odd(max(4 * nl - 1, 1))), gm = lane == 63 ? (_Float16)0 : gq;
      im[0] = v4u{pk(A, B), pk(C, C), pk(B, C), pk(C, Dd)}; im[1] = v4u{pk(g3, g2), pk(g1, g0), pk(g2, g1), pk(g0, gm)};
      im[2] = im[3] = im[4] = im[5] = v4u{0, 0, 0, 0};
    }
#pragma unroll
    for (int i = 0; i < 6; i++) pin(im[i]);
  }
  dma_flag<1>(srd(kSeq, 256), FLG + F_ST + 4 * cw);
  TR(2);
  lds_wait<F_FULL, need_W(UWAIT)>(fa);
  TR(3);
  v4u uv[2];
  auto ldu = [&]() LAM {
#pragma unroll
    for (int i = 0; i < 2; i++) uv[i] = __builtin_amdgcn_raw_buffer_load_b128(ru, lane * 16 + i * 1024, (j * 32 + p) * 8192 + cw * 2048, AUX_SC1);
  };
  ldu();
  SB();
  LAS char* cr = sm + ch * CH;
  LAS char* kc0 = opq(cr + (mh ? 3072 + 8 * lane : 32 * lane));
  LAS char* kc1 = opq(cr + (mh ? 3584 + 8 * lane : 2048 + 16 * lane));
  LAS char* uw = opq(sm + (lane & 1) * CH + C_U + 480 + cw * 1024 + (lane >> 1) * 16);
  LAS char* zw = opq(lane >= 34 ? sm + L_JUNK + (lane & 15) * 16 : cr + (lane < 30 ? C_U + lane * 16 : ((lane >> 1) & 1) * KCOPY + 4096 + (lane & 1) * 16));
  f32x4 acc[8];
#pragma unroll
  for (int c = 0; c < 8; c++) acc[c] = zero4();
  LAS char* ab[4];
#pragma unroll
  for (int i = 0; i < 4; i++) ab[i] = opq(cr + ((15 - tok) & 1) * KCOPY + 2 * (((15 - tok) & ~1) + 8 * g) + 64 * (4 - 4 * mh) + 1024 * i);
  LAS char* bb = opq(cr + C_U + 448 + 32 * tok + 16 * g - 64 * (4 * mh + 3));
  auto lda = [&](int i, int r, int k) LAM { return ld2(ab[(7 - i) >> 1] + 64 * (59 - 8 * i - r) - 1024 * ((7 - i) >> 1) + 8 * k); };
  SB();
#pragma nounroll
  for (int it = 0; it < POLL_CAP; it++) {
    if (!wany((unseal(uv[0], pat) | unseal(uv[1], pat)) != 0)) break;
    __builtin_amdgcn_s_sleep(SSLEEP);
    CFENCE();
    ldu();
  }
  TR(7);
  __builtin_amdgcn_s_setprio(IPRIO);
  lds_wait<F_ST, 1>(fa);
  lds_wait<F_FULL, need_W(15)>(fa);
  lds_wait<F_FREE, FREE_C>(fa);
  TR(8); TR(15);
  if (mh == 0) {
    *(LAS v4u*)(kc0) = im[0]; *(LAS v4u*)(kc0 + 16) = im[1]; *(LAS v4u*)(kc0 + KCOPY) = im[2]; *(LAS v4u*)(kc0 + KCOPY + 16) = im[3];
    *(LAS v4u*)(kc1) = im[4]; *(LAS v4u*)(kc1 + KCOPY) = im[5];
  } else {
    *(LAS v2u*)(kc0) = v2u{im[0].x, im[0].y}; *(LAS v2u*)(kc0 + KCOPY) = v2u{im[0].z, im[0].w};
    *(LAS v2u*)(kc1) = v2u{im[1].x, im[1].y}; *(LAS v2u*)(kc1 + KCOPY) = v2u{im[1].z, im[1].w};
    *(LAS v4u*)zw = im[2];
  }
#pragma unroll
  for (int i = 0; i < 2; i++) *(LAS v4u*)(uw + i * 512) = uv[i];
  lds_put(fme, 2);
  lds_wait<F_CW, 2>(fa);
  TR(6);
  lds_wait<0, 1>(FLG + F_GX + 4 * cw);
  __builtin_amdgcn_s_setprio(CPRIO);
  TR(9);
  v2u A[8][2]; v4u B[2][8];
#pragma unroll
  for (int i = 0; i < 8; i++) {
    B[0][i] = *(LAS v4u*)(bb + 64 * (8 * i + 3));
    A[7 - i][0] = lda(7 - i, 0, 0); A[7 - i][1] = lda(7 - i, 0, 1);
  }
  static_for<0, 4>([&](auto R) LAM {
    constexpr int r = R;
    static_for<0, 8>([&](auto II) LAM {
      constexpr int i = 7 - II;
      static_for<0, 8 - i>([&](auto K) LAM {
        constexpr int k = K;
        SB();
        mfma_h(acc[i + k], v4u{A[i][0].x, A[i][0].y, A[i][1].x, A[i][1].y}, B[r & 1][k]);
        SB();
        if constexpr (r < 3) {
          if constexpr (i < 7 && k < 2) A[i + 1][k] = lda(i + 1, r + 1, k);
          if constexpr (i <= 4 && i >= 1 && (k == 2 || k == 3)) B[(r + 1) & 1][2 * (4 - i) + k - 2] = *(LAS v4u*)(bb + 64 * (8 * (2 * (4 - i) + k - 2) + 3 - (r + 1)));
        }
        if constexpr (r == 3 && i == 4 && k == 0) lds_put(fme, 3);
      });
    });
    if constexpr (r < 3) { A[0][0] = lda(0, r + 1, 0); A[0][1] = lda(0, r + 1, 1); }
  });
  LAS char* exm = opq(cr + mh * 4096 + lane * 16);
  LAS char* exo = opq(cr + (mh ^ 1) * 4096 + lane * 16);
  int yoff = (g & 1) * 512 + (tok >> 2) * 128 + (g >> 1) * 64 + (tok & 3) * 16; pin(yoff);
  const rsrc_t ry = mk_rsrc(a.ws + WS_Y, SEQ * DM * 2);
  constexpr float YS = 1.f / 65536.f;
  MFMA_DONE();
  TR(10);
  lds_wait<0, 3>(fpa);
  auto finish = [&](auto MH) LAM {
    constexpr int own = 4 * MH;
#pragma unroll
    for (int c = 0; c < 4; c++) *(LAS f32x4*)(exm + c * 1024) = acc[(own ^ 4) + c];
    lds_put(fme, 4);
#pragma unroll
    for (int c = 0; c < 4; c++) { acc[own + c] *= YS; pin(acc[own + c]); }
    lds_wait<0, 4>(fpa);
    f32x4 o[4];
#pragma unroll
    for (int c = 0; c < 4; c++) o[c] = *(LAS f32x4*)(exo + c * 1024);
#pragma unroll
    for (int c = 0; c < 4; c += 2) {
      unsigned q[2][2];
#pragma unroll
      for (int t = 0; t < 2; t++)
#pragma unroll
        for (int r = 0; r < 2; r++) q[t][r] = pk_h(__builtin_fmaf(o[c + t][2 * r], YS, acc[own + c + t][2 * r]), __builtin_fmaf(o[c + t][2 * r + 1], YS, acc[own + c + t][2 * r + 1]));
      auto r0 = __builtin_amdgcn_permlane16_swap(q[0][0], q[1][0], false, false);
      auto r1 = __builtin_amdgcn_permlane16_swap(q[0][1], q[1][1], false, false);
      st_sc1(seal(v4u{r0[0], r1[0], r0[1], r1[1]}, pat), ry, yoff + c * 512, d * 4096 + MH * 2048);
    }
  };
  if (mh) finish(std::integral_constant<int, 1>{}); else finish(std::integral_constant<int, 0>{});
  lds_put(fme, 5);
  TR(11);
  LAS char* yw = opq(sm + L_Y + (16 * cw + (lane >> 3)) * YROW + (lane & 3) * 32 + ((lane >> 2) & 1) * 16);
  GAS u64* ps = gflag(a, GF_Y) + 64 * j + 16 * cw + tok; pin(ps);
  lds_wait<F_CW, 5>(fa);
  lds_wait<F_FREE, 17 + YGATE>(fa);
  TR(12);
  u64 sc;
#pragma nounroll
  for (int it = 0; it < POLL_CAP; it++) {
#pragma unroll
    for (int i = 0; i < 2; i++) uv[i] = __builtin_amdgcn_raw_buffer_load_b128(ry, (lane >> 3) * 4096 + (lane & 7) * 16, (64 * j + 16 * cw + 8 * i) * 4096 + p * 128, AUX_SC1);
    sc = __hip_atomic_load(ps, __ATOMIC_RELAXED, __HIP_MEMORY_SCOPE_AGENT);
    if (!wany((unseal(uv[0], pat) | unseal(uv[1], pat) | (unsigned)((sc ^ tagc) >> 32)) != 0)) break;
    __builtin_amdgcn_s_sleep(SSLEEP);
    CFENCE();
  }
  TR(13);
#pragma unroll
  for (int i = 0; i < 2; i++) *(LAS v4u*)(yw + 8 * i * YROW) = uv[i];
  if (g == 0) *(LAS unsigned*)(sm + L_YS + (16 * cw + tok) * 4) = (unsigned)sc;
  lds_put(fme, 6);
  TR(14);
  if (cw == 0) TR_DUMP(48, 18);
}

__global__ __launch_bounds__(NTHR, 3) NOPK void k_fused(Args a) {
  __shared__ __attribute__((aligned(16))) char smem[LDS_BYTES];
  LAS char* sm = (LAS char*)smem;
  const int lane = threadIdx.x & 63, w = __builtin_amdgcn_readfirstlane(threadIdx.x >> 6);
  const int b = blockIdx.x, p = (b & 7) * 4 + (b >> 6), j = (b >> 3) & 7;
  int vx[2];
  if (w == 4) {
    u64 pc; asm volatile("s_getpc_b64 %0" : "=s"(pc));
    dma_touch(srd((const void*)pc, CODE_BYTES), lane < 8 ? ((b >> 3) * 8 + lane) * 128 : 0x40000000);
    ((LAS unsigned*)(sm + FLG))[lane] = 0; LGKM0();
  }
  unsigned fa = FLG + 4 * (lane & 3);
  pin(fa);
  SB();
  __builtin_amdgcn_s_barrier();
  if (w >= 8) { __builtin_amdgcn_s_setprio(3); kgen_fetch(a, w - 8, lane, p, j); }
  else if (w < 4) { __builtin_amdgcn_s_setprio(2); loader_entry(a, w, lane, p, j, vx); __builtin_amdgcn_s_setprio(3); }
  else if (w == 5) { __builtin_amdgcn_s_sleep(W1DELAY); dma_touch(srd(a.w1, DM * FF * 2), lane < 43 ? (b * 43 + lane) * 128 : 0x40000000); }
  SB();
  if (w < 4) {
    role_loader(a, sm, w, lane, p, j, fa, vx);
  }
  else if (w < 8) { __builtin_amdgcn_s_setprio(2); role_gemm(a, sm, w - 4, lane, p, j, fa); }
  else role_conv(a, sm, w - 8, lane, p, j, fa);
}

extern "C" void kernel_launch(void* const* d_in, const int* in_sizes, int n_in, void* d_out, int out_size, void* d_ws, size_t ws_size, hipStream_t stream) {
  Args a;
  a.x = (const u16*)d_in[0]; a.lar = (const u16*)d_in[1]; a.aim = (const u16*)d_in[2]; a.cre = (const u16*)d_in[3]; a.cim = (const u16*)d_in[4];
  a.ldt = (const u16*)d_in[5]; a.dsk = (const u16*)d_in[6]; a.w1 = (const u16*)d_in[8]; a.b1 = (const u16*)d_in[9]; a.w2 = (const u16*)d_in[10];
  a.b2 = (const u16*)d_in[11]; a.out = (u16*)d_out; a.ws = (char*)d_ws;
  k_fused<<<256, NTHR, 0, stream>>>(a);
}
```

```cpp
#include <hip/hip_runtime.h>
#include <stdint.h>
#include <type_traits>

#define LAS __attribute__((address_space(3)))
#define GAS __attribute__((address_space(1)))
#if defined(__HIP_DEVICE_COMPILE__) && __HIP_DEVICE_COMPILE__
#define NOPK __attribute__((target("no-packed-fp32-ops")))
#else
#define NOPK
#endif
#define DEV __device__ __forceinline__ NOPK
#define LAM __attribute__((always_inline)) NOPK
typedef unsigned short u16;
typedef unsigned long long u64;
typedef unsigned v4u __attribute__((ext_vector_type(4)));
typedef unsigned v2u __attribute__((ext_vector_type(2)));
typedef unsigned v2u4 __attribute__((ext_vector_type(2), aligned(4)));
typedef __bf16 bf16x8 __attribute__((ext_vector_type(8)));
typedef _Float16 f16x8 __attribute__((ext_vector_type(8)));
typedef float f32x4 __attribute__((ext_vector_type(4)));
typedef short s16x4 __attribute__((ext_vector_type(4)));
typedef short s16x2 __attribute__((ext_vector_type(2)));
typedef __amdgpu_buffer_rsrc_t rsrc_t;

constexpr int SEQ = 2048, DM = 512, NMODE = 128, FF = 1376, NKS = 43;
constexpr size_t WS_H = 0, WS_U = 6u << 20, WS_Y = 10u << 20, WS_FLAG = 14u << 20, WS_DIAG = 16u << 20;
constexpr int GF_U = 0, GF_H = 1024, GF_Y = 2048, GF_EC = 3072, GF_ER = 3200;
#define FSTR 16
#define PSLEEP 2
#define UWAIT 10
#define YDELAY 8
#define CPRIO 3
#define TPRIO 3
#define PUBAT 1
#define W2PF 12
#define LPRIO 1
#define KCPRIO 3
#define SSLEEP 4
#define IPRIO 1
#define ND 6
#define SPRIO 3
#define YGATE 7
#define ROT 0
#define SKIP6 1
#define ROTF(j) (ROT == 1 ? unit_start(j) : ROT == 2 ? ((j) & 1) * 22 : ROT == 3 ? ((j) & 3) * 11 : 0)
#define HAUX AUX_SC1
#define DW 0
#define PREP 6
#define TFIRST 1
#define NPRE 11
#define VFIRST 9
#define PUTGATE 1
#define TDELAY 4
#define W1DELAY 6
#define VD1 12
#define XPOL ""
#define PPOL ""
#define EPRIO 0
#define DSM 0
#define XB0 5
#define SL0 5
#define SLN 1
#define NOWN 0
#define YAT 18
#define LPRIO2 1
#define PF_W
#define PF_CODE
#define HDELAY 3
constexpr int NTHR = 768;
constexpr int POLL_CAP = 1 << 14;
constexpr int AUX_SC1 = 16;

struct Args {
  const u16 *x, *w1, *lar, *aim, *cre, *cim, *ldt, *dsk, *b1, *w2, *b2;
  u16* out;
  char* ws;
};

#define TR_DECL
#define TR(i)
#define TR_DUMP(base, n)

template <int I, int N, class Fn> DEV void static_for(Fn&& f) {
  if constexpr (I < N) { f(std::integral_constant<int, I>{}); static_for<I + 1, N>(f); }
}
#define CE(x) (std::integral_constant<int, (x)>::value)
template <class T> DEV void pin(T& v) { asm volatile("" : "+v"(v)); }
template <class T> DEV void pins(T& v) { asm volatile("" : "+s"(v)); }
DEV float u2f(unsigned u) { return __builtin_bit_cast(float, u); }
DEV unsigned f2u(float f) { return __builtin_bit_cast(unsigned, f); }
DEV bool wany(bool c) { return __builtin_amdgcn_ballot_w64(c) != 0; }
DEV rsrc_t mk_rsrc(const void* p, int bytes) { return __builtin_amdgcn_make_buffer_rsrc((void*)p, 0, bytes, 0x00020000); }
DEV float bf_lo(unsigned u) { return u2f(u << 16); }
DEV float bf_hi(unsigned u) { return u2f(u & 0xffff0000u); }
typedef float f32x2 __attribute__((ext_vector_type(2)));
typedef __bf16 bf16x2 __attribute__((ext_vector_type(2)));
typedef _Float16 f16x2 __attribute__((ext_vector_type(2)));
DEV unsigned pk_bf(float a, float b) { return __builtin_bit_cast(unsigned, __builtin_convertvector(f32x2{a, b}, bf16x2)); }
DEV unsigned pk_h(float a, float b) { return __builtin_bit_cast(unsigned, __builtin_convertvector(f32x2{a, b}, f16x2)); }
DEV unsigned pk_hn(float a, float b) { return pk_h(a, -b); }
DEV float rbf(float a) { return u2f(pk_bf(0.f, a)); }
DEV unsigned relu_pk(unsigned v) { return __builtin_bit_cast(unsigned, __builtin_elementwise_max(__builtin_bit_cast(s16x2, v), s16x2{0, 0})); }
DEV v2u tr_read(LAS char* p) { return __builtin_bit_cast(v2u, __builtin_amdgcn_ds_read_tr16_b64_v4i16((LAS s16x4*)p)); }
DEV v2u ld2(LAS char* p) { return *(LAS v2u4*)p; }
DEV float xsum16(float v) { auto r = __builtin_amdgcn_permlane16_swap(f2u(v), f2u(v), false, false); return u2f(r[0]) + u2f(r[1]); }
DEV float xsum32(float v) { auto r = __builtin_amdgcn_permlane32_swap(f2u(v), f2u(v), false, false); return u2f(r[0]) + u2f(r[1]); }
template <int CTRL> DEV float dpp_add(float v) { return v + u2f((unsigned)__builtin_amdgcn_update_dpp(0, (int)f2u(v), CTRL, 0xf, 0xf, false)); }
DEV float wave_sum(float v) { v = dpp_add<0x121>(v); v = dpp_add<0x122>(v); v = dpp_add<0x124>(v); v = dpp_add<0x128>(v); return xsum32(xsum16(v)); }
DEV f32x4 sel4(int c, f32x4 a, f32x4 b) { return f32x4{c ? a[0] : b[0], c ? a[1] : b[1], c ? a[2] : b[2], c ? a[3] : b[3]}; }
DEV LAS char* opq(LAS char* p) { unsigned v = (unsigned)(size_t)p; pin(v); return (LAS char*)(size_t)v; }
DEV f32x4 zero4() { f32x4 z = {0, 0, 0, 0}; pin(z); return z; }
#define VMCNT(n) asm volatile("s_waitcnt vmcnt(%0)" ::"i"(n) : "memory")
#define LGKM0() asm volatile("s_waitcnt lgkmcnt(0)" ::: "memory")
#define CFENCE() asm volatile("" ::: "memory")
#define SB() __builtin_amdgcn_sched_barrier(0)
DEV void mfma_bf(f32x4& c, v4u a, v4u b) { asm volatile("v_mfma_f32_16x16x32_bf16 %0, %1, %2, %0" : "+v"(c) : "v"(a), "v"(b)); }
DEV void mfma_bf0(f32x4& d, v4u a, v4u b) { asm volatile("v_mfma_f32_16x16x32_bf16 %0, %1, %2, 0" : "=&v"(d) : "v"(a), "v"(b)); }
DEV void mfma_bf16k(f32x4& c, v2u a, v2u b) { asm volatile("v_mfma_f32_16x16x16_bf16 %0, %1, %2, %0" : "+v"(c) : "v"(a), "v"(b)); }
DEV void mfma_h(f32x4& c, v4u a, v4u b) { asm volatile("v_mfma_f32_16x16x32_f16 %0, %1, %2, %0" : "+v"(c) : "v"(a), "v"(b)); }
#define NOP2() do { SB(); asm volatile("s_nop 1"); SB(); } while (0)
#define MFMA_DONE() do { SB(); asm volatile("s_nop 7"); SB(); } while (0)

DEV void st_sc1(v4u v, rsrc_t r, int voff, int soff) { __builtin_amdgcn_raw_buffer_store_b128(v, r, voff, soff, AUX_SC1); NOP2(); }
DEV void lds_put(unsigned addr, unsigned v) { asm volatile("s_mov_b64 exec, 1\n\tds_write_b32 %0, %1\n\ts_mov_b64 exec, -1" ::"v"(addr), "v"(v) : "memory"); }
template <int OFF, int WANT> DEV void lds_wait(unsigned fa) {
  unsigned t, n;
  asm volatile("s_mov_b32 %1, 0x20000\n"
               "1:\n\tds_read_b32 %0, %2 offset:%3\n\ts_waitcnt lgkmcnt(0)\n\tv_cmp_gt_u32 vcc, %4, %0\n\ts_cbranch_vccz 2f\n\t"
               "s_sleep 1\n\ts_sub_u32 %1, %1, 1\n\ts_cbranch_scc0 1b\n2:"
               : "=&v"(t), "=&s"(n) : "v"(fa), "i"(OFF), "i"(WANT) : "memory", "vcc", "scc");
}
template <int OFF> DEV void lds_wait_s(unsigned fa, int want) {
  unsigned t, n;
  asm volatile("s_mov_b32 %1, 0x20000\n"
               "1:\n\tds_read_b32 %0, %2 offset:%3\n\ts_waitcnt lgkmcnt(0)\n\tv_cmp_gt_u32 vcc, %4, %0\n\ts_cbranch_vccz 2f\n\t"
               "s_sleep 1\n\ts_sub_u32 %1, %1, 1\n\ts_cbranch_scc0 1b\n2:"
               : "=&v"(t), "=&s"(n) : "v"(fa), "i"(OFF), "s"(want) : "memory", "vcc", "scc");
}
static __device__ const unsigned kSeq[64] = {0,1,2,3,4,5,6,7,8,9,10,11,12,13,14,15,16,17,18,19,20,21,22,23,24,25,26,27,28,29,30,31,32,
  33,34,35,36,37,38,39,40,41,42,43,44,45,46,47,48,49,50,51,52,53,54,55,56,57,58,59,60,61,62,63};
DEV v4u srd(const void* p, unsigned bytes) { const u64 v = (u64)p; return v4u{(unsigned)v, (unsigned)(v >> 32) & 0xffffu, bytes, 0x00020000u}; }
DEV void dma16(v4u rs, unsigned lds, int voff, int soff) {
  asm volatile("s_mov_b32 m0, %0\n\ts_nop 0\n\tbuffer_load_dwordx4 %1, %2, %3 offen lds" ::"s"(lds), "v"(voff), "s"(rs), "s"(soff) : "memory");
}
DEV void dma16x(v4u rs, unsigned lds, int voff, int soff) {
  asm volatile("s_mov_b32 m0, %0\n\ts_nop 0\n\tbuffer_load_dwordx4 %1, %2, %3 offen" XPOL " lds" ::"s"(lds), "v"(voff), "s"(rs), "s"(soff) : "memory");
}
template <int N> DEV void dma_flag(v4u rs, unsigned lds) {
  asm volatile("s_mov_b32 m0, %0\n\ts_mov_b64 exec, 1\n\tbuffer_load_dword off, %1, %2 lds\n\ts_mov_b64 exec, -1" ::"s"(lds), "s"(rs), "s"(N * 4) : "memory");
}
DEV void dma_ep(v4u rs, unsigned lds, int voff) {
  asm volatile("s_mov_b32 m0, %0\n\ts_mov_b64 exec, 3\n\tbuffer_load_dword %2, %1, 0 offen sc1 lds\n\ts_mov_b64 exec, -1" ::"s"(lds), "s"(rs), "v"(voff) : "memory");
}
constexpr int CODE_BYTES = 29 * 1024, L_JUNK = 163840 - 512, L_B2 = 163840 - 768;
DEV void dma_touch(v4u rs, int voff) {
  asm volatile("s_mov_b32 m0, %0\n\ts_nop 0\n\tbuffer_load_dword %2, %1, 0 offen lds" ::"s"(L_JUNK), "s"(rs), "v"(voff) : "memory");
}
DEV void dma_b2(v4u rs, int voff) {
  asm volatile("s_mov_b32 m0, %0\n\ts_mov_b64 exec, 0xffffffff\n\tbuffer_load_dword %2, %1, 0 offen lds\n\ts_mov_b64 exec, -1" ::"s"(L_B2), "s"(rs), "v"(voff) : "memory");
}
DEV void dma_touch1(v4u rs, int soff) {
  asm volatile("s_mov_b32 m0, %0\n\ts_mov_b64 exec, 1\n\tbuffer_load_dword off, %1, %2 lds\n\ts_mov_b64 exec, -1" ::"s"(L_JUNK), "s"(rs), "s"(soff) : "memory");
}
template <int OFF> DEV void put_tid(unsigned v) { asm volatile("ds_write_addtid_b32 %0 offset:%1" ::"v"(v), "i"(OFF) : "memory"); }
DEV void set_m0(unsigned v) { asm volatile("s_mov_b32 m0, %0\n\ts_nop 0" ::"s"(v) : "memory"); }
DEV GAS u64* gflag(const Args& a, int line) { return (GAS u64*)(a.ws + WS_FLAG) + line * FSTR; }
DEV void publish(GAS u64* f, u64 tag, int lane) {
  VMCNT(0);
  if (lane == 0) __hip_atomic_store(f, tag, __ATOMIC_RELAXED, __HIP_MEMORY_SCOPE_AGENT);
}
template <int N> DEV void flag_store(GAS u64* f, v2u tag) {
  int z = 0;
  asm volatile("s_waitcnt vmcnt(%3)\n\ts_mov_b64 exec, 1\n\tglobal_store_dwordx2 %0, %1, %2 sc1\n\ts_mov_b64 exec, -1" ::"v"(z), "v"(tag), "s"(f), "i"(N) : "memory");
}
DEV void poll(GAS u64* p, u64 tag) {
#pragma nounroll
  for (int it = 0; it < POLL_CAP; it++) {
    if (!wany(__hip_atomic_load(p, __ATOMIC_RELAXED, __HIP_MEMORY_SCOPE_AGENT) != tag)) break;
    __builtin_amdgcn_s_sleep(PSLEEP);
  }
  __builtin_amdgcn_fence(__ATOMIC_ACQUIRE, "wavefront");
}
DEV void poll2(GAS u64* p, GAS u64* q, u64 tag) {
#pragma nounroll
  for (int it = 0; it < POLL_CAP; it++) {
    const u64 x = __hip_atomic_load(p, __ATOMIC_RELAXED, __HIP_MEMORY_SCOPE_AGENT), y = __hip_atomic_load(q, __ATOMIC_RELAXED, __HIP_MEMORY_SCOPE_AGENT);
    if (!wany(x != tag || y != tag)) break;
    __builtin_amdgcn_s_sleep(PSLEEP);
  }
  __builtin_amdgcn_fence(__ATOMIC_ACQUIRE, "wavefront");
}
DEV int unit_start(int j) { return (j * 37 + 3) / 7; }

constexpr int KCOPY = 4160, C_U = 2 * KCOPY, CH = C_U + 4608, TABB = 4352, L_STAT = 31744;
constexpr int L_X = 32768, L_RING = L_X + 65536, L_TAB = L_RING + 24576;
constexpr int L_Y = 0, YROW = 160, L_EX = L_Y + 64 * YROW, L_YS = L_EX + 16384;
constexpr int LDS_BYTES = 163840, FLG = LDS_BYTES - 256;
static_assert(4 * TABB <= 36864 && 2 * CH <= L_STAT && L_YS + 256 <= L_STAT && L_RING + 61440 <= FLG, "lds");
constexpr int F_FULL = 0, F_FREE = 16, F_CW = 32, F_GX = 48, F_EP = 64, F_ST = 80, F_HOK = 96, F_GP = 112;

constexpr int NSTEP1 = 24;
#define KINDS 0, 0, 0, 0, 0, 0, 0, 0, 1, 1, 1, 1, 1, 1, 1, 1, 1, 1, 1, 1, 1, 1, 1, 1
constexpr int ST_KIND[NSTEP1] = {KINDS};
constexpr int st_idx(int s) { int n = 0; for (int i = 0; i < s; i++) n += ST_KIND[i] == ST_KIND[s]; return n; }
constexpr int step_of(int kind, int idx) { for (int s = 0; s < NSTEP1; s++) if (ST_KIND[s] == kind && st_idx(s) == idx) return s; return -1; }
constexpr int need_X(int c) { return step_of(0, c) + 1; }
constexpr int need_W(int u) { return step_of(1, u) + 1; }
constexpr int need_V(int t) { return NSTEP1 + t + 1; }
static_assert(need_X(0) < need_W(0) && need_X(1) < need_W(2) && need_X(2) < need_W(4) && need_X(3) < need_W(6) && need_X(7) < need_W(14), "x before its units");
constexpr int w_addr(int u) { return u % 7 < 5 ? L_RING + (u % 7) * 12288 : (u % 7 - 5) * 12288; }
constexpr int x_addr(int c) { return L_X + ((c + 1) & 7) * 8192; }
constexpr int FREE_C = 14;
constexpr int NVB = 14, L_V = L_X + 8192, VH = 7;
#define PD 7
#define VDEPTH 8

DEV int kstep(int v, int par, int us) { return v < NOWN ? us + v + par : min(v + par - NOWN < us ? v + par - NOWN : v + par, NKS - 1); }
#define NENTRY 4
DEV void x_step(v4u rx, const int (&vx)[2], int sx, int idx, int l, int j, int lds) {
  const int so = sx + ((idx + j + 1) & 7) * 128;
#pragma unroll
  for (int i = 0; i < 2; i++) dma16x(rx, lds + l * 2048 + i * 1024, vx[i], so);
}
DEV void loader_entry(const Args& a, int l, int lane, int p, int j, int (&vx)[2]) {
  vx[0] = (lane >> 3) * 1024 + (((lane & 7) ^ (lane >> 4)) * 16); vx[1] = (vx[0] ^ 64) + 8192;
  const v4u rx = srd(a.x, SEQ * DM * 2);
  static_for<0, NENTRY>([&](auto S) LAM { constexpr int s = S; x_step(rx, vx, p * 65536 + l * 16384, s, l, j, CE(x_addr(s))); });
}
DEV void role_loader(const Args& a, LAS char* sm, int l, int lane, int p, int j, unsigned fa, const int (&vx)[2]) {
  const v4u rx = srd(a.x, SEQ * DM * 2), rw = srd(a.w1, DM * FF * 2), rv = srd(a.w2, DM * FF * 2), rq = srd(kSeq, 256);
  int vw[3], vt, vv;
  const int sx = p * 65536 + l * 16384, sw = unit_start(j) * 64;
  const int st = (((int)blockIdx.x >> 3) * 43 + l) * 1024, sc = ((3 - (int)((size_t)a.w2 >> 7)) & 7) * 128;
  TR_DECL; TR(0);
  if (l == 3) dma_b2(srd(a.b2 + 64 * j, 128), lane * 4);
  if (l < 2) dma_ep(srd(a.ws + WS_FLAG + (size_t)(l ? GF_ER + 16 * p : GF_EC + 16 * j) * 8 * FSTR, 8), FLG + F_EP + 8 * l, lane * 4);
  static_for<NENTRY, NSTEP1>([&](auto S) LAM {
    constexpr int s = S, kind = ST_KIND[s], idx = st_idx(s);
    if constexpr (s % 4 == 0) TR(s / 4);
    if constexpr (s == W2PF) { if (l == 3) dma_touch(rv, vt); }
    if constexpr (s == PREP) {
      const int nown = SKIP6 ? (j == 7 ? NKS : unit_start(j + 1)) - unit_start(j) : 6;
#pragma unroll
      for (int n = 0; n < 3; n++) {
        const int sg = 16 * (3 * l + n) + (lane >> 2), r = (sg * 171) >> 10, qp = sg - 6 * r, q = qp ^ ((r >> 1) & 1);
        vw[n] = q < nown ? r * (FF * 2) + q * 64 + (((lane & 3) ^ (2 * ((r >> 3) & 1))) * 16) : 0x40000000;
        pin(vw[n]);
      }
      vt = lane < 43 ? (blockIdx.x * 43 + lane) * 128 : 0x40000000;
      vv = (8 * l + (lane >> 3)) * 1024 + (((((lane & 7) >> 1) ^ (((lane >> 4) & 1) | ((l & 1) << 1))) * 2 + (lane & 1)) * 16) + j * 128;
      pin(vt); pin(vv);
      __builtin_amdgcn_s_setprio(LPRIO);
    }
    if constexpr (kind == 0) {
      x_step(rx, vx, sx, idx, l, j, CE(x_addr(idx)));
    } else {
      if constexpr (idx >= 7) lds_wait<F_FREE, idx - 6>(fa);
      const int so = sw + ((((idx >> 1) + j + 1) & 7) * 64 + (idx & 1) * 32) * (FF * 2);
#pragma unroll
      for (int n = 0; n < 3; n++) {
        dma16(rw, CE(w_addr(idx)) + l * 3072 + n * 1024, vw[n], so);
      }
    }
    if constexpr (kind == 1 || idx == XB0 - 1 || idx == 7) dma_flag<s + 1>(rq, FLG + F_FULL + 4 * l);
    if constexpr (kind == 1 && idx >= SL0) {
#pragma unroll
      for (int q = 0; q < SLN; q++) if ((idx - SL0) * SLN + q < 11) dma_touch1(rv, min(st + ((idx - SL0) * SLN + q) * 4096, 1375 * 1024) + sc);
    }
  });
  TR(6);
  const int rot = unit_start(j);
  const rsrc_t rv2 = mk_rsrc(a.w2, DM * FF * 2);
  v4u wr[22][2];
  LAS char* wa[2] = {opq(sm + L_V + l * 1024 + lane * 16), opq(sm + L_V + VH * 8192 + l * 1024 + lane * 16)};
  volatile LAS unsigned* flw = (volatile LAS unsigned*)opq(sm + (lane ? L_JUNK + lane * 4 : FLG + F_FULL + 4 * l));
  auto get = [&](auto TT) LAM {
    constexpr int t = TT;
#pragma unroll
    for (int hf = 0; hf < (t < 21 ? 2 : 1); hf++) wr[t][hf] = __builtin_amdgcn_raw_buffer_load_b128(rv2, vv, kstep(2 * t, hf, rot) * 32768, 0);
    SB(); VMCNT(t < NPRE ? VD1 : VDEPTH);
  };
  auto put = [&](auto TT) LAM {
    constexpr int t = TT, q = t % NVB;
    if constexpr (t == NVB || (t > NVB + 2 && (t - NVB) % 2 == 1)) lds_wait<F_FREE, t == NVB ? 19 : 18 + t - NVB>(fa);
    if constexpr (t == NVB) __builtin_amdgcn_s_setprio(LPRIO2);
#pragma unroll
    for (int hf = 0; hf < (t < 21 ? 2 : 1); hf++) *(LAS v4u*)(wa[q / VH] + (q % VH) * 8192 + hf * 4096) = wr[t][hf];
    if constexpr ((t & 1) && t >= VFIRST) { CFENCE(); *flw = CE(need_V(t)); }
    SB();
  };
  static_for<0, NPRE>(get);
  TR(7);
  lds_wait<F_FREE, 16>(fa);
  lds_wait<F_CW, PUTGATE>(fa);
  static_for<0, NPRE>(put);
  TR(8);
  lds_wait<F_GP, 1>(fa);
  TR(9);
  static_for<NPRE, 22>(get);
  TR(11);
  static_for<NPRE, 22>(put);
  TR(10);
  if (l == 0) TR_DUMP(0, 12);
}

DEV u64 tag_of(LAS char* sm, int which) {
  const v2u e = *(LAS v2u*)(sm + FLG + F_EP + 8 * which);
  const u64 v = ((u64)(unsigned)__builtin_amdgcn_readfirstlane(e.y) << 32) | (unsigned)__builtin_amdgcn_readfirstlane(e.x);
  return (v + 1) * 0x9E3779B97F4A7C15ull;
}

struct cpx { float r, i; };
DEV cpx cmul(cpx a, cpx b) { return cpx{__builtin_fmaf(a.r, b.r, -a.i * b.i), __builtin_fmaf(a.r, b.i, a.i * b.r)}; }
DEV cpx cexp_p(float zr2, float zi2, float pos) {
  const float e = __builtin_amdgcn_exp2f(zr2 * pos), th = __builtin_amdgcn_fractf(zi2 * pos);
  return cpx{e * __builtin_amdgcn_cosf(th), e * __builtin_amdgcn_sinf(th)};
}
DEV float exp_bf(float v) { return rbf(__builtin_amdgcn_exp2f(v * 1.44269504088896f)); }
DEV void pow2_scale(float mx, float& s, float& inv) {
  const int e = min(max((int)(f2u(mx) >> 23), 15), 254);
  s = u2f((unsigned)(268 - e) << 23);
  inv = u2f((unsigned)(e - 14) << 23);
}

DEV void mfma_h0(f32x4& d, v4u a, v4u b) { asm volatile("v_mfma_f32_16x16x32_f16 %0, %1, %2, 0" : "=&v"(d) : "v"(a), "v"(b)); }
DEV cpx csq(cpx a) { return cpx{__builtin_fmaf(a.r, a.r, -a.i * a.i), 2.f * a.r * a.i}; }
DEV void cmul_h(unsigned re, unsigned im, unsigned m, unsigned& ore, unsigned& oim) {
  const f16x2 r = __builtin_bit_cast(f16x2, re), i = __builtin_bit_cast(f16x2, im), mm = __builtin_bit_cast(f16x2, m);
  const f16x2 mr = {mm.x, mm.x}, mi = {mm.y, mm.y};
  ore = __builtin_bit_cast(unsigned, __builtin_elementwise_fma(r, mr, -(i * mi)));
  oim = __builtin_bit_cast(unsigned, __builtin_elementwise_fma(r, mi, i * mr));
}

struct KIn { unsigned lar, aim, cre, cim, ldt, dsk; };
DEV int c_priv(int cw) { return cw < 3 ? 24576 + cw * 2048 : L_RING + 61440; }
static_assert(24576 + 3 * 2048 <= L_STAT && L_RING + 61440 + 2048 <= L_B2, "lds");
DEV void dma4(const void* base, unsigned lds, int voff) {
  asm volatile("s_mov_b32 m0, %0\n\ts_nop 0\n\tglobal_load_lds_dword %1, %2" PPOL ::"s"(lds), "v"(voff), "s"(base) : "memory");
}
DEV void kgen_fetch(const Args& a, int cw, int lane, int p, int j) {
  const int d = 64 * j + 2 * p + (cw >> 1), vo = d * 256 + lane * 4, lds = c_priv(cw);
  dma4(a.lar, lds, vo); dma4(a.aim, lds + 256, vo); dma4(a.cre, lds + 512, vo); dma4(a.cim, lds + 768, vo);
  int vs = (d >> 1) * 4; pin(vs);
  asm volatile("s_mov_b64 exec, 1" ::: "memory");
  dma4(a.ldt, lds + 1024, vs); dma4(a.dsk, lds + 1028, vs);
  asm volatile("s_mov_b64 exec, -1" ::: "memory");
}
DEV KIn kgen_read(LAS char* sm, int cw, int lane) {
  LAS char* q = sm + c_priv(cw);
  VMCNT(0);
  KIn k;
  k.lar = *(LAS unsigned*)(q + lane * 4); k.aim = *(LAS unsigned*)(q + 256 + lane * 4); k.cre = *(LAS unsigned*)(q + 512 + lane * 4); k.cim = *(LAS unsigned*)(q + 768 + lane * 4);
  k.ldt = *(LAS u16*)(q + 1024 + (cw & 2)); k.dsk = *(LAS u16*)(q + 1028 + (cw & 2));
  return k;
}
DEV cpx conj(cpx a) { return cpx{a.r, -a.i}; }
DEV void kgen_factors(KIn k, int cw, int lane, v4u (&f)[2][2][4], float& kinv, float& kd) {
  const int sh = (cw & 1) ? 0 : 16;
  const float ldt = bf_lo(k.ldt), dsk = bf_lo(k.dsk);
  const float dt = exp_bf(ldt);
  const float zr2 = -exp_bf(bf_hi(k.lar << sh)) * dt * 1.44269504088896f, zi2 = bf_hi(k.aim << sh) * dt * 0.159154943091895f;
  const cpx E1 = cexp_p(zr2, zi2, 1.f), E2 = csq(E1);
  {
    const cpx E8 = csq(csq(E2)), M = cexp_p(zr2, zi2, 32.f);
    cpx S[8];
    S[0] = cpx{1.f, 0.f}; S[1] = E8; S[2] = csq(E8); S[3] = cmul(S[2], E8); S[4] = M;
#pragma unroll
    for (int i = 1; i < 4; i++) S[4 + i] = cmul(S[i], M);
    const cpx M2 = csq(M), M4 = csq(M2), M6 = cmul(M2, M4);
    const unsigned mm[3] = {pk_h(M2.r, M2.i), pk_h(M4.r, M4.i), pk_h(M6.r, M6.i)};
#pragma unroll
    for (int e = 0; e < 4; e++) { f[1][0][0][e] = pk_h(S[2 * e].r, S[2 * e + 1].r); f[1][1][0][e] = pk_h(S[2 * e].i, S[2 * e + 1].i); }
#pragma unroll
    for (int q = 1; q < 4; q++)
#pragma unroll
      for (int e = 0; e < 4; e++) { unsigned re, im; cmul_h(f[1][0][0][e], f[1][1][0][e], mm[q - 1], re, im); f[1][0][q][e] = re; f[1][1][q][e] = im; }
  }
  float ks;
  {
    const float csum = wave_sum((__builtin_fabsf(bf_lo(k.cre)) + __builtin_fabsf(bf_hi(k.cre))) + (__builtin_fabsf(bf_lo(k.cim)) + __builtin_fabsf(bf_hi(k.cim))));
    pow2_scale(__builtin_fmaf(2.f * dt, csum, __builtin_fabsf(dsk)), ks, kinv);
    const float cs = 2.f * dt * ks;
    const cpx M = cexp_p(zr2, -zi2, 256.f);
    cpx S[8];
    S[0] = cpx{bf_hi(k.cre << sh) * cs, bf_hi(k.cim << sh) * -cs};
#pragma unroll
    for (int i = 1; i < 4; i++) S[i] = cmul(S[i - 1], conj(E2));
#pragma unroll
    for (int i = 0; i < 4; i++) S[4 + i] = cmul(S[i], M);
    const cpx M2 = csq(M), M4 = csq(M2), M6 = cmul(M2, M4), M8 = csq(M4);
    const unsigned m2 = pk_h(M2.r, M2.i), m4 = pk_h(M4.r, M4.i), m6 = pk_h(M6.r, M6.i), m1 = pk_h(E1.r, -E1.i), m8 = pk_h(M8.r, M8.i);
#pragma unroll
    for (int e = 0; e < 4; e++) { f[0][0][0][e] = pk_h(S[2 * e].r, S[2 * e + 1].r); f[0][1][0][e] = pk_h(S[2 * e].i, S[2 * e + 1].i); }
#pragma unroll
    for (int q = 1; q < 4; q++)
#pragma unroll
      for (int e = 0; e < 4; e++) {
        const int src = q >= 2 && (e & 1) ? e >> 1 : e;
        unsigned re = 0, im = 0;
        if (q < 3 || e < 3) cmul_h(f[0][0][0][src], f[0][1][0][src], q == 1 ? m2 : q == 2 ? ((e & 1) ? m1 : m4) : ((e & 1) ? m8 : m6), re, im);
        f[0][0][q][e] = re; f[0][1][q][e] = im;
      }
  }
#pragma unroll
  for (int q = 0; q < 16; q++) pin(f[q >> 3][(q >> 2) & 1][q & 3]);
  kd = ((cw & 1) | lane) == 0 ? dsk * ks : 0.f;
  pin(kinv); pin(kd);
}
DEV void kgen_table(const v4u (&f)[2][2][4], v4u oh, int lane, float kd, f32x4 (&kt)[2][2]) {
  v4u sel[2][2];
  bool oct[8];
#pragma unroll
  for (int q = 0; q < 8; q++) oct[q] = (lane >> 3) == q;
#pragma unroll
  for (int e = 0; e < 4; e++) { sel[0][0][e] = oct[0] ? oh[e] : 0u; sel[0][1][e] = oct[1] ? oh[e] : 0u; }
  pin(sel[0][0]); pin(sel[0][1]);
#pragma unroll
  for (int i = 0; i < 4; i++) kt[i >> 1][i & 1] = zero4();
  kt[0][0][0] = kd; pin(kt[0][0]);
  NOP2();
  f32x4 t[2][2][2];
  v4u o[2][2];
  static_for<0, 9>([&](auto H) LAM {
    constexpr int h = H, y = h & 1, gs = h >> 1, py = y ^ 1;
    static_for<0, 8>([&](auto I) LAM {
      constexpr int i = I;
      SB();
      if constexpr (h < 8) {
        if constexpr (i < 4) mfma_h0(t[y][i >> 1][i & 1], f[y][i & 1][2 * (i >> 1)], sel[gs & 1][0]);
        else mfma_h(t[y][(i - 4) >> 1][i & 1], f[y][i & 1][2 * ((i - 4) >> 1) + 1], sel[gs & 1][1]);
      } else if constexpr (i < 4) asm volatile("s_nop 1");
      SB();
      if constexpr (y == 1 && gs < 3 && i < 4) { sel[(gs + 1) & 1][0][i] = oct[2 * gs + 2] ? oh[i] : 0u; sel[(gs + 1) & 1][1][i] = oct[2 * gs + 3] ? oh[i] : 0u; }
      if constexpr (y == 1 && i == 4 && gs < 3) { pin(sel[(gs + 1) & 1][0]); pin(sel[(gs + 1) & 1][1]); }
      if constexpr (h > 0 && i >= 4) {
        const f32x4 v = t[py][(i - 4) >> 1][i & 1];
        o[py][(i - 4) >> 1][2 * (i & 1)] = pk_h(v[0], v[1]); o[py][(i - 4) >> 1][2 * (i & 1) + 1] = pk_h(v[2], v[3]);
      }
    });
    if constexpr (h > 0 && y == 0) {
      pin(o[0][0]); pin(o[0][1]); pin(o[1][0]); pin(o[1][1]);
      NOP2();
#pragma unroll
      for (int i = 0; i < 4; i++) mfma_h(kt[i >> 1][i & 1], o[0][i >> 1], o[1][i & 1]);
    }
  });
  MFMA_DONE();
}
constexpr unsigned TMASK = 0x00070007u;
DEV v4u seal(v4u v, v4u t) { for (int e = 0; e < 4; e++) v[e] = ((v[e] + 0x00040004u) & ~TMASK) | t[e]; return v; }
DEV unsigned unseal(v4u& v, v4u t) { v ^= t; return (v.x | v.y | v.z | v.w) & TMASK; }

#define TA1 "s_load_dword %0, %1, 0x0\n\t" "s_load_dword %0, %1, 0x80\n\t" "s_load_dword %0, %1, 0x100\n\t" "s_load_dword %0, %1, 0x180\n\t" "s_load_dword %0, %1, 0x200\n\t" "s_load_dword %0, %1, 0x280\n\t" "s_load_dword %0, %1, 0x300\n\t" "s_load_dword %0, %1, 0x380\n\t" "s_load_dword %0, %1, 0x400\n\t" "s_load_dword %0, %1, 0x480\n\t" "s_load_dword %0, %1, 0x500\n\t"
#define TA2 "s_load_dword %0, %1, 0x0\n\t" "s_load_dword %0, %1, 0x40\n\t" "s_load_dword %0, %1, 0x80\n\t" "s_load_dword %0, %1, 0xc0\n\t" "s_load_dword %0, %1, 0x100\n\t" "s_load_dword %0, %1, 0x140\n\t" "s_load_dword %0, %1, 0x180\n\t" "s_load_dword %0, %1, 0x1c0\n\t" "s_load_dword %0, %1, 0x200\n\t" "s_load_dword %0, %1, 0x240\n\t" "s_load_dword %0, %1, 0x280\n\t" "s_load_dword %0, %1, 0x2c0\n\t" "s_load_dword %0, %1, 0x300\n\t" "s_load_dword %0, %1, 0x340\n\t" "s_load_dword %0, %1, 0x380\n\t" "s_load_dword %0, %1, 0x3c0\n\t" "s_load_dword %0, %1, 0x400\n\t" "s_load_dword %0, %1, 0x440\n\t" "s_load_dword %0, %1, 0x480\n\t" "s_load_dword %0, %1, 0x4c0\n\t" "s_load_dword %0, %1, 0x500\n\t" "s_load_dword %0, %1, 0x540\n\t"
#define TB1 "s_load_dword %0, %2, 0x0\n\t" "s_load_dword %0, %2, 0x400\n\t" "s_load_dword %0, %2, 0x800\n\t" "s_load_dword %0, %2, 0xc00\n\t" "s_load_dword %0, %2, 0x1000\n\t" "s_load_dword %0, %2, 0x1400\n\t" "s_load_dword %0, %2, 0x1800\n\t" "s_load_dword %0, %2, 0x1c00\n\t" "s_load_dword %0, %2, 0x2000\n\t" "s_load_dword %0, %2, 0x2400\n\t" "s_load_dword %0, %2, 0x2800\n\t"
#define TB2 "s_load_dword %0, %2, 0x0\n\t" "s_load_dword %0, %2, 0x40\n\t" "s_load_dword %0, %2, 0x400\n\t" "s_load_dword %0, %2, 0x440\n\t" "s_load_dword %0, %2, 0x800\n\t" "s_load_dword %0, %2, 0x840\n\t" "s_load_dword %0, %2, 0xc00\n\t" "s_load_dword %0, %2, 0xc40\n\t" "s_load_dword %0, %2, 0x1000\n\t" "s_load_dword %0, %2, 0x1040\n\t" "s_load_dword %0, %2, 0x1400\n\t" "s_load_dword %0, %2, 0x1440\n\t" "s_load_dword %0, %2, 0x1800\n\t" "s_load_dword %0, %2, 0x1840\n\t" "s_load_dword %0, %2, 0x1c00\n\t" "s_load_dword %0, %2, 0x1c40\n\t" "s_load_dword %0, %2, 0x2000\n\t" "s_load_dword %0, %2, 0x2040\n\t" "s_load_dword %0, %2, 0x2400\n\t" "s_load_dword %0, %2, 0x2440\n\t" "s_load_dword %0, %2, 0x2800\n\t" "s_load_dword %0, %2, 0x2840\n\t"
DEV void role_gemm(const Args& a, LAS char* sm, int w, int lane, int p, int j, unsigned fa) {
  const int g = lane >> 4, tok = lane & 15, q4 = tok >> 2, pp = lane & 3;
  const v4u rq = srd(kSeq, 256);
  const unsigned fre = FLG + F_FREE + 4 * w;
  volatile LAS unsigned* full = (volatile LAS unsigned*)(size_t)(fa + F_FULL);
  const rsrc_t rh = mk_rsrc(a.ws + WS_H, 32 * NKS * 4096);
  const int ks = w >> 1, ah = w & 1;
  TR_DECL; TR(0);
  u64 tagr;
  v2u tgv;
  GAS u64* fh = gflag(a, GF_H + (p * 8 + j) * 4 + w); pins(fh);
  {
    const int us = unit_start(j), nown = (j == 7 ? NKS : unit_start(j + 1)) - us;
    unsigned bias[3];
#pragma unroll
    for (int i = 0; i < 3; i++) {
      bias[i] = *(const GAS u16*)(a.b1 + us * 32 + (3 * w + i) * 16 + tok);
    }
    LAS char* wb[2][3];
#pragma unroll
    for (int i = 0; i < 3; i++) {
      const int ft = 3 * w + i;
      wb[0][i] = opq(sm + (6 * (8 * g + q4) + ((ft >> 1) ^ ((q4 >> 1) & 1))) * 64 + (((ft & 1) ^ (g & 1)) * 32) + 8 * pp);
      wb[1][i] = opq(wb[0][i] + L_RING);
    }
    LAS char* xb[2];
#pragma unroll
    for (int s = 0; s < 2; s++) xb[s] = opq(sm + L_X + tok * 128 + (((4 * s + g) ^ ((tok >> 1) & 7)) * 16));
    int hoffv = (g & 1) * 1024 + (g >> 1) * 256 + tok * 16; pin(hoffv);
    f32x4 acc[3][4], cs[3];
#pragma unroll
    for (int i = 0; i < 3; i++) { cs[i] = zero4();
#pragma unroll
      for (int T = 0; T < 4; T++) acc[i][T] = zero4(); }
    v4u ones = {0x3f803f80u, 0x3f803f80u, 0x3f803f80u, 0x3f803f80u};
    pin(ones);
#pragma unroll
    for (int i = 0; i < 3; i++) { bias[i] = g ? 0u : bias[i] << 16; pin(bias[i]); }
    v2u wfr[2][3][2]; v4u xfr[2][4];
    auto rd = [&](auto U, auto K) LAM {
      constexpr int u = U, k = K, wa = w_addr(u), hi = wa >= L_RING;
      if constexpr (k >= 2 && k < 6) xfr[u & 1][k - 2] = *(LAS v4u*)(xb[u & 1] + (CE(x_addr(u >> 1)) - L_X) + (k - 2) * 2048);
      else { constexpr int i = k < 2 ? 0 : (k - 4) >> 1; wfr[u & 1][i][k & 1] = tr_read(wb[hi][i] + (wa - hi * L_RING) + (k & 1) * 1536); }
    };
    lds_wait<F_FULL, need_W(0)>(fa);
    TR(1);
    static_for<0, 10>([&](auto K) LAM { rd(std::integral_constant<int, 0>{}, K); });
    tagr = tag_of(sm, 1); pins(tagr);
    tgv = v2u{(unsigned)tagr, (unsigned)(tagr >> 32)}; pin(tgv);
    lds_wait<F_FULL, need_W(1)>(fa);
    if constexpr (DSM & 1) lds_put(fre, 1); else dma_flag<1>(rq, fre);
    int vo[3], so[3];
#pragma unroll
    for (int i = 0; i < 3; i++) {
      const int ft = 3 * w + i;
      so[i] = (p * NKS + us + (ft >> 1)) * 4096 + (ft & 1) * 512; pins(so[i]);
    }
    LAS char* sr = opq(sm + L_STAT + tok * 16);
    static_for<0, 15>([&](auto U) LAM {
      constexpr int u = U, b = u & 1;
      if constexpr (u == 4 || u == 8 || u == 12) TR(2 + (u - 4) / 4);
      unsigned fv; bool lack = false;
      if constexpr (u < 14) fv = *full;
      static_for<0, 15>([&](auto M) LAM {
        constexpr int m = M, i = m / 5, T = m % 5;
        const v4u wv = {wfr[b][i][0].x, wfr[b][i][0].y, wfr[b][i][1].x, wfr[b][i][1].y};
        SB();
        if constexpr (T < 4) mfma_bf(acc[i][T], wv, xfr[b][T]); else mfma_bf(cs[i], ones, wv);
        SB();
        if constexpr (u < 15 && m < 10) rd(std::integral_constant<int, u + 1>{}, M);
        if constexpr (u < 14 && m == 11) lack = wany(fv < CE(need_W(u + 2)));
        if constexpr (u < 14 && m == 13) __builtin_amdgcn_s_waitcnt(0xc07f);
        if constexpr (u + 2 <= 9 && ((DSM >> (u + 1)) & 1) && m == 10) { if constexpr (!((DSM >> u) & 1)) VMCNT(0); lds_put(fre, u + 2); }
        if constexpr (u + 2 <= 9 && !((DSM >> (u + 1)) & 1) && m == 13) dma_flag<u + 2>(rq, fre);
        if constexpr (u + 2 == FREE_C && m == 10) { VMCNT(0); lds_put(fre, u + 2); }
        if constexpr (u == 14 && m == 10) lds_put(fre, 16);
        if constexpr (u == 14 && m == 11) fv = full[F_CW / 4];
        if constexpr (u == 14 && m == 14) { __builtin_amdgcn_s_waitcnt(0xc07f); lack = wany(fv < 1); }
      });
      SB();
      if constexpr (u < 14) { if (__builtin_expect(lack, 0)) lds_wait<F_FULL, need_W(u + 2)>(fa); CFENCE(); }
      else { if (__builtin_expect(lack, 0)) lds_wait<F_CW, 1>(fa); CFENCE(); }
    });
    TR(5);
    v2u sb[4], av[3];
    float cr[3][3];
    auto wv = [&](int i) LAM { return v4u{wfr[1][i][0].x, wfr[1][i][0].y, wfr[1][i][1].x, wfr[1][i][1].y}; };
    auto epi = [&](int i, int T) LAM {
      unsigned pk[2][2];
#pragma unroll
      for (int t = 0; t < 2; t++)
#pragma unroll
        for (int r = 0; r < 2; r++) {
          const float t0 = acc[i][T + t][2 * r], t1 = acc[i][T + t][2 * r + 1];
          pk[t][r] = relu_pk(pk_bf(t0 * __builtin_fabsf(t0), t1 * __builtin_fabsf(t1)));
        }
      auto r0 = __builtin_amdgcn_permlane16_swap(pk[0][0], pk[1][0], false, false);
      auto r1 = __builtin_amdgcn_permlane16_swap(pk[0][1], pk[1][1], false, false);
      st_sc1(v4u{r0[0], r1[0], r0[1], r1[1]}, rh, vo[i] + T * 1024, so[i]);
    };
    static_for<0, 27>([&](auto M) LAM {
      constexpr int m = M;
      SB();
      if constexpr (m < 3) mfma_bf(cs[m], ones, wv(m));
      else if constexpr (m < 15) mfma_bf(acc[(m - 3) >> 2][(m - 3) & 3], wv((m - 3) >> 2), xfr[1][(m - 3) & 3]);
      else mfma_bf16k(acc[(m - 15) >> 2][(m - 15) & 3], av[(m - 15) >> 2], sb[(m - 15) & 3]);
      SB();
      if constexpr (m < 4) sb[m] = *(LAS v2u*)(sr + m * 256);
      if constexpr (m < 3) { vo[m] = hoffv | (((3 * w + m) >> 1) < nown ? 0 : 0x40000000); pin(vo[m]); }
      if constexpr (m >= 5 && m < 14) {
        constexpr int i = (m - 5) / 3, k = (m - 5) % 3;
        if constexpr (k == 0) { pin(cs[i]); cr[i][0] = g ? 0.f : cs[i][0]; cr[i][1] = rbf(cr[i][0]); pin(cr[i][0]); pin(cr[i][1]); }
        if constexpr (k == 1) { cr[i][1] = cr[i][0] - cr[i][1]; cr[i][2] = cr[i][1] - rbf(cr[i][1]); pin(cr[i][1]); pin(cr[i][2]); }
        if constexpr (k == 2) { av[i] = v2u{pk_bf(cr[i][0], cr[i][1]), pk_bf(cr[i][2], u2f(bias[i]))}; pin(av[i]); }
      }
    });
    SB();
#pragma unroll
    for (int i = 0; i < 6; i++) { epi(i >> 1, 2 * (i & 1)); SB(); }
  }
  lds_put(FLG + F_GX + 4 * w, 1);
  TR(6);
  __builtin_amdgcn_s_setprio(3);
  GAS u64* pf = gflag(a, GF_H + p * 32 + (lane & 31));
  LAS char* wb[2][4];
  const int phi = ((q4 >> 1) & 1) | ((g & 1) << 1);
#pragma unroll
  for (int e = 0; e < 4; e++) { wb[0][e] = opq(sm + L_V + ks * 4096 + (8 * g + q4) * 128 + ((e ^ phi) * 32) + 8 * pp); wb[1][e] = opq(wb[0][e] + VH * 8192); }
  f32x4 acc[4][2];
#pragma unroll
  for (int e = 0; e < 4; e++) { acc[e][0] = zero4(); acc[e][1] = zero4(); }
  v2u wfr[2][4][2];
  auto rd2 = [&](auto I, auto K) LAM {
    constexpr int i = I, k = K, t = i % NVB;
    wfr[i & 1][k >> 1][k & 1] = tr_read(wb[t / VH][k >> 1] + (t % VH) * 8192 + (k & 1) * 512);
  };
  const int T = 2 * ah + ks;
  int hv0 = lane * 16 + ks * 1024, hv1 = hv0 ^ 1024;
  volatile LAS unsigned* frw = (volatile LAS unsigned*)opq(sm + (lane ? L_JUNK + lane * 4 : (int)fre));
  int hso = p * NKS * 4096 + ah * 2048, rot = unit_start(j);
  pin(hv0); pin(hv1); pin(pf); pins(hso); pins(rot);
  SB();
  TR(16);
  flag_store<0>(fh, tgv);
  lds_put(FLG + F_GP + 4 * w, 1);
  TR(7);
  v4u hf[PD][2];
  auto ldh = [&](int i, int t) LAM { hf[i % PD][t] = __builtin_amdgcn_raw_buffer_load_b128(rh, t ? hv1 : hv0, hso + kstep(2 * i, ks, rot) * 4096, HAUX); };
  float rest[4][4];
#pragma unroll
  for (int e = 0; e < 4; e++) {
    const v2u b2 = *(LAS v2u*)(sm + L_B2 + 32 * e + 8 * g);
    const v2u xv = *(LAS v2u*)(sm + L_X + (16 * T + tok) * 128 + (((2 * e + (g >> 1)) ^ ((tok >> 1) & 7)) * 16) + (g & 1) * 8);
    rest[e][0] = bf_lo(b2.x) + bf_lo(xv.x); rest[e][1] = bf_hi(b2.x) + bf_hi(xv.x); rest[e][2] = bf_lo(b2.y) + bf_lo(xv.y); rest[e][3] = bf_hi(b2.y) + bf_hi(xv.y);
#pragma unroll
    for (int r = 0; r < 4; r++) pin(rest[e][r]);
  }
  float rs2 = *(LAS float*)(sm + L_STAT + (16 * T + tok) * 16 + 8);
  pin(rs2);
  if constexpr (NOWN > 0) {
    lds_wait<F_GP, 1>(fa);
#pragma unroll
    for (int i = 0; i < NOWN / 2; i++) { ldh(i, 0); ldh(i, 1); }
    SB();
  }
  u64 first = 0;
  TR(18);
  if (w == 0) { __builtin_amdgcn_s_sleep(HDELAY); first = __hip_atomic_load(pf, __ATOMIC_RELAXED, __HIP_MEMORY_SCOPE_AGENT); }
  SB();
  LAS char* exw = opq(sm + L_EX + w * 4096 + lane * 16);
  LAS char* rl = sm + L_EX + (w ^ 2) * 4096;
  LAS char* exr = opq(rl + lane * 16);
  LAS char* ya = opq(sm + L_Y + (4 * g + q4) * YROW + T * 32 + 8 * pp);
  LAS char* sa = opq(sm + L_YS + 16 * g);
  LAS char* rlw = opq(rl + tok * 144 + g * 8);
  LAS char* rlr = opq(rl + (lane >> 3) * 144 + (lane & 7) * 16);
  int ov = (lane >> 3) * 1024 + (lane & 7) * 16, oso = (64 * p + 16 * T) * 1024 + j * 128; pin(ov); pins(oso);
  lds_wait<F_FULL, need_V(VFIRST)>(fa);
  static_for<0, 8>([&](auto K) LAM { rd2(std::integral_constant<int, 0>{}, K); });
  SB();
  TR(17);
  if (w == 0) {
    if (wany(first != tagr)) poll(pf, tagr);
    __builtin_amdgcn_fence(__ATOMIC_ACQUIRE, "wavefront");
    lds_put(FLG + F_HOK, 1);
  } else lds_wait<0, 1>((unsigned)(FLG + F_HOK));
  TR(8);
#pragma unroll
  for (int i = NOWN / 2; i < PD - 1; i++) { ldh(i, 0); ldh(i, 1); }
  __builtin_amdgcn_s_setprio(2);
  v2u yv[4]; f32x4 ys[4];
  static_for<0, 22>([&](auto I) LAM {
    constexpr int i = I, b = i & 1, nx = i + PD - 1;
    if constexpr (i == 1 || i == 8 || i == 15) TR(9 + (i - 1) / 7);
    if (i < 21 || ks == 0) {
      constexpr bool chk = !(i & 1) && i + 3 > VFIRST && i + 2 < 22;
      constexpr int want = need_V(i + 3 < 22 ? i + 3 : 21), out = 2 * ((nx < 22 ? nx : 22) - 1 - i);
      unsigned fv, fy; bool lack = false, lacky = false;
      SB();
      __builtin_amdgcn_s_waitcnt((out & 15) | ((out >> 4) << 14) | 0x70);
      static_for<0, 8>([&](auto M) LAM {
        constexpr int m = M, e = m >> 1;
        const v4u wv = {wfr[b][e][0].x, wfr[b][e][0].y, wfr[b][e][1].x, wfr[b][e][1].y};
        SB();
        mfma_bf(acc[e][m & 1], wv, hf[i % PD][m & 1]);
        SB();
        if constexpr (chk && m == 0) fv = *full;
        if constexpr (i < 21 && m < 4) { rd2(std::integral_constant<int, i + 1>{}, std::integral_constant<int, 2 * m>{}); rd2(std::integral_constant<int, i + 1>{}, std::integral_constant<int, 2 * m + 1>{}); }
        if constexpr (nx < 22 && (m == 4 || m == 5)) ldh(nx, m - 4);
        if constexpr (i == YAT - 1 && m == 4) fy = full[F_CW / 4];
        if constexpr (i == YAT - 1 && m == 7) lacky = wany(fy < 6);
        if constexpr (i == YAT && m >= 4) { yv[m - 4] = tr_read(ya + 16 * (m - 4) * YROW); ys[m - 4] = *(LAS f32x4*)(sa + 64 * (m - 4)); }
        if constexpr ((i == YAT + 1 || i == YAT + 2) && m >= 4) {
          constexpr int q = 2 * (i - YAT - 1) + ((m - 4) >> 1), r = 2 * (m & 1);
          const unsigned yy = (m & 1) ? yv[q].y : yv[q].x;
          const f16x2 hh = __builtin_bit_cast(f16x2, yy);
          rest[q][r] = __builtin_fmaf((float)hh.x, ys[q][r], rest[q][r]); rest[q][r + 1] = __builtin_fmaf((float)hh.y, ys[q][r + 1], rest[q][r + 1]);
          pin(rest[q][r]); pin(rest[q][r + 1]);
        }
        if constexpr (m == 6 && (i & 1) && (i + NVB < 23 || i == YGATE)) { CFENCE(); *frw = 18 + i; }
        if constexpr (chk && m == 6) lack = wany(fv < want);
      });
      SB();
      if constexpr (chk) { if (__builtin_expect(lack, 0)) lds_wait<F_FULL, want>(fa); CFENCE(); }
      if constexpr (i == YAT - 1) { if (__builtin_expect(lacky, 0)) lds_wait<F_CW, 6>(fa); CFENCE(); }
    }
  });
  MFMA_DONE();
  TR(12);
#pragma unroll
  for (int e = 0; e < 4; e++) *(LAS f32x4*)(exw + e * 1024) = acc[e][1];
  lds_put(FLG + F_GX + 4 * w, 2);
#pragma unroll
  for (int e = 0; e < 4; e++)
#pragma unroll
    for (int r = 0; r < 4; r++) { rest[e][r] = __builtin_fmaf(acc[e][0][r], rs2, rest[e][r]); pin(rest[e][r]); }
  lds_wait<0, 2>(FLG + F_GX + 4 * (w ^ 2));
  TR(13); TR(15);
  f32x4 po[4];
#pragma unroll
  for (int e = 0; e < 4; e++) po[e] = *(LAS f32x4*)(exr + e * 1024);
  CFENCE();
#pragma unroll
  for (int e = 0; e < 4; e++)
    *(LAS v2u*)(rlw + e * 32) = v2u{pk_bf(__builtin_fmaf(po[e][0], rs2, rest[e][0]), __builtin_fmaf(po[e][1], rs2, rest[e][1])),
                                    pk_bf(__builtin_fmaf(po[e][2], rs2, rest[e][2]), __builtin_fmaf(po[e][3], rs2, rest[e][3]))};
  CFENCE();
  {
    const rsrc_t ro = mk_rsrc(a.out, SEQ * DM * 2);
    const v4u o0 = *(LAS v4u*)(rlr), o1 = *(LAS v4u*)(rlr + 8 * 144);
    st_sc1(o0, ro, ov, oso);
    st_sc1(o1, ro, ov, oso + 8192);
  }
  if (w == 0 && lane < 2 && (lane ? j == 0 : p == 0)) {
    const u64 e1 = *(LAS u64*)(sm + FLG + F_EP + lane * 8) + 1;
    __hip_atomic_store(gflag(a, lane ? GF_ER + 16 * p : GF_EC + 16 * j), e1, __ATOMIC_RELAXED, __HIP_MEMORY_SCOPE_AGENT);
  }
  TR(14);
  if (w == DW) TR_DUMP(16, 19);
}

DEV void role_conv(const Args& a, LAS char* sm, int cw, int lane, int p, int j, unsigned fa) {
  const int g = lane >> 4, tok = lane & 15;
  const int ch = cw >> 1, mh = cw & 1, d = 64 * j + 2 * p + ch;
  const unsigned fme = FLG + F_CW + 4 * cw, fpa = FLG + F_CW + 4 * (cw ^ 1);
  TR_DECL; TR(0);
  __builtin_amdgcn_s_setprio(KCPRIO);
  v4u oh, idm[2];
#pragma unroll
  for (int e = 0; e < 4; e++) {
    oh[e] = ((lane >> 1) & 3) == e ? 0x3c00u << (16 * (lane & 1)) : 0u;
    idm[0][e] = g == (tok >> 3) ? oh[e] : 0u; idm[1][e] = g - 2 == (tok >> 3) ? oh[e] : 0u;
  }
  pin(oh); pin(idm[0]); pin(idm[1]);
  f32x4 dm;
#pragma unroll
  for (int r = 0; r < 4; r++) dm[r] = 4 * g + r == tok ? 1.f / DM : 0.f;
  int uoff = (8 * (g & 1) + (tok >> 1)) * 8192 + (g >> 1) * 32 + (tok & 1) * 16;
  LAS char* xs[2];
#pragma unroll
  for (int s = 0; s < 2; s++) xs[s] = opq(sm + L_X + (16 * cw + tok) * 128 + (((4 * s + g) ^ ((tok >> 1) & 7)) * 16));
  pin(dm); pin(uoff);
  const rsrc_t ru = mk_rsrc(a.ws + WS_U, SEQ * DM * 2);
  int uso = j * 262144 + p * 256 + cw * 64; pins(uso);
  v4u kf[2][2][4];
  float kinv, kd;
  SB();
  kgen_factors(kgen_read(sm, cw, lane), cw, lane, kf, kinv, kd);
  TR(1);
  __builtin_amdgcn_s_setprio(TPRIO);
  v2u kh[4];
  {
    f32x4 kt[2][2];
    kgen_table(kf, oh, lane, kd, kt);
#pragma unroll
    for (int i = 0; i < 4; i++) { const f32x4 v = kt[i >> 1][i & 1]; kh[i] = v2u{pk_h(v[0], v[1]), pk_h(v[2], v[3])}; pin(kh[i]); }
  }
  LAS char* tw = opq(sm + c_priv(cw) + g * 256 + tok * 8);
  TR(17);
  u64 tagc;
  v4u stat, pat;
  {
    v4u xl[2];
    f32x4 sa = zero4(), gr = zero4();
    v4u ones = {0x3f803f80u, 0x3f803f80u, 0x3f803f80u, 0x3f803f80u};
    pin(ones);
    __builtin_amdgcn_s_setprio(3);
    static_for<0, 2>([&](auto B) LAM {
      constexpr int c0 = B == 0 ? 0 : XB0, c1 = B == 0 ? XB0 : 8;
      lds_wait<F_FULL, need_X(c1 - 1)>(fa);
      if constexpr (B == 1) TR(4);
      v4u f[2 * (c1 - c0)];
      unsigned en = 0;
      if constexpr (B == 0) en = *(LAS unsigned*)(sm + FLG + F_EP);
#pragma unroll
      for (int c = c0; c < c1; c++) { f[2 * (c - c0)] = *(LAS v4u*)(xs[0] + ((c + 1) & 7) * 8192); f[2 * (c - c0) + 1] = *(LAS v4u*)(xs[1] + ((c + 1) & 7) * 8192); }
      SB();
#pragma unroll
      for (int i = 0; i < 2 * (c1 - c0); i++) {
        mfma_bf(gr, f[i], f[i]);
        if (B == 0 && i >= 2 && i < 6) { SB(); unsigned t = ((en + (i - 2)) & 7) | (((en + 2 * (i - 2) + 1) & 7) << 16); pin(t); pat[i - 2] = t; SB(); }
        mfma_bf(sa, ones, f[i]);
      }
            if constexpr (B == 1) { xl[0] = f[2 * (7 - XB0)]; xl[1] = f[2 * (7 - XB0) + 1]; }
    });
    __builtin_amdgcn_s_setprio(SPRIO);
    float xf[2][8];
#pragma unroll
    for (int s = 0; s < 2; s++)
#pragma unroll
      for (int e = 0; e < 4; e++) { const unsigned uu = xl[s][e]; xf[s][2 * e] = bf_lo(uu); xf[s][2 * e + 1] = bf_hi(uu); pin(xf[s][2 * e]); pin(xf[s][2 * e + 1]); }
    pin(sa); pin(gr);
    SB();
    const float mean = sa[0] * (1.f / DM);
    const float ex2 = xsum32(xsum16(__builtin_fmaf(gr[0], dm[0], gr[1] * dm[1]) + __builtin_fmaf(gr[2], dm[2], gr[3] * dm[3])));
    float var = __builtin_fmaf(-mean, mean, ex2);
    if (__builtin_expect(wany(!(var >= ex2 * (1.f / 1024.f))), 0)) {
      float q = 0;
#pragma nounroll
      for (int c = 0; c < 16; c++) {
        const v4u u = *(LAS v4u*)((c & 1 ? xs[1] : xs[0]) + (c >> 1) * 8192);
#pragma unroll
        for (int e = 0; e < 4; e++) { const unsigned uu = u[e]; const float d0 = bf_lo(uu) - mean, d1 = bf_hi(uu) - mean; q = __builtin_fmaf(d0, d0, __builtin_fmaf(d1, d1, q)); }
      }
      var = xsum32(xsum16(q)) * (1.f / DM);
    }
    const float m = rbf(mean), sd = rbf(__builtin_amdgcn_sqrtf(rbf(rbf(var) + 1e-5f))), rs = __builtin_amdgcn_rcpf(sd);
    const float c0 = -m * rs;
    v4u xn[2]; f32x4 t[4];
#pragma unroll
    for (int s = 0; s < 2; s++) {
#pragma unroll
      for (int e = 0; e < 4; e++) xn[s][e] = pk_h(__builtin_fmaf(xf[s][2 * e], rs, c0), __builtin_fmaf(xf[s][2 * e + 1], rs, c0));
      pin(xn[s]);
      NOP2();
      mfma_h0(t[2 * s], xn[s], idm[0]); mfma_h0(t[2 * s + 1], xn[s], idm[1]);
      SB();
    }
#pragma unroll
    for (int s = 0; s < 2; s++) {
      auto r0 = __builtin_amdgcn_permlane16_swap(pk_h(t[2 * s][0], t[2 * s][1]), pk_h(t[2 * s + 1][0], t[2 * s + 1][1]), false, false);
      auto r1 = __builtin_amdgcn_permlane16_swap(pk_h(t[2 * s][2], t[2 * s][3]), pk_h(t[2 * s + 1][2], t[2 * s + 1][3]), false, false);
      st_sc1(seal(v4u{r0[0], r1[0], r0[1], r1[1]}, pat), ru, uoff, uso + s * 131072);
    }
    stat = v4u{pk_bf(-m, -m), pk_bf(-m, sd), f2u(rs * rs), 0u};
    pin(stat);
  }
  LAS char* sw = opq(sm + L_STAT + (16 * cw + tok) * 16);
  TR(5);
  __builtin_amdgcn_s_sleep(TDELAY);
#pragma unroll
  for (int i = 0; i < 4; i++) *(LAS v2u*)(tw + (i >> 1) * 1024 + (i & 1) * 128) = kh[i];
  if (g == 0) *(LAS v4u*)sw = stat;
  lds_put(fme, 1);
  __builtin_amdgcn_s_setprio(EPRIO);
  tagc = tag_of(sm, 0); pins(tagc);
  if (mh == 0 && lane == 0) __hip_atomic_store(gflag(a, GF_Y) + d, (tagc & 0xffffffff00000000ull) | f2u(kinv * 65536.f), __ATOMIC_RELAXED, __HIP_MEMORY_SCOPE_AGENT);
  TR(16);
  v4u im[6];
  {
    const int nh = 127 - lane, nl = 63 - lane, to = c_priv(cw ^ 1) - c_priv(cw);
    LAS char* tb = sm + c_priv(cw);
    auto odd = [&](int q) LAM { return 1028 + (q >> 3) * 8 + ((q >> 2) & 1) * 256 + (q & 2); };
    auto tv = [&](int off) LAM { return *(LAS _Float16*)(tb + off) + *(LAS _Float16*)(tb + to + off); };
    auto pk = [&](_Float16 x, _Float16 y) LAM { return __builtin_bit_cast(unsigned, f16x2{x, y}); };
    lds_wait<0, 1>(fpa);
    if (mh == 0) {
      const _Float16 A = tv(lane ? 16 * nh + 16 : 1540), B = tv(16 * nh + 8), C = tv(16 * nh), A4 = tv(8 * nh + 8), B4 = tv(8 * nh + 4), C4 = tv(8 * nh);
      const unsigned aa = pk(A, A), bb2 = pk(B, B), cc = pk(C, C), b4 = pk(B4, B4), c4 = pk(C4, C4);
      im[0] = v4u{aa, aa, bb2, bb2}; im[1] = v4u{bb2, pk(B, C), cc, cc}; im[2] = v4u{aa, pk(A, B), bb2, bb2}; im[3] = v4u{bb2, cc, cc, cc};
      im[4] = v4u{pk(A4, A4), b4, pk(B4, C4), c4}; im[5] = v4u{pk(A4, B4), b4, c4, c4};
    } else {
      const _Float16 A = tv(4 * nh + 4), B = tv(4 * nh + 2), C = tv(4 * nh), Dd = tv(lane == 63 ? 1028 + 31 * 8 + 258 : 4 * nh);
      const _Float16 g3 = tv(odd(4 * nl + 3)), g2 = tv(4 * nl + 2), g1 = tv(odd(4 * nl + 1)), g0 = tv(4 * nl), gq = tv(odd(max(4 * nl - 1, 1))), gm = lane == 63 ? (_Float16)0 : gq;
      im[0] = v4u{pk(A, B), pk(C, C), pk(B, C), pk(C, Dd)}; im[1] = v4u{pk(g3, g2), pk(g1, g0), pk(g2, g1), pk(g0, gm)};
      im[2] = im[3] = im[4] = im[5] = v4u{0, 0, 0, 0};
    }
#pragma unroll
    for (int i = 0; i < 6; i++) pin(im[i]);
  }
  dma_flag<1>(srd(kSeq, 256), FLG + F_ST + 4 * cw);
  TR(2);
  lds_wait<F_FULL, need_W(UWAIT)>(fa);
  TR(3);
  v4u uv[2];
  auto ldu = [&]() LAM {
#pragma unroll
    for (int i = 0; i < 2; i++) uv[i] = __builtin_amdgcn_raw_buffer_load_b128(ru, lane * 16 + i * 1024, (j * 32 + p) * 8192 + cw * 2048, AUX_SC1);
  };
  ldu();
  SB();
  LAS char* cr = sm + ch * CH;
  LAS char* kc0 = opq(cr + (mh ? 3072 + 8 * lane : 32 * lane));
  LAS char* kc1 = opq(cr + (mh ? 3584 + 8 * lane : 2048 + 16 * lane));
  LAS char* uw = opq(sm + (lane & 1) * CH + C_U + 480 + cw * 1024 + (lane >> 1) * 16);
  LAS char* zw = opq(lane >= 34 ? sm + L_JUNK + (lane & 15) * 16 : cr + (lane < 30 ? C_U + lane * 16 : ((lane >> 1) & 1) * KCOPY + 4096 + (lane & 1) * 16));
  f32x4 acc[8];
#pragma unroll
  for (int c = 0; c < 8; c++) acc[c] = zero4();
  LAS char* ab[4];
#pragma unroll
  for (int i = 0; i < 4; i++) ab[i] = opq(cr + ((15 - tok) & 1) * KCOPY + 2 * (((15 - tok) & ~1) + 8 * g) + 64 * (4 - 4 * mh) + 1024 * i);
  LAS char* bb = opq(cr + C_U + 448 + 32 * tok + 16 * g - 64 * (4 * mh + 3));
  auto lda = [&](int i, int r, int k) LAM { return ld2(ab[(7 - i) >> 1] + 64 * (59 - 8 * i - r) - 1024 * ((7 - i) >> 1) + 8 * k); };
  SB();
#pragma nounroll
  for (int it = 0; it < POLL_CAP; it++) {
    if (!wany((unseal(uv[0], pat) | unseal(uv[1], pat)) != 0)) break;
    __builtin_amdgcn_s_sleep(SSLEEP);
    CFENCE();
    ldu();
  }
  TR(7);
  __builtin_amdgcn_s_setprio(IPRIO);
  lds_wait<F_ST, 1>(fa);
  lds_wait<F_FULL, need_W(15)>(fa);
  lds_wait<F_FREE, FREE_C>(fa);
  TR(8); TR(15);
  if (mh == 0) {
    *(LAS v4u*)(kc0) = im[0]; *(LAS v4u*)(kc0 + 16) = im[1]; *(LAS v4u*)(kc0 + KCOPY) = im[2]; *(LAS v4u*)(kc0 + KCOPY + 16) = im[3];
    *(LAS v4u*)(kc1) = im[4]; *(LAS v4u*)(kc1 + KCOPY) = im[5];
  } else {
    *(LAS v2u*)(kc0) = v2u{im[0].x, im[0].y}; *(LAS v2u*)(kc0 + KCOPY) = v2u{im[0].z, im[0].w};
    *(LAS v2u*)(kc1) = v2u{im[1].x, im[1].y}; *(LAS v2u*)(kc1 + KCOPY) = v2u{im[1].z, im[1].w};
    *(LAS v4u*)zw = im[2];
  }
#pragma unroll
  for (int i = 0; i < 2; i++) *(LAS v4u*)(uw + i * 512) = uv[i];
  lds_put(fme, 2);
  lds_wait<F_CW, 2>(fa);
  TR(6);
  lds_wait<0, 1>(FLG + F_GX + 4 * cw);
  __builtin_amdgcn_s_setprio(CPRIO);
  TR(9);
  v2u A[8][2]; v4u B[2][8];
#pragma unroll
  for (int i = 0; i < 8; i++) {
    B[0][i] = *(LAS v4u*)(bb + 64 * (8 * i + 3));
    A[7 - i][0] = lda(7 - i, 0, 0); A[7 - i][1] = lda(7 - i, 0, 1);
  }
  static_for<0, 4>([&](auto R) LAM {
    constexpr int r = R;
    static_for<0, 8>([&](auto II) LAM {
      constexpr int i = 7 - II;
      static_for<0, 8 - i>([&](auto K) LAM {
        constexpr int k = K;
        SB();
        mfma_h(acc[i + k], v4u{A[i][0].x, A[i][0].y, A[i][1].x, A[i][1].y}, B[r & 1][k]);
        SB();
        if constexpr (r < 3) {
          if constexpr (i < 7 && k < 2) A[i + 1][k] = lda(i + 1, r + 1, k);
          if constexpr (i <= 4 && i >= 1 && (k == 2 || k == 3)) B[(r + 1) & 1][2 * (4 - i) + k - 2] = *(LAS v4u*)(bb + 64 * (8 * (2 * (4 - i) + k - 2) + 3 - (r + 1)));
        }
        if constexpr (r == 3 && i == 4 && k == 0) lds_put(fme, 3);
      });
    });
    if constexpr (r < 3) { A[0][0] = lda(0, r + 1, 0); A[0][1] = lda(0, r + 1, 1); }
  });
  LAS char* exm = opq(cr + mh * 4096 + lane * 16);
  LAS char* exo = opq(cr + (mh ^ 1) * 4096 + lane * 16);
  int yoff = (g & 1) * 512 + (tok >> 2) * 128 + (g >> 1) * 64 + (tok & 3) * 16; pin(yoff);
  const rsrc_t ry = mk_rsrc(a.ws + WS_Y, SEQ * DM * 2);
  constexpr float YS = 1.f / 65536.f;
  MFMA_DONE();
  TR(10);
  lds_wait<0, 3>(fpa);
  auto finish = [&](auto MH) LAM {
    constexpr int own = 4 * MH;
#pragma unroll
    for (int c = 0; c < 4; c++) *(LAS f32x4*)(exm + c * 1024) = acc[(own ^ 4) + c];
    lds_put(fme, 4);
#pragma unroll
    for (int c = 0; c < 4; c++) { acc[own + c] *= YS; pin(acc[own + c]); }
    lds_wait<0, 4>(fpa);
    f32x4 o[4];
#pragma unroll
    for (int c = 0; c < 4; c++) o[c] = *(LAS f32x4*)(exo + c * 1024);
#pragma unroll
    for (int c = 0; c < 4; c += 2) {
      unsigned q[2][2];
#pragma unroll
      for (int t = 0; t < 2; t++)
#pragma unroll
        for (int r = 0; r < 2; r++) q[t][r] = pk_h(__builtin_fmaf(o[c + t][2 * r], YS, acc[own + c + t][2 * r]), __builtin_fmaf(o[c + t][2 * r + 1], YS, acc[own + c + t][2 * r + 1]));
      auto r0 = __builtin_amdgcn_permlane16_swap(q[0][0], q[1][0], false, false);
      auto r1 = __builtin_amdgcn_permlane16_swap(q[0][1], q[1][1], false, false);
      st_sc1(seal(v4u{r0[0], r1[0], r0[1], r1[1]}, pat), ry, yoff + c * 512, d * 4096 + MH * 2048);
    }
  };
  if (mh) finish(std::integral_constant<int, 1>{}); else finish(std::integral_constant<int, 0>{});
  lds_put(fme, 5);
  TR(11);
  LAS char* yw = opq(sm + L_Y + (16 * cw + (lane >> 3)) * YROW + (lane & 3) * 32 + ((lane >> 2) & 1) * 16);
  GAS u64* ps = gflag(a, GF_Y) + 64 * j + 16 * cw + tok; pin(ps);
  lds_wait<F_CW, 5>(fa);
  lds_wait<F_FREE, 17 + YGATE>(fa);
  TR(12);
  u64 sc;
#pragma nounroll
  for (int it = 0; it < POLL_CAP; it++) {
#pragma unroll
    for (int i = 0; i < 2; i++) uv[i] = __builtin_amdgcn_raw_buffer_load_b128(ry, (lane >> 3) * 4096 + (lane & 7) * 16, (64 * j + 16 * cw + 8 * i) * 4096 + p * 128, AUX_SC1);
    sc = __hip_atomic_load(ps, __ATOMIC_RELAXED, __HIP_MEMORY_SCOPE_AGENT);
    if (!wany((unseal(uv[0], pat) | unseal(uv[1], pat) | (unsigned)((sc ^ tagc) >> 32)) != 0)) break;
    __builtin_amdgcn_s_sleep(SSLEEP);
    CFENCE();
  }
  TR(13);
#pragma unroll
  for (int i = 0; i < 2; i++) *(LAS v4u*)(yw + 8 * i * YROW) = uv[i];
  if (g == 0) *(LAS unsigned*)(sm + L_YS + (16 * cw + tok) * 4) = (unsigned)sc;
  lds_put(fme, 6);
  TR(14);
  if (cw == 0) TR_DUMP(48, 18);
}

__global__ __launch_bounds__(NTHR, 3) NOPK void k_fused(Args a) {
  __shared__ __attribute__((aligned(16))) char smem[LDS_BYTES];
  LAS char* sm = (LAS char*)smem;
  const int lane = threadIdx.x & 63, w = __builtin_amdgcn_readfirstlane(threadIdx.x >> 6);
  const int b = blockIdx.x, p = (b & 7) * 4 + (b >> 6), j = (b >> 3) & 7;
  int vx[2];
  if (w == 4) {
    u64 pc; asm volatile("s_getpc_b64 %0" : "=s"(pc));
    dma_touch(srd((const void*)pc, CODE_BYTES), lane < 8 ? ((b >> 3) * 8 + lane) * 128 : 0x40000000);
    ((LAS unsigned*)(sm + FLG))[lane] = 0; LGKM0();
  }
  unsigned fa = FLG + 4 * (lane & 3);
  pin(fa);
  SB();
  __builtin_amdgcn_s_barrier();
  if (w >= 8) { __builtin_amdgcn_s_setprio(3); kgen_fetch(a, w - 8, lane, p, j); }
  else if (w < 4) { __builtin_amdgcn_s_setprio(2); loader_entry(a, w, lane, p, j, vx); __builtin_amdgcn_s_setprio(3); }
  else if (w == 5) { __builtin_amdgcn_s_sleep(W1DELAY); dma_touch(srd(a.w1, DM * FF * 2), lane < 43 ? (b * 43 + lane) * 128 : 0x40000000); }
  SB();
  if (w < 4) {
    role_loader(a, sm, w, lane, p, j, fa, vx);
  }
  else if (w < 8) { __builtin_amdgcn_s_setprio(2); role_gemm(a, sm, w - 4, lane, p, j, fa); }
  else role_conv(a, sm, w - 8, lane, p, j, fa);
}

extern "C" void kernel_launch(void* const* d_in, const int* in_sizes, int n_in, void* d_out, int out_size, void* d_ws, size_t ws_size, hipStream_t stream) {
  Args a;
  a.x = (const u16*)d_in[0]; a.lar = (const u16*)d_in[1]; a.aim = (const u16*)d_in[2]; a.cre = (const u16*)d_in[3]; a.cim = (const u16*)d_in[4];
  a.ldt = (const u16*)d_in[5]; a.dsk = (const u16*)d_in[6]; a.w1 = (const u16*)d_in[8]; a.b1 = (const u16*)d_in[9]; a.w2 = (const u16*)d_in[10];
  a.b2 = (const u16*)d_in[11]; a.out = (u16*)d_out; a.ws = (char*)d_ws;
  k_fused<<<256, NTHR, 0, stream>>>(a);
}
```

```cpp
#include <hip/hip_runtime.h>
#include <stdint.h>
#include <type_traits>

#define LAS __attribute__((address_space(3)))
#define GAS __attribute__((address_space(1)))
#if defined(__HIP_DEVICE_COMPILE__) && __HIP_DEVICE_COMPILE__
#define NOPK __attribute__((target("no-packed-fp32-ops")))
#else
#define NOPK
#endif
#define DEV __device__ __forceinline__ NOPK
#define LAM __attribute__((always_inline)) NOPK
typedef unsigned short u16;
typedef unsigned long long u64;
typedef unsigned v4u __attribute__((ext_vector_type(4)));
typedef unsigned v2u __attribute__((ext_vector_type(2)));
typedef unsigned v2u4 __attribute__((ext_vector_type(2), aligned(4)));
typedef float f32x4 __attribute__((ext_vector_type(4)));
typedef short s16x4 __attribute__((ext_vector_type(4)));
typedef short s16x2 __attribute__((ext_vector_type(2)));
typedef __amdgpu_buffer_rsrc_t rsrc_t;

constexpr int SEQ = 2048, DM = 512, NMODE = 128, FF = 1376, NKS = 43;
constexpr size_t WS_H = 0, WS_U = 6u << 20, WS_Y = 10u << 20, WS_FLAG = 14u << 20;
constexpr int GF_H = 1024, GF_Y = 2048, GF_EC = 3072, GF_ER = 3200;
constexpr int FSTR = 16, PSLEEP = 2, UWAIT = 10, CPRIO = 3, W2PF = 12, LPRIO = 1, SSLEEP = 4, IPRIO = 1, YGATE = 7, PREP = 4, NPRE = 11, VFIRST = 9, PUTGATE = 1, TDELAY = 4, W1DELAY = 6, VD1 = 12, EPRIO = 0, XB0 = 5, YAT = 18, RD0 = 7, HDELAY = 3, PD = 7, VDEPTH = 8, NENTRY = 4;
constexpr int NTHR = 768;
constexpr int POLL_CAP = 1 << 15;
constexpr int AUX_SC1 = 16;

struct Args {
  const u16 *x, *w1, *lar, *aim, *cre, *cim, *ldt, *dsk, *b1, *w2, *b2;
  u16* out;
  char* ws;
};

template <int I, int N, class Fn> DEV void static_for(Fn&& f) {
  if constexpr (I < N) { f(std::integral_constant<int, I>{}); static_for<I + 1, N>(f); }
}
#define CE(x) (std::integral_constant<int, (x)>::value)
template <class T> DEV void pin(T& v) { asm volatile("" : "+v"(v)); }
template <class T> DEV void pins(T& v) { asm volatile("" : "+s"(v)); }
DEV float u2f(unsigned u) { return __builtin_bit_cast(float, u); }
DEV unsigned f2u(float f) { return __builtin_bit_cast(unsigned, f); }
DEV bool wany(bool c) { return __builtin_amdgcn_ballot_w64(c) != 0; }
DEV rsrc_t mk_rsrc(const void* p, int bytes) { return __builtin_amdgcn_make_buffer_rsrc((void*)p, 0, bytes, 0x00020000); }
DEV float bf_lo(unsigned u) { return u2f(u << 16); }
DEV float bf_hi(unsigned u) { return u2f(u & 0xffff0000u); }
typedef float f32x2 __attribute__((ext_vector_type(2)));
typedef __bf16 bf16x2 __attribute__((ext_vector_type(2)));
typedef _Float16 f16x2 __attribute__((ext_vector_type(2)));
DEV unsigned pk_bf(float a, float b) { return __builtin_bit_cast(unsigned, __builtin_convertvector(f32x2{a, b}, bf16x2)); }
DEV unsigned pk_h(float a, float b) { return __builtin_bit_cast(unsigned, __builtin_convertvector(f32x2{a, b}, f16x2)); }
DEV float rbf(float a) { return u2f(pk_bf(0.f, a)); }
DEV unsigned relu_pk(unsigned v) { return __builtin_bit_cast(unsigned, __builtin_elementwise_max(__builtin_bit_cast(s16x2, v), s16x2{0, 0})); }
DEV v2u tr_read(LAS char* p) { return __builtin_bit_cast(v2u, __builtin_amdgcn_ds_read_tr16_b64_v4i16((LAS s16x4*)p)); }
DEV v2u ld2(LAS char* p) { return *(LAS v2u4*)p; }
DEV float xsum16(float v) { auto r = __builtin_amdgcn_permlane16_swap(f2u(v), f2u(v), false, false); return u2f(r[0]) + u2f(r[1]); }
DEV float xsum32(float v) { auto r = __builtin_amdgcn_permlane32_swap(f2u(v), f2u(v), false, false); return u2f(r[0]) + u2f(r[1]); }
template <int CTRL> DEV float dpp_add(float v) { return v + u2f((unsigned)__builtin_amdgcn_update_dpp(0, (int)f2u(v), CTRL, 0xf, 0xf, false)); }
DEV float wave_sum(float v) { v = dpp_add<0x121>(v); v = dpp_add<0x122>(v); v = dpp_add<0x124>(v); v = dpp_add<0x128>(v); return xsum32(xsum16(v)); }
DEV LAS char* opq(LAS char* p) { unsigned v = (unsigned)(size_t)p; pin(v); return (LAS char*)(size_t)v; }
DEV f32x4 zero4() { f32x4 z = {0, 0, 0, 0}; pin(z); return z; }
#define VMCNT(n) asm volatile("s_waitcnt vmcnt(%0)" ::"i"(n) : "memory")
#define LGKM0() asm volatile("s_waitcnt lgkmcnt(0)" ::: "memory")
#define CFENCE() asm volatile("" ::: "memory")
#define SB() __builtin_amdgcn_sched_barrier(0)
DEV void mfma_bf(f32x4& c, v4u a, v4u b) { asm volatile("v_mfma_f32_16x16x32_bf16 %0, %1, %2, %0" : "+v"(c) : "v"(a), "v"(b)); }
DEV void mfma_bf16k(f32x4& c, v2u a, v2u b) { asm volatile("v_mfma_f32_16x16x16_bf16 %0, %1, %2, %0" : "+v"(c) : "v"(a), "v"(b)); }
DEV void mfma_h(f32x4& c, v4u a, v4u b) { asm volatile("v_mfma_f32_16x16x32_f16 %0, %1, %2, %0" : "+v"(c) : "v"(a), "v"(b)); }
#define NOP2() do { SB(); asm volatile("s_nop 1"); SB(); } while (0)
#define MFMA_DONE() do { SB(); asm volatile("s_nop 7"); SB(); } while (0)

DEV void st_sc1(v4u v, rsrc_t r, int voff, int soff) { __builtin_amdgcn_raw_buffer_store_b128(v, r, voff, soff, AUX_SC1); NOP2(); }
DEV void lds_put(unsigned addr, unsigned v) { asm volatile("s_mov_b64 exec, 1\n\tds_write_b32 %0, %1\n\ts_mov_b64 exec, -1" ::"v"(addr), "v"(v) : "memory"); }
template <int OFF, int WANT> DEV void lds_wait(unsigned fa) {
  unsigned t, n;
  asm volatile("s_mov_b32 %1, 0x80000\n"
               "1:\n\tds_read_b32 %0, %2 offset:%3\n\ts_waitcnt lgkmcnt(0)\n\tv_cmp_gt_u32 vcc, %4, %0\n\ts_cbranch_vccz 2f\n\t"
               "s_sleep 1\n\ts_sub_u32 %1, %1, 1\n\ts_cbranch_scc0 1b\n2:"
               : "=&v"(t), "=&s"(n) : "v"(fa), "i"(OFF), "i"(WANT) : "memory", "vcc", "scc");
}
static __device__ const unsigned kSeq[64] = {0,1,2,3,4,5,6,7,8,9,10,11,12,13,14,15,16,17,18,19,20,21,22,23,24,25,26,27,28,29,30,31,32,
  33,34,35,36,37,38,39,40,41,42,43,44,45,46,47,48,49,50,51,52,53,54,55,56,57,58,59,60,61,62,63};
DEV v4u srd(const void* p, unsigned bytes) { const u64 v = (u64)p; return v4u{(unsigned)v, (unsigned)(v >> 32) & 0xffffu, bytes, 0x00020000u}; }
DEV void dma16(v4u rs, unsigned lds, int voff, int soff) {
  asm volatile("s_mov_b32 m0, %0\n\ts_nop 0\n\tbuffer_load_dwordx4 %1, %2, %3 offen lds" ::"s"(lds), "v"(voff), "s"(rs), "s"(soff) : "memory");
}
template <int N> DEV void dma_flag(v4u rs, unsigned lds) {
  asm volatile("s_mov_b32 m0, %0\n\ts_mov_b64 exec, 1\n\tbuffer_load_dword off, %1, %2 lds\n\ts_mov_b64 exec, -1" ::"s"(lds), "s"(rs), "s"(N * 4) : "memory");
}
DEV void dma_ep(v4u rs, unsigned lds, int voff) {
  asm volatile("s_mov_b32 m0, %0\n\ts_mov_b64 exec, 3\n\tbuffer_load_dword %2, %1, 0 offen sc1 lds\n\ts_mov_b64 exec, -1" ::"s"(lds), "s"(rs), "v"(voff) : "memory");
}
constexpr int CODE_BYTES = 31 * 1024, L_JUNK = 163840 - 512, L_B2 = 163840 - 768;
DEV void dma_touch(v4u rs, int voff) {
  asm volatile("s_mov_b32 m0, %0\n\ts_nop 0\n\tbuffer_load_dword %2, %1, 0 offen lds" ::"s"(L_JUNK), "s"(rs), "v"(voff) : "memory");
}
DEV void dma_b2(v4u rs, int voff) {
  asm volatile("s_mov_b32 m0, %0\n\ts_mov_b64 exec, 0xffffffff\n\tbuffer_load_dword %2, %1, 0 offen lds\n\ts_mov_b64 exec, -1" ::"s"(L_B2), "s"(rs), "v"(voff) : "memory");
}
DEV void dma_touch1(v4u rs, int soff) {
  asm volatile("s_mov_b32 m0, %0\n\ts_mov_b64 exec, 1\n\tbuffer_load_dword off, %1, %2 lds\n\ts_mov_b64 exec, -1" ::"s"(L_JUNK), "s"(rs), "s"(soff) : "memory");
}
DEV GAS u64* gflag(const Args& a, int line) { return (GAS u64*)(a.ws + WS_FLAG) + line * FSTR; }
template <int N> DEV void flag_store(GAS u64* f, v2u tag) {
  int z = 0;
  asm volatile("s_waitcnt vmcnt(%3)\n\ts_mov_b64 exec, 1\n\tglobal_store_dwordx2 %0, %1, %2 sc1\n\ts_mov_b64 exec, -1" ::"v"(z), "v"(tag), "s"(f), "i"(N) : "memory");
}
DEV void poll(GAS u64* p, u64 tag) {
#pragma nounroll
  for (int it = 0; it < POLL_CAP; it++) {
    if (!wany(__hip_atomic_load(p, __ATOMIC_RELAXED, __HIP_MEMORY_SCOPE_AGENT) != tag)) break;
    __builtin_amdgcn_s_sleep(PSLEEP);
  }
  __builtin_amdgcn_fence(__ATOMIC_ACQUIRE, "wavefront");
}
DEV int unit_start(int j) { return (j * 37 + 3) / 7; }

constexpr int KCOPY = 4160, C_U = 2 * KCOPY, CH = C_U + 4608, L_STAT = 31744;
constexpr int L_X = 32768, L_RING = L_X + 65536;
constexpr int L_Y = 0, YROW = 160, L_EX = L_Y + 64 * YROW, L_YS = L_EX + 16384;
constexpr int LDS_BYTES = 163840, FLG = LDS_BYTES - 256;
static_assert(2 * CH <= L_STAT && L_YS + 256 <= L_STAT && L_RING + 61440 <= FLG, "lds");
constexpr int F_FULL = 0, F_FREE = 16, F_CW = 32, F_GX = 48, F_EP = 64, F_ST = 80, F_HOK = 96, F_GP = 112;

constexpr int NSTEP1 = 24;
constexpr int ST_KIND[NSTEP1] = {0, 0, 0, 0, 1, 1, 0, 0, 1, 0, 0, 1, 1, 1, 1, 1, 1, 1, 1, 1, 1, 1, 1, 1};
constexpr int st_idx(int s) { int n = 0; for (int i = 0; i < s; i++) n += ST_KIND[i] == ST_KIND[s]; return n; }
constexpr int step_of(int kind, int idx) { for (int s = 0; s < NSTEP1; s++) if (ST_KIND[s] == kind && st_idx(s) == idx) return s; return -1; }
constexpr int need_X(int c) { return step_of(0, c) + 1; }
constexpr int need_W(int u) { return step_of(1, u) + 1; }
constexpr int need_V(int t) { return NSTEP1 + t + 1; }
static_assert(need_X(0) < need_W(0) && need_X(1) < need_W(2) && need_X(2) < need_W(4) && need_X(3) < need_W(6) && need_X(7) < need_W(14), "x before its units");
constexpr int w_addr(int u) { return u % 7 < 5 ? L_RING + (u % 7) * 12288 : (u % 7 - 5) * 12288; }
constexpr int x_addr(int c) { return L_X + ((c + 1) & 7) * 8192; }
constexpr int FREE_C = 14;
constexpr int NVB = 14, L_V = L_X + 8192, VH = 7;

DEV void x_step(v4u rx, const int (&vx)[2], int sx, int idx, int l, int j, int lds) {
  const int so = sx + ((idx + j + 1) & 7) * 128;
#pragma unroll
  for (int i = 0; i < 2; i++) dma16(rx, lds + l * 2048 + i * 1024, vx[i], so);
}
DEV void loader_entry(const Args& a, int l, int lane, int p, int j, int (&vx)[2]) {
  vx[0] = (lane >> 3) * 1024 + (((lane & 7) ^ (lane >> 4)) * 16); vx[1] = (vx[0] ^ 64) + 8192;
  const v4u rx = srd(a.x, SEQ * DM * 2);
  static_for<0, NENTRY>([&](auto S) LAM { constexpr int s = S; x_step(rx, vx, p * 65536 + l * 16384, s, l, j, CE(x_addr(s))); });
}
DEV void role_loader(const Args& a, LAS char* sm, int l, int lane, int p, int j, unsigned fa, const int (&vx)[2]) {
  const v4u rx = srd(a.x, SEQ * DM * 2), rw = srd(a.w1, DM * FF * 2), rv = srd(a.w2, DM * FF * 2), rq = srd(kSeq, 256);
  int vw[3], vt, vv;
  const int sx = p * 65536 + l * 16384, sw = unit_start(j) * 64;
  const int st = (((int)blockIdx.x >> 3) * 43 + l) * 1024, sc = ((3 - (int)((size_t)a.w2 >> 7)) & 7) * 128;
  if (l == 3) dma_b2(srd(a.b2 + 64 * j, 128), lane * 4);
  if (l < 2) dma_ep(srd(a.ws + WS_FLAG + (size_t)(l ? GF_ER + 16 * p : GF_EC + 16 * j) * 8 * FSTR, 8), FLG + F_EP + 8 * l, lane * 4);
  static_for<NENTRY, NSTEP1>([&](auto S) LAM {
    constexpr int s = S, kind = ST_KIND[s], idx = st_idx(s);
    if constexpr (s == W2PF) { if (l == 3) dma_touch(rv, vt); }
    if constexpr (s == PREP) {
      const int nown = (j == 7 ? NKS : unit_start(j + 1)) - unit_start(j);
#pragma unroll
      for (int n = 0; n < 3; n++) {
        const int sg = 16 * (3 * l + n) + (lane >> 2), r = (sg * 171) >> 10, qp = sg - 6 * r, q = qp ^ ((r >> 1) & 1);
        vw[n] = q < nown ? r * (FF * 2) + q * 64 + (((lane & 3) ^ (2 * ((r >> 3) & 1))) * 16) : 0x40000000;
        pin(vw[n]);
      }
      vt = lane < 43 ? (blockIdx.x * 43 + lane) * 128 : 0x40000000;
      vv = (8 * l + (lane >> 3)) * 1024 + (((((lane & 7) >> 1) ^ (((lane >> 4) & 1) | ((l & 1) << 1))) * 2 + (lane & 1)) * 16) + j * 128;
      pin(vt); pin(vv);
      __builtin_amdgcn_s_setprio(LPRIO);
    }
    if constexpr (kind == 0) {
      x_step(rx, vx, sx, idx, l, j, CE(x_addr(idx)));
    } else {
      if constexpr (idx >= 7) lds_wait<F_FREE, idx - 6>(fa);
      const int so = sw + ((((idx >> 1) + j + 1) & 7) * 64 + (idx & 1) * 32) * (FF * 2);
#pragma unroll
      for (int n = 0; n < 3; n++) {
        dma16(rw, CE(w_addr(idx)) + l * 3072 + n * 1024, vw[n], so);
      }
    }
    if constexpr (kind == 1 || idx == XB0 - 1 || idx == 7) dma_flag<s + 1>(rq, FLG + F_FULL + 4 * l);
    if constexpr (kind == 1 && idx >= 5) dma_touch1(rv, min(st + (idx - 5) * 4096, 1375 * 1024) + sc);
  });
  const rsrc_t rv2 = mk_rsrc(a.w2, DM * FF * 2);
  v4u wr[22][2];
  LAS char* wa[2] = {opq(sm + L_V + l * 1024 + lane * 16), opq(sm + L_V + VH * 8192 + l * 1024 + lane * 16)};
  volatile LAS unsigned* flw = (volatile LAS unsigned*)opq(sm + (lane ? L_JUNK + lane * 4 : FLG + F_FULL + 4 * l));
  auto get = [&](auto TT) LAM {
    constexpr int t = TT;
#pragma unroll
    for (int hf = 0; hf < (t < 21 ? 2 : 1); hf++) wr[t][hf] = __builtin_amdgcn_raw_buffer_load_b128(rv2, vv, (2 * t + hf) * 32768, 0);
    SB(); VMCNT(t < NPRE ? VD1 : VDEPTH);
  };
  auto put = [&](auto TT) LAM {
    constexpr int t = TT, q = t % NVB;
    if constexpr (t == NVB || (t > NVB + 2 && (t - NVB) % 2 == 1)) lds_wait<F_FREE, t == NVB ? 19 : 18 + t - NVB>(fa);
#pragma unroll
    for (int hf = 0; hf < (t < 21 ? 2 : 1); hf++) *(LAS v4u*)(wa[q / VH] + (q % VH) * 8192 + hf * 4096) = wr[t][hf];
    if constexpr ((t & 1) && t >= VFIRST) { CFENCE(); *flw = CE(need_V(t)); }
    SB();
  };
  static_for<0, NPRE>(get);
  lds_wait<F_FREE, 16>(fa);
  lds_wait<F_CW, PUTGATE>(fa);
  static_for<0, NPRE>(put);
  lds_wait<F_GP, 1>(fa);
  static_for<NPRE, 22>(get);
  static_for<NPRE, 22>(put);
}

DEV u64 tag_of(LAS char* sm, int which) {
  const v2u e = *(LAS v2u*)(sm + FLG + F_EP + 8 * which);
  const u64 v = ((u64)(unsigned)__builtin_amdgcn_readfirstlane(e.y) << 32) | (unsigned)__builtin_amdgcn_readfirstlane(e.x);
  return (v + 1) * 0x9E3779B97F4A7C15ull;
}

struct cpx { float r, i; };
DEV cpx cmul(cpx a, cpx b) { return cpx{__builtin_fmaf(a.r, b.r, -a.i * b.i), __builtin_fmaf(a.r, b.i, a.i * b.r)}; }
DEV cpx cexp_p(float zr2, float zi2, float pos) {
  const float e = __builtin_amdgcn_exp2f(zr2 * pos), th = __builtin_amdgcn_fractf(zi2 * pos);
  return cpx{e * __builtin_amdgcn_cosf(th), e * __builtin_amdgcn_sinf(th)};
}
DEV float exp_bf(float v) { return rbf(__builtin_amdgcn_exp2f(v * 1.44269504088896f)); }
DEV void pow2_scale(float mx, float& s, float& inv) {
  const int e = min(max((int)(f2u(mx) >> 23), 15), 254);
  s = u2f((unsigned)(268 - e) << 23);
  inv = u2f((unsigned)(e - 14) << 23);
}

DEV void mfma_h0(f32x4& d, v4u a, v4u b) { asm volatile("v_mfma_f32_16x16x32_f16 %0, %1, %2, 0" : "=&v"(d) : "v"(a), "v"(b)); }
DEV cpx csq(cpx a) { return cpx{__builtin_fmaf(a.r, a.r, -a.i * a.i), 2.f * a.r * a.i}; }
DEV void cmul_h(unsigned re, unsigned im, unsigned m, unsigned& ore, unsigned& oim) {
  const f16x2 r = __builtin_bit_cast(f16x2, re), i = __builtin_bit_cast(f16x2, im), mm = __builtin_bit_cast(f16x2, m);
  const f16x2 mr = {mm.x, mm.x}, mi = {mm.y, mm.y};
  ore = __builtin_bit_cast(unsigned, __builtin_elementwise_fma(r, mr, -(i * mi)));
  oim = __builtin_bit_cast(unsigned, __builtin_elementwise_fma(r, mi, i * mr));
}

struct KIn { unsigned lar, aim, cre, cim, ldt, dsk; };
DEV int c_priv(int cw) { return cw < 3 ? 24576 + cw * 2048 : L_RING + 61440; }
static_assert(24576 + 3 * 2048 <= L_STAT && L_RING + 61440 + 2048 <= L_B2, "lds");
DEV void dma4(const void* base, unsigned lds, int voff) {
  asm volatile("s_mov_b32 m0, %0\n\ts_nop 0\n\tglobal_load_lds_dword %1, %2" ::"s"(lds), "v"(voff), "s"(base) : "memory");
}
DEV void kgen_fetch(const Args& a, int cw, int lane, int p, int j) {
  const int d = 64 * j + 2 * p + (cw >> 1), vo = d * 256 + lane * 4, lds = c_priv(cw);
  dma4(a.lar, lds, vo); dma4(a.aim, lds + 256, vo); dma4(a.cre, lds + 512, vo); dma4(a.cim, lds + 768, vo);
  int vs = (d >> 1) * 4; pin(vs);
  asm volatile("s_mov_b64 exec, 1" ::: "memory");
  dma4(a.ldt, lds + 1024, vs); dma4(a.dsk, lds + 1028, vs);
  asm volatile("s_mov_b64 exec, -1" ::: "memory");
}
DEV KIn kgen_read(LAS char* sm, int cw, int lane) {
  LAS char* q = sm + c_priv(cw);
  VMCNT(0);
  KIn k;
  k.lar = *(LAS unsigned*)(q + lane * 4); k.aim = *(LAS unsigned*)(q + 256 + lane * 4); k.cre = *(LAS unsigned*)(q + 512 + lane * 4); k.cim = *(LAS unsigned*)(q + 768 + lane * 4);
  k.ldt = *(LAS u16*)(q + 1024 + (cw & 2)); k.dsk = *(LAS u16*)(q + 1028 + (cw & 2));
  return k;
}
DEV cpx conj(cpx a) { return cpx{a.r, -a.i}; }
DEV void kgen_factors(KIn k, int cw, int lane, v4u (&f)[2][2][4], float& kinv, float& kd) {
  const int sh = (cw & 1) ? 0 : 16;
  const float ldt = bf_lo(k.ldt), dsk = bf_lo(k.dsk);
  const float dt = exp_bf(ldt);
  const float zr2 = -exp_bf(bf_hi(k.lar << sh)) * dt * 1.44269504088896f, zi2 = bf_hi(k.aim << sh) * dt * 0.159154943091895f;
  const cpx E1 = cexp_p(zr2, zi2, 1.f), E2 = csq(E1);
  {
    const cpx E8 = csq(csq(E2)), M = cexp_p(zr2, zi2, 32.f);
    cpx S[8];
    S[0] = cpx{1.f, 0.f}; S[1] = E8; S[2] = csq(E8); S[3] = cmul(S[2], E8); S[4] = M;
#pragma unroll
    for (int i = 1; i < 4; i++) S[4 + i] = cmul(S[i], M);
    const cpx M2 = csq(M), M4 = csq(M2), M6 = cmul(M2, M4);
    const unsigned mm[3] = {pk_h(M2.r, M2.i), pk_h(M4.r, M4.i), pk_h(M6.r, M6.i)};
#pragma unroll
    for (int e = 0; e < 4; e++) { f[1][0][0][e] = pk_h(S[2 * e].r, S[2 * e + 1].r); f[1][1][0][e] = pk_h(S[2 * e].i, S[2 * e + 1].i); }
#pragma unroll
    for (int q = 1; q < 4; q++)
#pragma unroll
      for (int e = 0; e < 4; e++) { unsigned re, im; cmul_h(f[1][0][0][e], f[1][1][0][e], mm[q - 1], re, im); f[1][0][q][e] = re; f[1][1][q][e] = im; }
  }
  float ks;
  {
    const float csum = wave_sum((__builtin_fabsf(bf_lo(k.cre)) + __builtin_fabsf(bf_hi(k.cre))) + (__builtin_fabsf(bf_lo(k.cim)) + __builtin_fabsf(bf_hi(k.cim))));
    pow2_scale(__builtin_fmaf(2.f * dt, csum, __builtin_fabsf(dsk)), ks, kinv);
    const float cs = 2.f * dt * ks;
    const cpx M = cexp_p(zr2, -zi2, 256.f);
    cpx S[8];
    S[0] = cpx{bf_hi(k.cre << sh) * cs, bf_hi(k.cim << sh) * -cs};
#pragma unroll
    for (int i = 1; i < 4; i++) S[i] = cmul(S[i - 1], conj(E2));
#pragma unroll
    for (int i = 0; i < 4; i++) S[4 + i] = cmul(S[i], M);
    const cpx M2 = csq(M), M4 = csq(M2), M6 = cmul(M2, M4), M8 = csq(M4);
    const unsigned m2 = pk_h(M2.r, M2.i), m4 = pk_h(M4.r, M4.i), m6 = pk_h(M6.r, M6.i), m1 = pk_h(E1.r, -E1.i), m8 = pk_h(M8.r, M8.i);
#pragma unroll
    for (int e = 0; e < 4; e++) { f[0][0][0][e] = pk_h(S[2 * e].r, S[2 * e + 1].r); f[0][1][0][e] = pk_h(S[2 * e].i, S[2 * e + 1].i); }
#pragma unroll
    for (int q = 1; q < 4; q++)
#pragma unroll
      for (int e = 0; e < 4; e++) {
        const int src = q >= 2 && (e & 1) ? e >> 1 : e;
        unsigned re = 0, im = 0;
        if (q < 3 || e < 3) cmul_h(f[0][0][0][src], f[0][1][0][src], q == 1 ? m2 : q == 2 ? ((e & 1) ? m1 : m4) : ((e & 1) ? m8 : m6), re, im);
        f[0][0][q][e] = re; f[0][1][q][e] = im;
      }
  }
#pragma unroll
  for (int q = 0; q < 16; q++) pin(f[q >> 3][(q >> 2) & 1][q & 3]);
  kd = ((cw & 1) | lane) == 0 ? dsk * ks : 0.f;
  pin(kinv); pin(kd);
}
DEV void kgen_table(const v4u (&f)[2][2][4], v4u oh, int lane, float kd, f32x4 (&kt)[2][2]) {
  v4u sel[2][2];
  bool oct[8];
#pragma unroll
  for (int q = 0; q < 8; q++) oct[q] = (lane >> 3) == q;
#pragma unroll
  for (int e = 0; e < 4; e++) { sel[0][0][e] = oct[0] ? oh[e] : 0u; sel[0][1][e] = oct[1] ? oh[e] : 0u; }
  pin(sel[0][0]); pin(sel[0][1]);
#pragma unroll
  for (int i = 0; i < 4; i++) kt[i >> 1][i & 1] = zero4();
  kt[0][0][0] = kd; pin(kt[0][0]);
  NOP2();
  f32x4 t[2][2][2];
  v4u o[2][2];
  static_for<0, 9>([&](auto H) LAM {
    constexpr int h = H, y = h & 1, gs = h >> 1, py = y ^ 1;
    static_for<0, 8>([&](auto I) LAM {
      constexpr int i = I;
      SB();
      if constexpr (h < 8) {
        if constexpr (i < 4) mfma_h0(t[y][i >> 1][i & 1], f[y][i & 1][2 * (i >> 1)], sel[gs & 1][0]);
        else mfma_h(t[y][(i - 4) >> 1][i & 1], f[y][i & 1][2 * ((i - 4) >> 1) + 1], sel[gs & 1][1]);
      } else if constexpr (i < 4) asm volatile("s_nop 1");
      SB();
      if constexpr (y == 1 && gs < 3 && i < 4) { sel[(gs + 1) & 1][0][i] = oct[2 * gs + 2] ? oh[i] : 0u; sel[(gs + 1) & 1][1][i] = oct[2 * gs + 3] ? oh[i] : 0u; }
      if constexpr (y == 1 && i == 4 && gs < 3) { pin(sel[(gs + 1) & 1][0]); pin(sel[(gs + 1) & 1][1]); }
      if constexpr (h > 0 && i >= 4) {
        const f32x4 v = t[py][(i - 4) >> 1][i & 1];
        o[py][(i - 4) >> 1][2 * (i & 1)] = pk_h(v[0], v[1]); o[py][(i - 4) >> 1][2 * (i & 1) + 1] = pk_h(v[2], v[3]);
      }
    });
    if constexpr (h > 0 && y == 0) {
      pin(o[0][0]); pin(o[0][1]); pin(o[1][0]); pin(o[1][1]);
      NOP2();
#pragma unroll
      for (int i = 0; i < 4; i++) mfma_h(kt[i >> 1][i & 1], o[0][i >> 1], o[1][i & 1]);
    }
  });
  MFMA_DONE();
}
constexpr unsigned TMASK = 0x00070007u;
DEV v4u seal(v4u v, v4u t) { for (int e = 0; e < 4; e++) v[e] = ((v[e] + 0x00040004u) & ~TMASK) | t[e]; return v; }
DEV unsigned unseal(v4u& v, v4u t) { v ^= t; return (v.x | v.y | v.z | v.w) & TMASK; }

DEV void role_gemm(const Args& a, LAS char* sm, int w, int lane, int p, int j, unsigned fa) {
  const int g = lane >> 4, tok = lane & 15, q4 = tok >> 2, pp = lane & 3;
  const v4u rq = srd(kSeq, 256);
  const unsigned fre = FLG + F_FREE + 4 * w;
  volatile LAS unsigned* full = (volatile LAS unsigned*)(size_t)(fa + F_FULL);
  const rsrc_t rh = mk_rsrc(a.ws + WS_H, 32 * NKS * 4096);
  const int ks = w >> 1, ah = w & 1;
  u64 tagr;
  v2u tgv;
  GAS u64* fh = gflag(a, GF_H + (p * 8 + j) * 4 + w); pins(fh);
  {
    const int us = unit_start(j), nown = (j == 7 ? NKS : unit_start(j + 1)) - us;
    unsigned bias[3];
#pragma unroll
    for (int i = 0; i < 3; i++) {
      bias[i] = *(const GAS u16*)(a.b1 + us * 32 + (3 * w + i) * 16 + tok);
    }
    LAS char* wb[2][3];
#pragma unroll
    for (int i = 0; i < 3; i++) {
      const int ft = 3 * w + i;
      wb[0][i] = opq(sm + (6 * (8 * g + q4) + ((ft >> 1) ^ ((q4 >> 1) & 1))) * 64 + (((ft & 1) ^ (g & 1)) * 32) + 8 * pp);
      wb[1][i] = opq(wb[0][i] + L_RING);
    }
    LAS char* xb[2];
#pragma unroll
    for (int s = 0; s < 2; s++) xb[s] = opq(sm + L_X + tok * 128 + (((4 * s + g) ^ ((tok >> 1) & 7)) * 16));
    int hoffv = (g & 1) * 1024 + (g >> 1) * 256 + tok * 16; pin(hoffv);
    f32x4 acc[3][4], cs[3];
#pragma unroll
    for (int i = 0; i < 3; i++) { cs[i] = zero4();
#pragma unroll
      for (int T = 0; T < 4; T++) acc[i][T] = zero4(); }
    v4u ones = {0x3f803f80u, 0x3f803f80u, 0x3f803f80u, 0x3f803f80u};
    pin(ones);
#pragma unroll
    for (int i = 0; i < 3; i++) { bias[i] = g ? 0u : bias[i] << 16; pin(bias[i]); }
    v2u wfr[2][3][2]; v4u xfr[2][4];
    auto rd = [&](auto U, auto K) LAM {
      constexpr int u = U, k = K, wa = w_addr(u), hi = wa >= L_RING;
      if constexpr (k >= 2 && k < 6) xfr[u & 1][k - 2] = *(LAS v4u*)(xb[u & 1] + (CE(x_addr(u >> 1)) - L_X) + (k - 2) * 2048);
      else { constexpr int i = k < 2 ? 0 : (k - 4) >> 1; wfr[u & 1][i][k & 1] = tr_read(wb[hi][i] + (wa - hi * L_RING) + (k & 1) * 1536); }
    };
    int vo[3], so[3];
#pragma unroll
    for (int i = 0; i < 3; i++) {
      const int ft = 3 * w + i;
      so[i] = (p * NKS + us + (ft >> 1)) * 4096 + (ft & 1) * 512; pins(so[i]);
    }
    LAS char* sr = opq(sm + L_STAT + tok * 16);
    lds_wait<F_FULL, 1>(fa);
    tagr = tag_of(sm, 1); pins(tagr);
    tgv = v2u{(unsigned)tagr, (unsigned)(tagr >> 32)}; pin(tgv);
    lds_wait<F_FULL, need_W(0)>(fa);
    static_for<0, 10>([&](auto K) LAM { rd(std::integral_constant<int, 0>{}, K); });
    static_for<0, 15>([&](auto U) LAM {
      constexpr int u = U, b = u & 1;
      unsigned fv, fs;
      static_for<0, 15>([&](auto M) LAM {
        constexpr int m = M, i = m / 5, T = m % 5;
        const v4u wv = {wfr[b][i][0].x, wfr[b][i][0].y, wfr[b][i][1].x, wfr[b][i][1].y};
        SB();
        if constexpr (T < 4) mfma_bf(acc[i][T], wv, xfr[b][T]); else mfma_bf(cs[i], ones, wv);
        SB();
        if constexpr (m == 1) __builtin_amdgcn_s_waitcnt(0xc07f);
        if constexpr (m == 1 && u + 1 <= 9) dma_flag<u + 1>(rq, fre);
        if constexpr (m == RD0 - 4) fv = *full;
        if constexpr (m == RD0 - 1) { if (__builtin_expect(wany(fv < CE(need_W(u + 1))), 0)) lds_wait<F_FULL, need_W(u + 1)>(fa); CFENCE(); }
        if constexpr (m >= RD0 && m < RD0 + 5) { rd(std::integral_constant<int, u + 1>{}, std::integral_constant<int, 2 * (m - RD0)>{}); rd(std::integral_constant<int, u + 1>{}, std::integral_constant<int, 2 * (m - RD0) + 1>{}); }
        if constexpr (u + 2 == FREE_C && m == RD0 + 5) { VMCNT(0); lds_put(fre, u + 2); }
        if constexpr (u == 14 && m == RD0 + 5) lds_put(fre, 16);
        if constexpr (u == 13 && m == 2) fs = full[F_CW / 4];
        if constexpr (u == 13 && m == 14) { if (__builtin_expect(wany(fs < 1), 0)) lds_wait<F_CW, 1>(fa); CFENCE(); }
      });
    });
    v2u sb[4], av[3];
    float cr[3][3];
    auto wv = [&](int i) LAM { return v4u{wfr[1][i][0].x, wfr[1][i][0].y, wfr[1][i][1].x, wfr[1][i][1].y}; };
    unsigned pk[6][4];
    auto epi = [&](int n) LAM {
      const int grp = n / 5, k = n % 5, i = grp >> 1, T = 2 * (grp & 1);
      if (k < 4) {
        const float t0 = acc[i][T + (k >> 1)][2 * (k & 1)], t1 = acc[i][T + (k >> 1)][2 * (k & 1) + 1];
        pk[grp][k] = relu_pk(pk_bf(t0 * __builtin_fabsf(t0), t1 * __builtin_fabsf(t1)));
      } else {
        auto r0 = __builtin_amdgcn_permlane16_swap(pk[grp][0], pk[grp][2], false, false);
        auto r1 = __builtin_amdgcn_permlane16_swap(pk[grp][1], pk[grp][3], false, false);
        st_sc1(v4u{r0[0], r1[0], r0[1], r1[1]}, rh, vo[i] + T * 1024, so[i]);
      }
    };
    static_for<0, 27>([&](auto M) LAM {
      constexpr int m = M, q = (m - 3) >> 2, T = (m - 3) & 3;
      SB();
      if constexpr (m < 3) mfma_bf(cs[m], ones, wv(m));
      else if constexpr (q == 0 || q == 1 || q == 3) mfma_bf(acc[q == 3 ? 2 : q][T], wv(q == 3 ? 2 : q), xfr[1][T]);
      else mfma_bf16k(acc[q == 2 ? 0 : q - 3][T], av[q == 2 ? 0 : q - 3], sb[T]);
      SB();
      if constexpr (m < 4) sb[m] = *(LAS v2u*)(sr + m * 256);
      if constexpr (m < 3) { vo[m] = hoffv | (((3 * w + m) >> 1) < nown ? 0 : 0x40000000); pin(vo[m]); }
      if constexpr (m >= 5 && m < 14) {
        constexpr int i = (m - 5) / 3, k = (m - 5) % 3;
        if constexpr (k == 0) { pin(cs[i]); cr[i][0] = g ? 0.f : cs[i][0]; cr[i][1] = rbf(cr[i][0]); pin(cr[i][0]); pin(cr[i][1]); }
        if constexpr (k == 1) { cr[i][1] = cr[i][0] - cr[i][1]; cr[i][2] = cr[i][1] - rbf(cr[i][1]); pin(cr[i][1]); pin(cr[i][2]); }
        if constexpr (k == 2) { av[i] = v2u{pk_bf(cr[i][0], cr[i][1]), pk_bf(cr[i][2], u2f(bias[i]))}; pin(av[i]); }
      }
      if constexpr (m >= 17) epi(m - 17);
    });
#pragma unroll
    for (int n = 10; n < 30; n++) { if (n % 5 == 0) SB(); epi(n); }
  }
  lds_put(FLG + F_GX + 4 * w, 1);
  __builtin_amdgcn_s_setprio(3);
  GAS u64* pf = gflag(a, GF_H + p * 32 + (lane & 31));
  LAS char* wb[2][4];
  const int phi = ((q4 >> 1) & 1) | ((g & 1) << 1);
#pragma unroll
  for (int e = 0; e < 4; e++) { wb[0][e] = opq(sm + L_V + ks * 4096 + (8 * g + q4) * 128 + ((e ^ phi) * 32) + 8 * pp); wb[1][e] = opq(wb[0][e] + VH * 8192); }
  f32x4 acc[4][2];
#pragma unroll
  for (int e = 0; e < 4; e++) { acc[e][0] = zero4(); acc[e][1] = zero4(); }
  v2u wfr[2][4][2];
  auto rd2 = [&](auto I, auto K) LAM {
    constexpr int i = I, k = K, t = i % NVB;
    wfr[i & 1][k >> 1][k & 1] = tr_read(wb[t / VH][k >> 1] + (t % VH) * 8192 + (k & 1) * 512);
  };
  const int T = 2 * ah + ks;
  int hv0 = lane * 16 + ks * 1024, hv1 = hv0 ^ 1024;
  volatile LAS unsigned* frw = (volatile LAS unsigned*)opq(sm + (lane ? L_JUNK + lane * 4 : (int)fre));
  int hso = p * NKS * 4096 + ah * 2048;
  pin(hv0); pin(hv1); pin(pf); pins(hso);
  SB();
  flag_store<0>(fh, tgv);
  lds_put(FLG + F_GP + 4 * w, 1);
  v4u hf[PD][2];
  auto ldh = [&](int i, int t) LAM { hf[i % PD][t] = __builtin_amdgcn_raw_buffer_load_b128(rh, t ? hv1 : hv0, hso + min(2 * i + ks, NKS - 1) * 4096, AUX_SC1); };
  float rest[4][4];
#pragma unroll
  for (int e = 0; e < 4; e++) {
    const v2u b2 = *(LAS v2u*)(sm + L_B2 + 32 * e + 8 * g);
    const v2u xv = *(LAS v2u*)(sm + L_X + (16 * T + tok) * 128 + (((2 * e + (g >> 1)) ^ ((tok >> 1) & 7)) * 16) + (g & 1) * 8);
    rest[e][0] = bf_lo(b2.x) + bf_lo(xv.x); rest[e][1] = bf_hi(b2.x) + bf_hi(xv.x); rest[e][2] = bf_lo(b2.y) + bf_lo(xv.y); rest[e][3] = bf_hi(b2.y) + bf_hi(xv.y);
#pragma unroll
    for (int r = 0; r < 4; r++) pin(rest[e][r]);
  }
  float rs2 = *(LAS float*)(sm + L_STAT + (16 * T + tok) * 16 + 8);
  pin(rs2);
  u64 first = 0;
  if (w == 0) { __builtin_amdgcn_s_sleep(HDELAY); first = __hip_atomic_load(pf, __ATOMIC_RELAXED, __HIP_MEMORY_SCOPE_AGENT); }
  SB();
  LAS char* exw = opq(sm + L_EX + w * 4096 + lane * 16);
  LAS char* rl = sm + L_EX + (w ^ 2) * 4096;
  LAS char* exr = opq(rl + lane * 16);
  LAS char* ya = opq(sm + L_Y + (4 * g + q4) * YROW + T * 32 + 8 * pp);
  LAS char* sa = opq(sm + L_YS + 16 * g);
  LAS char* rlw = opq(rl + tok * 144 + g * 8);
  LAS char* rlr = opq(rl + (lane >> 3) * 144 + (lane & 7) * 16);
  int ov = (lane >> 3) * 1024 + (lane & 7) * 16, oso = (64 * p + 16 * T) * 1024 + j * 128; pin(ov); pins(oso);
  lds_wait<F_FULL, need_V(VFIRST)>(fa);
  static_for<0, 8>([&](auto K) LAM { rd2(std::integral_constant<int, 0>{}, K); });
  SB();
  if (w == 0) {
    if (wany(first != tagr)) poll(pf, tagr);
    __builtin_amdgcn_fence(__ATOMIC_ACQUIRE, "wavefront");
    lds_put(FLG + F_HOK, 1);
  } else lds_wait<0, 1>((unsigned)(FLG + F_HOK));
#pragma unroll
  for (int i = 0; i < PD - 1; i++) { ldh(i, 0); ldh(i, 1); }
  __builtin_amdgcn_s_setprio(2);
  v2u yv[4]; f32x4 ys[4];
  static_for<0, 22>([&](auto I) LAM {
    constexpr int i = I, b = i & 1, nx = i + PD - 1;
    if (i < 21 || ks == 0) {
      constexpr bool chk = !(i & 1) && i + 3 > VFIRST && i + 2 < 22;
      constexpr int want = need_V(i + 3 < 22 ? i + 3 : 21), out = 2 * ((nx < 22 ? nx : 22) - 1 - i);
      unsigned fv, fy; bool lack = false, lacky = false;
      SB();
      __builtin_amdgcn_s_waitcnt((out & 15) | ((out >> 4) << 14) | 0x70);
      static_for<0, 8>([&](auto M) LAM {
        constexpr int m = M, e = m >> 1;
        const v4u wv = {wfr[b][e][0].x, wfr[b][e][0].y, wfr[b][e][1].x, wfr[b][e][1].y};
        SB();
        mfma_bf(acc[e][m & 1], wv, hf[i % PD][m & 1]);
        SB();
        if constexpr (chk && m == 0) fv = *full;
        if constexpr (i < 21 && m < 4) { rd2(std::integral_constant<int, i + 1>{}, std::integral_constant<int, 2 * m>{}); rd2(std::integral_constant<int, i + 1>{}, std::integral_constant<int, 2 * m + 1>{}); }
        if constexpr (nx < 22 && (m == 4 || m == 5)) ldh(nx, m - 4);
        if constexpr (i == YAT - 1 && m == 4) fy = full[F_CW / 4];
        if constexpr (i == YAT - 1 && m == 7) lacky = wany(fy < 6);
        if constexpr (i == YAT && m >= 4) { yv[m - 4] = tr_read(ya + 16 * (m - 4) * YROW); ys[m - 4] = *(LAS f32x4*)(sa + 64 * (m - 4)); }
        if constexpr ((i == YAT + 1 || i == YAT + 2) && m >= 4) {
          constexpr int q = 2 * (i - YAT - 1) + ((m - 4) >> 1), r = 2 * (m & 1);
          const unsigned yy = (m & 1) ? yv[q].y : yv[q].x;
          const f16x2 hh = __builtin_bit_cast(f16x2, yy);
          rest[q][r] = __builtin_fmaf((float)hh.x, ys[q][r], rest[q][r]); rest[q][r + 1] = __builtin_fmaf((float)hh.y, ys[q][r + 1], rest[q][r + 1]);
          pin(rest[q][r]); pin(rest[q][r + 1]);
        }
        if constexpr (m == 6 && (i & 1) && (i + NVB < 23 || i == YGATE)) { CFENCE(); *frw = 18 + i; }
        if constexpr (chk && m == 6) lack = wany(fv < want);
      });
      SB();
      if constexpr (chk) { if (__builtin_expect(lack, 0)) lds_wait<F_FULL, want>(fa); CFENCE(); }
      if constexpr (i == YAT - 1) { if (__builtin_expect(lacky, 0)) lds_wait<F_CW, 6>(fa); CFENCE(); }
    }
  });
  MFMA_DONE();
#pragma unroll
  for (int e = 0; e < 4; e++) *(LAS f32x4*)(exw + e * 1024) = acc[e][1];
  lds_put(FLG + F_GX + 4 * w, 2);
#pragma unroll
  for (int e = 0; e < 4; e++)
#pragma unroll
    for (int r = 0; r < 4; r++) { rest[e][r] = __builtin_fmaf(acc[e][0][r], rs2, rest[e][r]); pin(rest[e][r]); }
  lds_wait<0, 2>(FLG + F_GX + 4 * (w ^ 2));
  f32x4 po[4];
#pragma unroll
  for (int e = 0; e < 4; e++) po[e] = *(LAS f32x4*)(exr + e * 1024);
  CFENCE();
#pragma unroll
  for (int e = 0; e < 4; e++)
    *(LAS v2u*)(rlw + e * 32) = v2u{pk_bf(__builtin_fmaf(po[e][0], rs2, rest[e][0]), __builtin_fmaf(po[e][1], rs2, rest[e][1])),
                                    pk_bf(__builtin_fmaf(po[e][2], rs2, rest[e][2]), __builtin_fmaf(po[e][3], rs2, rest[e][3]))};
  CFENCE();
  {
    const rsrc_t ro = mk_rsrc(a.out, SEQ * DM * 2);
    const v4u o0 = *(LAS v4u*)(rlr), o1 = *(LAS v4u*)(rlr + 8 * 144);
    st_sc1(o0, ro, ov, oso);
    st_sc1(o1, ro, ov, oso + 8192);
  }
  if (w == 0 && lane < 2 && (lane ? j == 0 : p == 0)) {
    const u64 e1 = *(LAS u64*)(sm + FLG + F_EP + lane * 8) + 1;
    __hip_atomic_store(gflag(a, lane ? GF_ER + 16 * p : GF_EC + 16 * j), e1, __ATOMIC_RELAXED, __HIP_MEMORY_SCOPE_AGENT);
  }
}

DEV void role_conv(const Args& a, LAS char* sm, int cw, int lane, int p, int j, unsigned fa) {
  const int g = lane >> 4, tok = lane & 15;
  const int ch = cw >> 1, mh = cw & 1, d = 64 * j + 2 * p + ch;
  const unsigned fme = FLG + F_CW + 4 * cw, fpa = FLG + F_CW + 4 * (cw ^ 1);
  v4u oh, idm[2];
#pragma unroll
  for (int e = 0; e < 4; e++) {
    oh[e] = ((lane >> 1) & 3) == e ? 0x3c00u << (16 * (lane & 1)) : 0u;
    idm[0][e] = g == (tok >> 3) ? oh[e] : 0u; idm[1][e] = g - 2 == (tok >> 3) ? oh[e] : 0u;
  }
  pin(oh); pin(idm[0]); pin(idm[1]);
  f32x4 dm;
#pragma unroll
  for (int r = 0; r < 4; r++) dm[r] = 4 * g + r == tok ? 1.f / DM : 0.f;
  int uoff = (8 * (g & 1) + (tok >> 1)) * 8192 + (g >> 1) * 32 + (tok & 1) * 16;
  LAS char* xs[2];
#pragma unroll
  for (int s = 0; s < 2; s++) xs[s] = opq(sm + L_X + (16 * cw + tok) * 128 + (((4 * s + g) ^ ((tok >> 1) & 7)) * 16));
  pin(dm); pin(uoff);
  const rsrc_t ru = mk_rsrc(a.ws + WS_U, SEQ * DM * 2);
  int uso = j * 262144 + p * 256 + cw * 64; pins(uso);
  v4u kf[2][2][4];
  float kinv, kd;
  SB();
  kgen_factors(kgen_read(sm, cw, lane), cw, lane, kf, kinv, kd);
  v2u kh[4];
  {
    f32x4 kt[2][2];
    kgen_table(kf, oh, lane, kd, kt);
#pragma unroll
    for (int i = 0; i < 4; i++) { const f32x4 v = kt[i >> 1][i & 1]; kh[i] = v2u{pk_h(v[0], v[1]), pk_h(v[2], v[3])}; pin(kh[i]); }
  }
  LAS char* tw = opq(sm + c_priv(cw) + g * 256 + tok * 8);
  u64 tagc;
  v4u stat, pat;
  {
    v4u xl[2];
    f32x4 sa = zero4(), gr = zero4();
    v4u ones = {0x3f803f80u, 0x3f803f80u, 0x3f803f80u, 0x3f803f80u};
    pin(ones);
    static_for<0, 2>([&](auto B) LAM {
      constexpr int c0 = B == 0 ? 0 : XB0, c1 = B == 0 ? XB0 : 8;
      lds_wait<F_FULL, need_X(c1 - 1)>(fa);
      v4u f[2 * (c1 - c0)];
      unsigned en = 0;
      if constexpr (B == 0) en = *(LAS unsigned*)(sm + FLG + F_EP);
#pragma unroll
      for (int c = c0; c < c1; c++) { f[2 * (c - c0)] = *(LAS v4u*)(xs[0] + ((c + 1) & 7) * 8192); f[2 * (c - c0) + 1] = *(LAS v4u*)(xs[1] + ((c + 1) & 7) * 8192); }
      SB();
#pragma unroll
      for (int i = 0; i < 2 * (c1 - c0); i++) {
        mfma_bf(gr, f[i], f[i]);
        if (B == 0 && i >= 2 && i < 6) { SB(); unsigned t = ((en + (i - 2)) & 7) | (((en + 2 * (i - 2) + 1) & 7) << 16); pin(t); pat[i - 2] = t; SB(); }
        mfma_bf(sa, ones, f[i]);
      }
      if constexpr (B == 1) { xl[0] = f[2 * (7 - XB0)]; xl[1] = f[2 * (7 - XB0) + 1]; }
    });
    float xf[2][8];
#pragma unroll
    for (int s = 0; s < 2; s++)
#pragma unroll
      for (int e = 0; e < 4; e++) { const unsigned uu = xl[s][e]; xf[s][2 * e] = bf_lo(uu); xf[s][2 * e + 1] = bf_hi(uu); pin(xf[s][2 * e]); pin(xf[s][2 * e + 1]); }
    pin(sa); pin(gr);
    SB();
    const float mean = sa[0] * (1.f / DM);
    const float ex2 = xsum32(xsum16(__builtin_fmaf(gr[0], dm[0], gr[1] * dm[1]) + __builtin_fmaf(gr[2], dm[2], gr[3] * dm[3])));
    float var = __builtin_fmaf(-mean, mean, ex2);
    if (__builtin_expect(wany(!(var >= ex2 * (1.f / 1024.f))), 0)) {
      float q = 0;
#pragma nounroll
      for (int c = 0; c < 16; c++) {
        const v4u u = *(LAS v4u*)((c & 1 ? xs[1] : xs[0]) + (c >> 1) * 8192);
#pragma unroll
        for (int e = 0; e < 4; e++) { const unsigned uu = u[e]; const float d0 = bf_lo(uu) - mean, d1 = bf_hi(uu) - mean; q = __builtin_fmaf(d0, d0, __builtin_fmaf(d1, d1, q)); }
      }
      var = xsum32(xsum16(q)) * (1.f / DM);
    }
    const float m = rbf(mean), sd = rbf(__builtin_amdgcn_sqrtf(rbf(rbf(var) + 1e-5f))), rs = __builtin_amdgcn_rcpf(sd);
    const float c0 = -m * rs;
    v4u xn[2]; f32x4 t[4];
#pragma unroll
    for (int s = 0; s < 2; s++) {
#pragma unroll
      for (int e = 0; e < 4; e++) xn[s][e] = pk_h(__builtin_fmaf(xf[s][2 * e], rs, c0), __builtin_fmaf(xf[s][2 * e + 1], rs, c0));
      pin(xn[s]);
      NOP2();
      mfma_h0(t[2 * s], xn[s], idm[0]); mfma_h0(t[2 * s + 1], xn[s], idm[1]);
      SB();
    }
#pragma unroll
    for (int s = 0; s < 2; s++) {
      auto r0 = __builtin_amdgcn_permlane16_swap(pk_h(t[2 * s][0], t[2 * s][1]), pk_h(t[2 * s + 1][0], t[2 * s + 1][1]), false, false);
      auto r1 = __builtin_amdgcn_permlane16_swap(pk_h(t[2 * s][2], t[2 * s][3]), pk_h(t[2 * s + 1][2], t[2 * s + 1][3]), false, false);
      st_sc1(seal(v4u{r0[0], r1[0], r0[1], r1[1]}, pat), ru, uoff, uso + s * 131072);
    }
    stat = v4u{pk_bf(-m, -m), pk_bf(-m, sd), f2u(rs * rs), 0u};
    pin(stat);
  }
  LAS char* sw = opq(sm + L_STAT + (16 * cw + tok) * 16);
  __builtin_amdgcn_s_sleep(TDELAY);
#pragma unroll
  for (int i = 0; i < 4; i++) *(LAS v2u*)(tw + (i >> 1) * 1024 + (i & 1) * 128) = kh[i];
  if (g == 0) *(LAS v4u*)sw = stat;
  lds_put(fme, 1);
  __builtin_amdgcn_s_setprio(EPRIO);
  tagc = tag_of(sm, 0); pins(tagc);
  if (mh == 0 && lane == 0) __hip_atomic_store(gflag(a, GF_Y) + d, (tagc & 0xffffffff00000000ull) | f2u(kinv * 65536.f), __ATOMIC_RELAXED, __HIP_MEMORY_SCOPE_AGENT);
  v4u im[6];
  {
    const int nh = 127 - lane, nl = 63 - lane, to = c_priv(cw ^ 1) - c_priv(cw);
    LAS char* tb = sm + c_priv(cw);
    auto odd = [&](int q) LAM { return 1028 + (q >> 3) * 8 + ((q >> 2) & 1) * 256 + (q & 2); };
    auto tv = [&](int off) LAM { return *(LAS _Float16*)(tb + off) + *(LAS _Float16*)(tb + to + off); };
    auto pk = [&](_Float16 x, _Float16 y) LAM { return __builtin_bit_cast(unsigned, f16x2{x, y}); };
    lds_wait<0, 1>(fpa);
    if (mh == 0) {
      const _Float16 A = tv(lane ? 16 * nh + 16 : 1540), B = tv(16 * nh + 8), C = tv(16 * nh), A4 = tv(8 * nh + 8), B4 = tv(8 * nh + 4), C4 = tv(8 * nh);
      const unsigned aa = pk(A, A), bb2 = pk(B, B), cc = pk(C, C), b4 = pk(B4, B4), c4 = pk(C4, C4);
      im[0] = v4u{aa, aa, bb2, bb2}; im[1] = v4u{bb2, pk(B, C), cc, cc}; im[2] = v4u{aa, pk(A, B), bb2, bb2}; im[3] = v4u{bb2, cc, cc, cc};
      im[4] = v4u{pk(A4, A4), b4, pk(B4, C4), c4}; im[5] = v4u{pk(A4, B4), b4, c4, c4};
    } else {
      const _Float16 A = tv(4 * nh + 4), B = tv(4 * nh + 2), C = tv(4 * nh), Dd = tv(lane == 63 ? 1028 + 31 * 8 + 258 : 4 * nh);
      const _Float16 g3 = tv(odd(4 * nl + 3)), g2 = tv(4 * nl + 2), g1 = tv(odd(4 * nl + 1)), g0 = tv(4 * nl), gq = tv(odd(max(4 * nl - 1, 1))), gm = lane == 63 ? (_Float16)0 : gq;
      im[0] = v4u{pk(A, B), pk(C, C), pk(B, C), pk(C, Dd)}; im[1] = v4u{pk(g3, g2), pk(g1, g0), pk(g2, g1), pk(g0, gm)};
      im[2] = im[3] = im[4] = im[5] = v4u{0, 0, 0, 0};
    }
#pragma unroll
    for (int i = 0; i < 6; i++) pin(im[i]);
  }
  dma_flag<1>(srd(kSeq, 256), FLG + F_ST + 4 * cw);
  lds_wait<F_FULL, need_W(UWAIT)>(fa);
  v4u uv[2];
  auto ldu = [&]() LAM {
#pragma unroll
    for (int i = 0; i < 2; i++) uv[i] = __builtin_amdgcn_raw_buffer_load_b128(ru, lane * 16 + i * 1024, (j * 32 + p) * 8192 + cw * 2048, AUX_SC1);
  };
  ldu();
  SB();
  LAS char* cr = sm + ch * CH;
  LAS char* kc0 = opq(cr + (mh ? 3072 + 8 * lane : 32 * lane));
  LAS char* kc1 = opq(cr + (mh ? 3584 + 8 * lane : 2048 + 16 * lane));
  LAS char* uw = opq(sm + (lane & 1) * CH + C_U + 480 + cw * 1024 + (lane >> 1) * 16);
  LAS char* zw = opq(lane >= 34 ? sm + L_JUNK + (lane & 15) * 16 : cr + (lane < 30 ? C_U + lane * 16 : ((lane >> 1) & 1) * KCOPY + 4096 + (lane & 1) * 16));
  f32x4 acc[8];
#pragma unroll
  for (int c = 0; c < 8; c++) acc[c] = zero4();
  LAS char* ab[4];
#pragma unroll
  for (int i = 0; i < 4; i++) ab[i] = opq(cr + ((15 - tok) & 1) * KCOPY + 2 * (((15 - tok) & ~1) + 8 * g) + 64 * (4 - 4 * mh) + 1024 * i);
  LAS char* bb = opq(cr + C_U + 448 + 32 * tok + 16 * g - 64 * (4 * mh + 3));
  auto lda = [&](int i, int r, int k) LAM { return ld2(ab[(7 - i) >> 1] + 64 * (59 - 8 * i - r) - 1024 * ((7 - i) >> 1) + 8 * k); };
  SB();
#pragma nounroll
  for (int it = 0; it < POLL_CAP; it++) {
    if (!wany((unseal(uv[0], pat) | unseal(uv[1], pat)) != 0)) break;
    __builtin_amdgcn_s_sleep(SSLEEP);
    CFENCE();
    ldu();
  }
  __builtin_amdgcn_s_setprio(IPRIO);
  lds_wait<F_ST, 1>(fa);
  lds_wait<F_FULL, need_W(15)>(fa);
  lds_wait<F_FREE, FREE_C>(fa);
  if (mh == 0) {
    *(LAS v4u*)(kc0) = im[0]; *(LAS v4u*)(kc0 + 16) = im[1]; *(LAS v4u*)(kc0 + KCOPY) = im[2]; *(LAS v4u*)(kc0 + KCOPY + 16) = im[3];
    *(LAS v4u*)(kc1) = im[4]; *(LAS v4u*)(kc1 + KCOPY) = im[5];
  } else {
    *(LAS v2u*)(kc0) = v2u{im[0].x, im[0].y}; *(LAS v2u*)(kc0 + KCOPY) = v2u{im[0].z, im[0].w};
    *(LAS v2u*)(kc1) = v2u{im[1].x, im[1].y}; *(LAS v2u*)(kc1 + KCOPY) = v2u{im[1].z, im[1].w};
    *(LAS v4u*)zw = im[2];
  }
#pragma unroll
  for (int i = 0; i < 2; i++) *(LAS v4u*)(uw + i * 512) = uv[i];
  lds_put(fme, 2);
  lds_wait<F_CW, 2>(fa);
  v2u A[8][2]; v4u B[2][8];
#pragma unroll
  for (int i = 0; i < 8; i++) {
    B[0][i] = *(LAS v4u*)(bb + 64 * (8 * i + 3));
    A[7 - i][0] = lda(7 - i, 0, 0); A[7 - i][1] = lda(7 - i, 0, 1);
  }
  SB();
  lds_wait<0, 1>(FLG + F_GX + 4 * cw);
  __builtin_amdgcn_s_setprio(CPRIO);
  static_for<0, 4>([&](auto R) LAM {
    constexpr int r = R;
    static_for<0, 8>([&](auto II) LAM {
      constexpr int i = 7 - II;
      static_for<0, 8 - i>([&](auto K) LAM {
        constexpr int k = K;
        SB();
        mfma_h(acc[i + k], v4u{A[i][0].x, A[i][0].y, A[i][1].x, A[i][1].y}, B[r & 1][k]);
        SB();
        if constexpr (r < 3) {
          if constexpr (i < 7 && k < 2) A[i + 1][k] = lda(i + 1, r + 1, k);
          if constexpr (i <= 4 && i >= 1 && (k == 2 || k == 3)) B[(r + 1) & 1][2 * (4 - i) + k - 2] = *(LAS v4u*)(bb + 64 * (8 * (2 * (4 - i) + k - 2) + 3 - (r + 1)));
        }
        if constexpr (r == 3 && i == 4 && k == 0) lds_put(fme, 3);
      });
    });
    if constexpr (r < 3) { A[0][0] = lda(0, r + 1, 0); A[0][1] = lda(0, r + 1, 1); }
  });
  LAS char* exm = opq(cr + mh * 4096 + lane * 16);
  LAS char* exo = opq(cr + (mh ^ 1) * 4096 + lane * 16);
  int yoff = (g & 1) * 512 + (tok >> 2) * 128 + (g >> 1) * 64 + (tok & 3) * 16; pin(yoff);
  const rsrc_t ry = mk_rsrc(a.ws + WS_Y, SEQ * DM * 2);
  constexpr float YS = 1.f / 65536.f;
  MFMA_DONE();
  lds_wait<0, 3>(fpa);
  auto finish = [&](auto MH) LAM {
    constexpr int own = 4 * MH;
#pragma unroll
    for (int c = 0; c < 4; c++) *(LAS f32x4*)(exm + c * 1024) = acc[(own ^ 4) + c];
    lds_put(fme, 4);
#pragma unroll
    for (int c = 0; c < 4; c++) { acc[own + c] *= YS; pin(acc[own + c]); }
    lds_wait<0, 4>(fpa);
    f32x4 o[4];
#pragma unroll
    for (int c = 0; c < 4; c++) o[c] = *(LAS f32x4*)(exo + c * 1024);
#pragma unroll
    for (int c = 0; c < 4; c += 2) {
      unsigned q[2][2];
#pragma unroll
      for (int t = 0; t < 2; t++)
#pragma unroll
        for (int r = 0; r < 2; r++) q[t][r] = pk_h(__builtin_fmaf(o[c + t][2 * r], YS, acc[own + c + t][2 * r]), __builtin_fmaf(o[c + t][2 * r + 1], YS, acc[own + c + t][2 * r + 1]));
      auto r0 = __builtin_amdgcn_permlane16_swap(q[0][0], q[1][0], false, false);
      auto r1 = __builtin_amdgcn_permlane16_swap(q[0][1], q[1][1], false, false);
      st_sc1(seal(v4u{r0[0], r1[0], r0[1], r1[1]}, pat), ry, yoff + c * 512, d * 4096 + MH * 2048);
    }
  };
  if (mh) finish(std::integral_constant<int, 1>{}); else finish(std::integral_constant<int, 0>{});
  lds_put(fme, 5);
  LAS char* yw = opq(sm + L_Y + (16 * cw + (lane >> 3)) * YROW + (lane & 3) * 32 + ((lane >> 2) & 1) * 16);
  GAS u64* ps = gflag(a, GF_Y) + 64 * j + 16 * cw + tok; pin(ps);
  lds_wait<F_CW, 5>(fa);
  lds_wait<F_FREE, 17 + YGATE>(fa);
  u64 sc;
#pragma nounroll
  for (int it = 0; it < POLL_CAP; it++) {
#pragma unroll
    for (int i = 0; i < 2; i++) uv[i] = __builtin_amdgcn_raw_buffer_load_b128(ry, (lane >> 3) * 4096 + (lane & 7) * 16, (64 * j + 16 * cw + 8 * i) * 4096 + p * 128, AUX_SC1);
    sc = __hip_atomic_load(ps, __ATOMIC_RELAXED, __HIP_MEMORY_SCOPE_AGENT);
    if (!wany((unseal(uv[0], pat) | unseal(uv[1], pat) | (unsigned)((sc ^ tagc) >> 32)) != 0)) break;
    __builtin_amdgcn_s_sleep(SSLEEP);
    CFENCE();
  }
#pragma unroll
  for (int i = 0; i < 2; i++) *(LAS v4u*)(yw + 8 * i * YROW) = uv[i];
  if (g == 0) *(LAS unsigned*)(sm + L_YS + (16 * cw + tok) * 4) = (unsigned)sc;
  lds_put(fme, 6);
}

__global__ __launch_bounds__(NTHR, 3) NOPK void k_fused(Args a) {
  __shared__ __attribute__((aligned(16))) char smem[LDS_BYTES];
  LAS char* sm = (LAS char*)smem;
  const int lane = threadIdx.x & 63, w = __builtin_amdgcn_readfirstlane(threadIdx.x >> 6);
  const int b = blockIdx.x, p = (b & 7) * 4 + (b >> 6), j = (b >> 3) & 7;
  int vx[2];
  if (w == 4) {
    u64 pc; asm volatile("s_getpc_b64 %0" : "=s"(pc));
    dma_touch(srd((const void*)pc, CODE_BYTES), lane < 8 ? ((b >> 3) * 8 + lane) * 128 : 0x40000000);
    ((LAS unsigned*)(sm + FLG))[lane] = 0; LGKM0();
  }
  unsigned fa = FLG + 4 * (lane & 3);
  pin(fa);
  SB();
  __builtin_amdgcn_s_barrier();
  if (w >= 8) { __builtin_amdgcn_s_setprio(3); kgen_fetch(a, w - 8, lane, p, j); }
  else if (w < 4) { __builtin_amdgcn_s_setprio(2); loader_entry(a, w, lane, p, j, vx); __builtin_amdgcn_s_setprio(3); }
  else if (w == 5) { __builtin_amdgcn_s_sleep(W1DELAY); dma_touch(srd(a.w1, DM * FF * 2), lane < 43 ? (b * 43 + lane) * 128 : 0x40000000); }
  SB();
  if (w < 4) {
    role_loader(a, sm, w, lane, p, j, fa, vx);
  }
  else if (w < 8) { __builtin_amdgcn_s_setprio(2); role_gemm(a, sm, w - 4, lane, p, j, fa); }
  else role_conv(a, sm, w - 8, lane, p, j, fa);
}

extern "C" void kernel_launch(void* const* d_in, const int* in_sizes, int n_in, void* d_out, int out_size, void* d_ws, size_t ws_size, hipStream_t stream) {
  Args a;
  a.x = (const u16*)d_in[0]; a.lar = (const u16*)d_in[1]; a.aim = (const u16*)d_in[2]; a.cre = (const u16*)d_in[3]; a.cim = (const u16*)d_in[4];
  a.ldt = (const u16*)d_in[5]; a.dsk = (const u16*)d_in[6]; a.w1 = (const u16*)d_in[8]; a.b1 = (const u16*)d_in[9]; a.w2 = (const u16*)d_in[10];
  a.b2 = (const u16*)d_in[11]; a.out = (u16*)d_out; a.ws = (char*)d_ws;
  k_fused<<<256, NTHR, 0, stream>>>(a);
}
```
